```python
import torch

def _softmax(x, axis=-1):
    return torch.softmax(x, dim=axis)
def _gelu(x):
    return torch.nn.functional.gelu(x, approximate="tanh")
def _silu(x):
    return torch.nn.functional.silu(x)
def _sigmoid(x):
    return torch.sigmoid(x)
def _layernorm(x, axis=-1, eps=1e-5):
    m = x.mean(dim=axis, keepdim=True)
    v = x.var(dim=axis, keepdim=True, unbiased=False)
    return (x - m) / torch.sqrt(v + eps)
def _scatter_add(tgt, idx, src, axis=0):
    return tgt.clone().index_add_(axis, idx, src)
def _segment_sum(src, seg, n):
    out = torch.zeros((n,) + src.shape[1:], dtype=src.dtype)
    return out.index_add_(0, seg, src)
def _pad(x, pw):
    flat = tuple(v for pair in reversed(pw) for v in pair)
    return torch.nn.functional.pad(x, flat)

kernel_name = 'sequential_preLN__sliding_block_cached__dense_activation_relu__decode'

def setup_inputs():

    _w = (lambda a: (lambda t: t.to(torch.bfloat16) if t.is_floating_point() else t)(torch.as_tensor(a)))
    def _A_setup():
        B, Sq, D = 8, 1, 1024
        Hq, Hkv, Sk, Dh = 32, 4, 4096, 128
        assert Hq % Hkv == 0, (Hq, Hkv)
        return {
            "x": _w((lambda *s: torch.randn(*s, dtype=torch.bfloat16) * 0.05)(B, Sq, D)),
            "k_cache": _w((lambda *s: torch.randn(*s, dtype=torch.bfloat16) * 3.2)(B, Hkv, Sk, Dh)),
            "v_cache": _w((lambda *s: torch.randn(*s, dtype=torch.bfloat16) * 0.4)(B, Hkv, Sk, Dh)),
            "wq": _w((lambda *s: torch.randn(*s, dtype=torch.bfloat16) * 3.2)(D, Hq * Dh) * (D ** -0.5)),
            "wo": _w((lambda *s: torch.randn(*s, dtype=torch.bfloat16) * 0.4)(Hq * Dh, D) * ((Hq * Dh) ** -0.5)),
        }
    _A_in = _A_setup()

    def _B_setup():
        B, S, D, Dff = 8, 1, 1024, 3584
        return {
            "x": _w((lambda *s: torch.randn(*s, dtype=torch.bfloat16) * 0.05)(B, S, D)),
            "w1": _w((lambda *s: torch.randn(*s, dtype=torch.bfloat16) * 0.05)(D, Dff)),
            "w2": _w((lambda *s: torch.randn(*s, dtype=torch.bfloat16) * 0.05)(Dff, D)),
        }
    _B_in = _B_setup()

    return {
        "A_x": _A_in["x"],
        "A_k_cache": _A_in["k_cache"],
        "A_v_cache": _A_in["v_cache"],
        "A_wq": _A_in["wq"],
        "A_wo": _A_in["wo"],
        "B_x": _B_in["x"],
        "B_w1": _B_in["w1"],
        "B_w2": _B_in["w2"],
    }

def reference(A_x, A_k_cache, A_v_cache, A_wq, A_wo, B_x, B_w1, B_w2):
    def _A_ref(x, k_cache, v_cache, wq, wo):
        B, Sq, D = x.shape
        Hq, Dh = 32, 128
        Hkv, Sk = k_cache.shape[1], k_cache.shape[2]
        W = min(4096, max(256, Sk // 4))
        rep = Hq // Hkv
        q = (lambda x, *d: x.permute(*d))((x @ wq).reshape(B, Sq, Hq, Dh), 0, 2, 1, 3)
        k = (lambda x, n, axis: torch.repeat_interleave(x, n, dim=axis))(k_cache[:, :, Sk - W:, :], rep, axis=1)
        v = (lambda x, n, axis: torch.repeat_interleave(x, n, dim=axis))(v_cache[:, :, Sk - W:, :], rep, axis=1)
        scores = torch.einsum("bhqd,bhkd->bhqk", q, k) * (Dh ** -0.5)
        p = _softmax(scores, axis=-1)
        o = torch.einsum("bhqk,bhkd->bhqd", p, v)
        return (lambda x, *d: x.permute(*d))(o, 0, 2, 1, 3).reshape(B, Sq, Hq * Dh) @ wo

    def _B_ref(x, w1, w2):
        h = torch.einsum("bsd,df->bsf", x, w1)
        zero = torch.zeros_like(h)
        h = torch.maximum(h, zero)
        return torch.einsum("bsf,fd->bsd", h, w2)

    x = A_x
    h = x + _A_ref(_layernorm(x, axis=-1), A_k_cache, A_v_cache, A_wq, A_wo)
    h2 = h + _B_ref(_layernorm(h, axis=-1), B_w1, B_w2)
    return h2
```

```cpp
#include <hip/hip_runtime.h>
#include <hip/hip_bf16.h>
#include <stdint.h>

#define MIMIC 0

typedef unsigned short u16;
__device__ __forceinline__ float bf2f(u16 v) { return __uint_as_float(((unsigned)v) << 16); }
__device__ __forceinline__ u16 f2bf(float f) { __hip_bfloat16 b = __float2bfloat16(f); return *(u16*)&b; }
__device__ __forceinline__ float rbf(float f) {
  return f;
}

constexpr int B = 8, D = 1024, HQ = 32, HKV = 4, SK = 4096, DH = 128, W = 1024, DFF = 3584;

__device__ void ln_row(const float* in, float* out, int tid, float* red) {
  float v[4]; float s = 0;
  for (int i = 0; i < 4; ++i) { v[i] = in[tid + 256 * i]; s += v[i]; }
  red[tid] = s; __syncthreads();
  for (int o = 128; o > 0; o >>= 1) { if (tid < o) red[tid] += red[tid + o]; __syncthreads(); }
  float mean = red[0] / 1024.f; __syncthreads();
  float q = 0; for (int i = 0; i < 4; ++i) { float d = v[i] - mean; q += d * d; }
  red[tid] = q; __syncthreads();
  for (int o = 128; o > 0; o >>= 1) { if (tid < o) red[tid] += red[tid + o]; __syncthreads(); }
  float var = red[0] / 1024.f; __syncthreads();
  float m = rbf(mean), vv = rbf(var);
  float den = rbf(sqrtf(rbf(vv + 1e-5f)));
  for (int i = 0; i < 4; ++i) out[tid + 256 * i] = rbf(rbf(v[i] - m) / den);
}

__global__ void k_ln1(const u16* x, float* xf, float* xn) {
  __shared__ float red[256];
  int b = blockIdx.x, tid = threadIdx.x;
  for (int i = 0; i < 4; ++i) xf[b * D + tid + 256 * i] = bf2f(x[b * D + tid + 256 * i]);
  __syncthreads();
  ln_row(xf + b * D, xn + b * D, tid, red);
}
__global__ void k_ln2(const float* h, float* hn) {
  __shared__ float red[256];
  ln_row(h + blockIdx.x * D, hn + blockIdx.x * D, threadIdx.x, red);
}

__global__ void k_gemv(const float* a, const u16* Wt, int K, int N, float* y, const float* resid, u16* outb, int mode) {
  __shared__ float red[16][8][16];
  int col = blockIdx.x * 16 + (threadIdx.x & 15), kg = threadIdx.x >> 4;
  float acc[8] = {0};
  for (int k = kg; k < K; k += 16) {
    float w = bf2f(Wt[(size_t)k * N + col]);
    for (int b = 0; b < 8; ++b) acc[b] += a[b * K + k] * w;
  }
  for (int b = 0; b < 8; ++b) red[kg][b][threadIdx.x & 15] = acc[b];
  __syncthreads();
  if (threadIdx.x < 128) {
    int b = threadIdx.x >> 4, c = threadIdx.x & 15;
    float s = 0; for (int g = 0; g < 16; ++g) s += red[g][b][c];
    int n = blockIdx.x * 16 + c;
    s = rbf(s);
    if (mode == 1) s = fmaxf(s, 0.f);
    if (mode >= 2) s = rbf(resid[b * N + n] + s);
    if (mode == 3) outb[b * N + n] = f2bf(s); else y[b * N + n] = s;
  }
}

__global__ void k_attn(const float* q, const u16* kc, const u16* vc, float* o) {
  __shared__ float sc[W]; __shared__ float red[256]; __shared__ float qs[DH];
  int b = blockIdx.x / HQ, h = blockIdx.x % HQ, kv = h / 8, tid = threadIdx.x;
  if (tid < DH) qs[tid] = q[b * HQ * DH + h * DH + tid];
  __syncthreads();
  const u16* kb = kc + ((size_t)(b * HKV + kv) * SK + (SK - W)) * DH;
  const u16* vb = vc + ((size_t)(b * HKV + kv) * SK + (SK - W)) * DH;
  float mx = -INFINITY;
  for (int j = tid; j < W; j += 256) {
    float s = 0; for (int e = 0; e < DH; ++e) s += qs[e] * bf2f(kb[j * DH + e]);
    s = rbf(rbf(s) * 0.08838834764831845f);
    sc[j] = s; mx = fmaxf(mx, s);
  }
  red[tid] = mx; __syncthreads();
  for (int o2 = 128; o2 > 0; o2 >>= 1) { if (tid < o2) red[tid] = fmaxf(red[tid], red[tid + o2]); __syncthreads(); }
  mx = red[0]; __syncthreads();
  float sum = 0;
  for (int j = tid; j < W; j += 256) { float p = expf(sc[j] - mx); sc[j] = p; sum += p; }
  red[tid] = sum; __syncthreads();
  for (int o2 = 128; o2 > 0; o2 >>= 1) { if (tid < o2) red[tid] += red[tid + o2]; __syncthreads(); }
  sum = red[0]; __syncthreads();
  int e = tid & 127, half = tid >> 7;
  float acc = 0;
  for (int j = half; j < W; j += 2) acc += rbf(sc[j] / sum) * bf2f(vb[j * DH + e]);
  red[tid] = acc; __syncthreads();
  if (tid < 128) o[b * HQ * DH + h * DH + tid] = rbf(red[tid] + red[tid + 128]);
}

extern "C" void kernel_launch(void* const* d_in, const int* in_sizes, int n_in, void* d_out, int out_size, void* d_ws, size_t ws_size, hipStream_t stream) {
  const u16* x = (const u16*)d_in[0]; const u16* kc = (const u16*)d_in[1]; const u16* vc = (const u16*)d_in[2];
  const u16* wq = (const u16*)d_in[3]; const u16* wo = (const u16*)d_in[4];
  const u16* w1 = (const u16*)d_in[6]; const u16* w2 = (const u16*)d_in[7];
  float* ws = (float*)d_ws;
  float* xf = ws; float* xn = xf + 8192; float* q = xn + 8192; float* o = q + 32768; float* h = o + 32768; float* hn = h + 8192; float* g = hn + 8192;
  k_ln1<<<8, 256, 0, stream>>>(x, xf, xn);
  k_gemv<<<4096 / 16, 256, 0, stream>>>(xn, wq, 1024, 4096, q, nullptr, nullptr, 0);
  k_attn<<<256, 256, 0, stream>>>(q, kc, vc, o);
  k_gemv<<<1024 / 16, 256, 0, stream>>>(o, wo, 4096, 1024, h, xf, nullptr, 2);
  k_ln2<<<8, 256, 0, stream>>>(h, hn);
  k_gemv<<<3584 / 16, 256, 0, stream>>>(hn, w1, 1024, 3584, g, nullptr, nullptr, 1);
  k_gemv<<<1024 / 16, 256, 0, stream>>>(g, w2, 3584, 1024, nullptr, h, (u16*)d_out, 3);
}
```

```cpp
#include <hip/hip_runtime.h>
#include <hip/hip_bf16.h>
#include <stdint.h>

#define DIAG 0
#define NTW 1
#define A_START_DELAY 0
#define A_Q_DELAY 60
#define B_WO_DELAY 0
#define B_R_DELAY 8
#define B_F_DELAY 40

#define LAS __attribute__((address_space(3)))
typedef unsigned u32;
typedef unsigned long long u64;
typedef unsigned v4u __attribute__((ext_vector_type(4)));
typedef unsigned v2u __attribute__((ext_vector_type(2)));
typedef float f32x4 __attribute__((ext_vector_type(4)));
typedef short s16x4 __attribute__((ext_vector_type(4)));
typedef short s16x8 __attribute__((ext_vector_type(8)));
typedef __bf16 bf16x2 __attribute__((ext_vector_type(2)));
typedef __bf16 bf16x8 __attribute__((ext_vector_type(8)));
typedef __amdgpu_buffer_rsrc_t rsrc_t;

namespace {

constexpr int NWG = 256, NTHR = 512;
constexpr u32 SENT = 0xFFFFFFFFu;
constexpr int AUX_SC1 = 16;
constexpr int SPIN_CAP = 20000;

constexpr int WS_EPOCH = 0;
constexpr int WS_FLAG = 1024;
constexpr int WS_LSE = 4096;
constexpr int WS_H = 32768;
constexpr int WS_QG = 65536;
constexpr int WS_OP = 1 << 20;
constexpr int WS_WP = 2 << 20;
constexpr int WS_MP = 4 << 20;
constexpr int WS_END = 6 << 20;
constexpr int WS_STAMP = 8 << 20;

constexpr int L_R0 = 0, L_R1 = 65536, L_MISC = 131072;
constexpr int L_ACT = L_MISC;
constexpr int L_P = L_ACT + 8 * 2080;
constexpr int L_ST = L_P + 4736;
constexpr int L_BYTES = L_ST + 2560;

__device__ __forceinline__ float bflo(u32 v) { return __uint_as_float(v << 16); }
__device__ __forceinline__ float bfhi(u32 v) { return __uint_as_float(v & 0xFFFF0000u); }
__device__ __forceinline__ u32 pk2(float lo, float hi) {
  bf16x2 r = {(__bf16)lo, (__bf16)hi};
  return __builtin_bit_cast(u32, r);
}
__device__ __forceinline__ float rbf(float x) { return (float)(__bf16)x; }

template <int CTRL> __device__ __forceinline__ float dpp(float x) {
  return __builtin_bit_cast(float, __builtin_amdgcn_mov_dpp(__builtin_bit_cast(int, x), CTRL, 0xf, 0xf, true));
}
constexpr int DPP_X1 = 0xB1, DPP_X2 = 0x4E, DPP_HM = 0x141, DPP_M = 0x140, DPP_R8 = 0x128;
__device__ __forceinline__ float sum16_32(float x) {
  auto s = __builtin_amdgcn_permlane16_swap(__float_as_uint(x), __float_as_uint(x), false, false);
  x = __uint_as_float(s[0]) + __uint_as_float(s[1]);
  auto t = __builtin_amdgcn_permlane32_swap(__float_as_uint(x), __float_as_uint(x), false, false);
  return __uint_as_float(t[0]) + __uint_as_float(t[1]);
}
__device__ __forceinline__ float max16_32(float x) {
  auto s = __builtin_amdgcn_permlane16_swap(__float_as_uint(x), __float_as_uint(x), false, false);
  x = fmaxf(__uint_as_float(s[0]), __uint_as_float(s[1]));
  auto t = __builtin_amdgcn_permlane32_swap(__float_as_uint(x), __float_as_uint(x), false, false);
  return fmaxf(__uint_as_float(t[0]), __uint_as_float(t[1]));
}
__device__ __forceinline__ float wave_sum(float x) {
  x += dpp<DPP_X1>(x); x += dpp<DPP_X2>(x); x += dpp<DPP_HM>(x); x += dpp<DPP_M>(x);
  return sum16_32(x);
}
__device__ __forceinline__ f32x4 mfma(s16x8 a, s16x8 b, f32x4 c) {
  return __builtin_amdgcn_mfma_f32_16x16x32_bf16(__builtin_bit_cast(bf16x8, a), __builtin_bit_cast(bf16x8, b), c, 0, 0, 0);
}
__device__ __forceinline__ s16x8 trfrag(LAS char* p, int half_off) {
  typedef short v4i16_t __attribute__((ext_vector_type(4)));
  s16x4 lo = __builtin_amdgcn_ds_read_tr16_b64_v4i16((LAS v4i16_t*)p);
  s16x4 hi = __builtin_amdgcn_ds_read_tr16_b64_v4i16((LAS v4i16_t*)(p + half_off));
  return __builtin_shufflevector(lo, hi, 0, 1, 2, 3, 4, 5, 6, 7);
}
__device__ __forceinline__ void dma(rsrc_t rs, u32 voff, u32 soff, u32 lds) {
  asm volatile("s_mov_b32 m0, %0\n\ts_nop 0\n\tbuffer_load_dwordx4 %1, %2, %3 offen nt lds" ::"s"(lds), "v"(voff), "s"(rs), "s"(soff) : "memory");
}
#define BARRIER() asm volatile("s_waitcnt lgkmcnt(0)\n\ts_barrier" ::: "memory")
#define POLL_LOOP _Pragma("nounroll") for (int it = 0; it < SPIN_CAP; ++it)
#define RELOAD() asm volatile("" ::: "memory")
#define VMWAIT(n) asm volatile("s_waitcnt vmcnt(" #n ")" ::: "memory")
template <int N> __device__ __forceinline__ void nap() {
  if constexpr (N > 0) { __builtin_amdgcn_s_sleep(N > 127 ? 127 : N); nap<(N > 127 ? N - 127 : 0)>(); }
}
__device__ __forceinline__ v4u ld16(rsrc_t rs, u32 off) { return __builtin_amdgcn_raw_buffer_load_b128(rs, off, 0, AUX_SC1); }
__device__ __forceinline__ u32 ld4(rsrc_t rs, u32 off) { return __builtin_amdgcn_raw_buffer_load_b32(rs, off, 0, AUX_SC1); }
__device__ __forceinline__ void st16(rsrc_t rs, u32 off, v4u v) { __builtin_amdgcn_raw_buffer_store_b128(v, rs, off, 0, AUX_SC1); }
__device__ __forceinline__ bool nosent(v4u v) { return (v.x != SENT) & (v.y != SENT) & (v.z != SENT) & (v.w != SENT); }

__device__ __forceinline__ void ln_row(const v4u a, const v4u b, v4u& oa, v4u& ob) {
  float v[16];
#pragma unroll
  for (int i = 0; i < 4; ++i) { v[2 * i] = bflo(a[i]); v[2 * i + 1] = bfhi(a[i]); v[8 + 2 * i] = bflo(b[i]); v[9 + 2 * i] = bfhi(b[i]); }
  float s = 0.f;
#pragma unroll
  for (int i = 0; i < 16; ++i) s += v[i];
  const float mean = wave_sum(s) * (1.f / 1024.f);
  float q = 0.f;
#pragma unroll
  for (int i = 0; i < 16; ++i) { const float d = v[i] - mean; q = fmaf(d, d, q); }
  const float var = wave_sum(q) * (1.f / 1024.f);
  const float mb = rbf(mean);
  const float den = rbf(__builtin_amdgcn_sqrtf(rbf(rbf(var) + 1e-5f)));
  const float inv = __builtin_amdgcn_rcpf(den);
#pragma unroll
  for (int i = 0; i < 4; ++i) {
    const u32 da = pk2(v[2 * i] - mb, v[2 * i + 1] - mb), db = pk2(v[8 + 2 * i] - mb, v[9 + 2 * i] - mb);
    oa[i] = pk2(bflo(da) * inv, bfhi(da) * inv);
    ob[i] = pk2(bflo(db) * inv, bfhi(db) * inv);
  }
}

#define STAMP(i) do {} while (0)
#define STAMP_DECL() do {} while (0)
#define STAMP_FLUSH() do {} while (0)

}

__global__ void __launch_bounds__(NTHR, 2)
fused_decode_layer(const char* __restrict__ x, const char* __restrict__ kc, const char* __restrict__ vc, const char* __restrict__ wq,
                   const char* __restrict__ wo, const char* __restrict__ w1, const char* __restrict__ w2, char* __restrict__ out, char* ws) {
  extern __shared__ __attribute__((aligned(16))) char smem[];
  LAS char* L = (LAS char*)smem;
  const u32 lbase = (u32)(size_t)L;
  const int tid = threadIdx.x, l = tid & 63, w = __builtin_amdgcn_readfirstlane(tid >> 6), c = blockIdx.x;
  const int g = l >> 4, q4 = (l >> 2) & 3, p4 = l & 3, b8 = l & 7, d8 = (l >> 3) & 1;
  const int fsw = q4 | ((g & 1) << 2);
  const rsrc_t rsW = __builtin_amdgcn_make_buffer_rsrc((void*)ws, 0, WS_END, 0x00020000);
  const v4u sent4 = {SENT, SENT, SENT, SENT};
  STAMP_DECL();
  STAMP(0);

  if (c < 128) {
    const int j = c, xcd = j & 7, sj = j >> 3;
    const int cg = j >> 1, kh = j & 1;
    const int kvh = sj >> 2, et = xcd, nq = sj & 3;
    const int rb = j >> 4, cb = j & 15;
    u32 tagLo = 0, tagHi = 0;
    v2u xres = {0u, 0u};
    if (w < 4) {
      const rsrc_t rs = __builtin_amdgcn_make_buffer_rsrc((void*)wq, 0, 1024 * 4096 * 2, 0x00020000);
      const int r = l >> 3;
      const u32 v0 = r * 8192 + (((((l & 7) >> 1) ^ ((r >> 1) & 1)) << 1) | (l & 1)) * 16;
      const u32 v1 = v0 ^ 64;
      const u32 sb = (512 * kh + 128 * w) * 8192 + 128 * cg;
#pragma unroll
      for (int i = 0; i < 16; ++i) dma(rs, (i & 1) ? v1 : v0, sb + i * 8 * 8192, lbase + L_R0 + (16 * w + i) * 1024);
      const v2u e = __builtin_amdgcn_raw_buffer_load_b64(rsW, WS_EPOCH, 0, AUX_SC1);
      if (w == 0) {
        const rsrc_t rx = __builtin_amdgcn_make_buffer_rsrc((void*)x, 0, 8 * 1024 * 2, 0x00020000);
        xres = __builtin_amdgcn_raw_buffer_load_b64(rx, rb * 2048 + cb * 128 + (l & 15) * 8, 0, 0);
      }
      tagLo = e.x + 1u; tagHi = e.y + 1u;
      STAMP(1);
      VMWAIT(0);
      STAMP(2);
    } else {
      const rsrc_t rx = __builtin_amdgcn_make_buffer_rsrc((void*)x, 0, 8 * 1024 * 2, 0x00020000);
      const int r0 = 2 * (w - 4);
      v4u xa[2], xb[2];
#pragma unroll
      for (int i = 0; i < 2; ++i) {
        xa[i] = __builtin_amdgcn_raw_buffer_load_b128(rx, (r0 + i) * 2048 + l * 16, 0, 0);
        xb[i] = __builtin_amdgcn_raw_buffer_load_b128(rx, (r0 + i) * 2048 + 1024 + l * 16, 0, 0);
      }
      const v2u e = __builtin_amdgcn_raw_buffer_load_b64(rsW, WS_EPOCH, 0, AUX_SC1);
      tagLo = e.x + 1u; tagHi = e.y + 1u;
#pragma unroll
      for (int i = 0; i < 2; ++i) {
        v4u oa, ob;
        ln_row(xa[i], xb[i], oa, ob);
        *(LAS v4u*)(L + L_ACT + (r0 + i) * 1056 + l * 16) = kh ? ob : oa;
      }
      STAMP(2);
    }
    BARRIER();
    if (w < 4) {
      LAS char* pa = L + L_R0 + (8 * g + q4) * 128 + ((w ^ (((q4 >> 1) & 1) | ((g & 1) << 1))) << 5) + 8 * p4;
      LAS char* pb = L + L_ACT + b8 * 1056 + 16 * g;
      f32x4 acc = {0.f, 0.f, 0.f, 0.f};
#pragma unroll
      for (int ks = 0; ks < 16; ++ks) acc = mfma(trfrag(pa + ks * 4096, 512), *(LAS s16x8*)(pb + ks * 64), acc);
      const int col = 64 * cg + 16 * w + 4 * g + 2 * d8;
      v4u o;
      o.x = __float_as_uint(d8 ? acc[2] : acc[0]); o.y = tagLo; o.z = __float_as_uint(d8 ? acc[3] : acc[1]); o.w = tagHi;
      st16(rsW, WS_QG + (((b8 * 4 + (col >> 10)) * 2 + kh) * 1024 + (col & 1023)) * 8, o);
      STAMP(3);
    } else {
      nap<6>();
      const int u = w - 4;
#pragma unroll
      for (int i = 0; i < 2; ++i) st16(rsW, WS_WP + j * 8192 + (2 * u + i) * 1024 + l * 16, sent4);
#pragma unroll
      for (int i = 0; i < 4; ++i) if (4 * u + i < 14) st16(rsW, WS_MP + j * 16384 + (4 * u + i) * 1024 + l * 16, sent4);
      if (u == 3 && l < 8) st16(rsW, WS_H + rb * 2048 + cb * 128 + l * 16, sent4);
      VMWAIT(0);
      STAMP(3);
    }
    BARRIER();
    if (tid == 448) __builtin_amdgcn_raw_buffer_store_b64(v2u{tagLo, tagHi}, rsW, WS_FLAG + c * 8, 0, AUX_SC1);
    nap<B_WO_DELAY>();
    {
      const rsrc_t rs = __builtin_amdgcn_make_buffer_rsrc((void*)wo, 0, 4096 * 1024 * 2, 0x00020000);
      const u32 v0 = (l >> 5) * 2048 + (((((l & 31) >> 1) ^ (l >> 5)) << 1) | (l & 1)) * 16;
      const u32 sb = ((kvh * 8 + w) * 128 + 16 * et) * 2048 + 512 * nq;
#pragma unroll
      for (int i = 0; i < 8; ++i) dma(rs, v0 ^ (64 * (i & 1)) ^ (128 * (i >> 2)), sb + i * 4096, lbase + L_R1 + (8 * w + i) * 1024);
    }
    STAMP(4);
    if (w == 7) {
      POLL_LOOP {
        RELOAD();
        const v4u f0 = ld16(rsW, WS_FLAG + l * 16), f1 = ld16(rsW, WS_FLAG + 1024 + l * 16);
        const bool ok = (f0.x == tagLo) & (f0.y == tagHi) & (f0.z == tagLo) & (f0.w == tagHi) & (f1.x == tagLo) & (f1.y == tagHi) & (f1.z == tagLo) & (f1.w == tagHi);
        if (__all(ok)) break;
        nap<2>();
      }
    }
    VMWAIT(0);
    STAMP(5);
    BARRIER();
    s16x8 wf[2][4];
#pragma unroll
    for (int T = 0; T < 2; ++T) {
      LAS char* pa = L + L_R1 + (8 * g + q4) * 512 + (((2 * w + T) ^ fsw) << 5) + 8 * p4;
#pragma unroll
      for (int ks = 0; ks < 4; ++ks) wf[T][ks] = trfrag(pa + ks * 16384, 2048);
    }
    {
      const int hh = (l >> 2) & 7, sh = l >> 5;
      const u32 lo0 = WS_LSE + ((w * 4 + kvh) * 4 + sh) * 128 + hh * 4;
      u32 e0 = SENT, e1 = SENT;
      POLL_LOOP {
        RELOAD();
        e0 = ld4(rsW, lo0); e1 = ld4(rsW, lo0 + 256);
        if (__all((e0 != SENT) & (e1 != SENT))) break;
        nap<1>();
      }
      STAMP(6);
      const u32 ob = WS_OP + ((kvh * 8 + et) * 8 + w) * 2048 + l * 16;
      v4u d0 = sent4, d1 = sent4;
      POLL_LOOP {
        RELOAD();
        d0 = ld16(rsW, ob); d1 = ld16(rsW, ob + 1024);
        if (__all(nosent(d0) & nosent(d1))) break;
      }
      STAMP(7);
      const float l0 = __uint_as_float(e0), l1 = __uint_as_float(e1);
      float mx = fmaxf(l0, l1);
      { auto t = __builtin_amdgcn_permlane32_swap(__float_as_uint(mx), __float_as_uint(mx), false, false); mx = fmaxf(__uint_as_float(t[0]), __uint_as_float(t[1])); }
      const float w0 = __builtin_amdgcn_exp2f(l0 - mx), w1_ = __builtin_amdgcn_exp2f(l1 - mx);
      float tot = w0 + w1_;
      { auto t = __builtin_amdgcn_permlane32_swap(__float_as_uint(tot), __float_as_uint(tot), false, false); tot = __uint_as_float(t[0]) + __uint_as_float(t[1]); }
      const float it_ = __builtin_amdgcn_rcpf(tot);
      float o[4];
#pragma unroll
      for (int i = 0; i < 4; ++i) {
        const float v = w0 * __uint_as_float(d0[i]) + w1_ * __uint_as_float(d1[i]);
        auto t = __builtin_amdgcn_permlane32_swap(__float_as_uint(v), __float_as_uint(v), false, false);
        o[i] = (__uint_as_float(t[0]) + __uint_as_float(t[1])) * it_;
      }
      if (l < 32) *(LAS v2u*)(L + L_ACT + w * 288 + l * 8) = v2u{pk2(o[0], o[1]), pk2(o[2], o[3])};
    }
    BARRIER();
    {
      LAS char* pb = L + L_ACT + b8 * 288 + 16 * g;
      f32x4 a0 = {0.f, 0.f, 0.f, 0.f}, a1 = {0.f, 0.f, 0.f, 0.f};
#pragma unroll
      for (int ks = 0; ks < 4; ++ks) { const s16x8 bf = *(LAS s16x8*)(pb + ks * 64); a0 = mfma(wf[0][ks], bf, a0); a1 = mfma(wf[1][ks], bf, a1); }
      const int col = 256 * nq + 32 * w + 16 * d8 + 4 * g;
      v4u o;
#pragma unroll
      for (int i = 0; i < 4; ++i) o[i] = __float_as_uint(d8 ? a1[i] : a0[i]);
      st16(rsW, WS_WP + (b8 * 16 + (col >> 6)) * 8192 + (kvh * 8 + et) * 256 + (col & 63) * 4, o);
    }
    STAMP(8);
    if (w == 0) {
      nap<B_R_DELAY>();
      v4u d[8];
      POLL_LOOP {
        RELOAD();
        bool ok = true;
#pragma unroll
        for (int i = 0; i < 8; ++i) d[i] = ld16(rsW, WS_WP + j * 8192 + i * 1024 + l * 16);
#pragma unroll
        for (int i = 0; i < 8; ++i) ok &= nosent(d[i]);
        if (__all(ok)) break;
      }
      STAMP(9);
      float hv[4];
#pragma unroll
      for (int k = 0; k < 4; ++k) {
        float s = ((__uint_as_float(d[0][k]) + __uint_as_float(d[1][k])) + (__uint_as_float(d[2][k]) + __uint_as_float(d[3][k]))) +
                  ((__uint_as_float(d[4][k]) + __uint_as_float(d[5][k])) + (__uint_as_float(d[6][k]) + __uint_as_float(d[7][k])));
        s = sum16_32(s);
        const float xv = (k & 1) ? bfhi(xres[k >> 1]) : bflo(xres[k >> 1]);
        hv[k] = xv + rbf(s);
      }
      const v2u hp = {pk2(hv[0], hv[1]), pk2(hv[2], hv[3])};
      if (l < 16) {
        __builtin_amdgcn_raw_buffer_store_b64(hp, rsW, WS_H + rb * 2048 + cb * 128 + l * 8, 0, AUX_SC1);
        *(LAS f32x4*)(L + L_ST + 2048 + l * 16) = f32x4{bflo(hp.x), bfhi(hp.x), bflo(hp.y), bfhi(hp.y)};
      }
      STAMP(10);
    }
    BARRIER();
    if (w < 7) {
      nap<B_F_DELAY>();
      const u32 mb = WS_MP + j * 16384 + w * 2048 + l * 16;
      v4u d0 = sent4, d1 = sent4;
      POLL_LOOP {
        RELOAD();
        d0 = ld16(rsW, mb); d1 = ld16(rsW, mb + 1024);
        if (__all(nosent(d0) & nosent(d1))) break;
        nap<1>();
      }
      STAMP(11);
      float v[8];
#pragma unroll
      for (int i = 0; i < 4; ++i) { v[2 * i] = bflo(d0[i]) + bflo(d1[i]); v[2 * i + 1] = bfhi(d0[i]) + bfhi(d1[i]); }
#pragma unroll
      for (int i = 0; i < 4; ++i) { auto t = __builtin_amdgcn_permlane32_swap(__float_as_uint(v[i]), __float_as_uint(v[i + 4]), false, false); v[i] = __uint_as_float(t[0]) + __uint_as_float(t[1]); }
#pragma unroll
      for (int i = 0; i < 2; ++i) { auto t = __builtin_amdgcn_permlane16_swap(__float_as_uint(v[i]), __float_as_uint(v[i + 2]), false, false); v[i] = __uint_as_float(t[0]) + __uint_as_float(t[1]); }
      const float t0 = v[0] + dpp<DPP_R8>(v[0]), t1 = v[1] + dpp<DPP_R8>(v[1]);
      *(LAS float*)(L + L_ST + w * 256 + l * 4) = d8 ? t1 : t0;
    }
    BARRIER();
    if (w == 7) {
      float s = 0.f;
#pragma unroll
      for (int i = 0; i < 7; ++i) s += *(LAS float*)(L + L_ST + i * 256 + l * 4);
      const int e = l >> 3;
      const int col = 32 * (b8 >> 2) + 16 * (e >> 2) + 4 * (b8 & 3) + (e & 3);
      const float hv = *(LAS float*)(L + L_ST + 2048 + col * 4);
      const u32 r = pk2(hv + rbf(s), 0.f);
      const rsrc_t ro = __builtin_amdgcn_make_buffer_rsrc((void*)out, 0, 8 * 1024 * 2, 0x00020000);
      __builtin_amdgcn_raw_buffer_store_b16((unsigned short)r, ro, (rb * 1024 + cb * 64 + col) * 2, 0, 0);
      if (c == 0 && l == 0) __builtin_amdgcn_raw_buffer_store_b64(v2u{tagLo, tagHi}, rsW, WS_EPOCH, 0, AUX_SC1);
      STAMP(12);
    }
    STAMP_FLUSH();
    return;
  }

  const int a = c - 128, xcd = a & 7, sa = a >> 3;
  const int s = sa & 3, bk = (sa >> 2) * 8 + xcd, b = bk >> 2, kvh = bk & 3;
  const int m = ((sa >> 1) * 8 + xcd) * 2 + (sa & 1);
  u32 tagLo = 0, tagHi = 0;
  if (w < 4) {
    nap<A_START_DELAY>();
    const rsrc_t rk = __builtin_amdgcn_make_buffer_rsrc((void*)kc, 0, 8 * 4 * 4096 * 128 * 2, 0x00020000);
    const rsrc_t rv = __builtin_amdgcn_make_buffer_rsrc((void*)vc, 0, 8 * 4 * 4096 * 128 * 2, 0x00020000);
    const u32 sb = (bk * 4096 + 3072 + 256 * s + 64 * w) * 256;
    const u32 vk = g * 256 + ((l & 15) ^ g) * 16;
#pragma unroll
    for (int i = 0; i < 16; ++i) dma(rk, vk ^ (64 * (i & 3)), sb + i * 1024, lbase + L_R0 + (16 * w + i) * 1024);
    const u32 vv = g * 256 + (((((l & 15) >> 1) ^ g) << 1) | (l & 1)) * 16;
#pragma unroll
    for (int i = 0; i < 16; ++i) dma(rv, vv ^ (128 * ((i >> 1) & 1)), sb + i * 1024, lbase + L_R1 + (16 * w + i) * 1024);
    STAMP(1);
    VMWAIT(16);
    STAMP(2);
  } else {
    const int u = w - 4;
    const v2u e = __builtin_amdgcn_raw_buffer_load_b64(rsW, WS_EPOCH, 0, AUX_SC1);
    if (u == 0) {
#pragma unroll
      for (int i = 0; i < 4; ++i) st16(rsW, WS_OP + (((kvh * 8 + 2 * i + (l >> 5)) * 8 + b) * 4 + s) * 512 + (l & 31) * 16, sent4);
      if (l < 2) st16(rsW, WS_LSE + (bk * 4 + s) * 128 + l * 16, sent4);
    }
    tagLo = e.x + 1u; tagHi = e.y + 1u;
    if (u == 0) {
      VMWAIT(0);
      if (l == 0) __builtin_amdgcn_raw_buffer_store_b64(v2u{tagLo, tagHi}, rsW, WS_FLAG + c * 8, 0, AUX_SC1);
    }
    STAMP(1);
    nap<A_Q_DELAY>();
    const u32 qb = WS_QG + bk * 16384 + u * 2048 + l * 16;
    v4u d[4];
    POLL_LOOP {
        RELOAD();
      bool ok = true;
#pragma unroll
      for (int i = 0; i < 4; ++i) d[i] = ld16(rsW, qb + (i >> 1) * 8192 + (i & 1) * 1024);
#pragma unroll
      for (int i = 0; i < 4; ++i) ok &= (d[i].y == tagLo) & (d[i].w == tagHi);
      if (__all(ok)) break;
      nap<1>();
    }
#pragma unroll
    for (int i = 0; i < 2; ++i)
      *(LAS u32*)(L + L_ACT + (2 * u + i) * 288 + l * 4) = pk2(__uint_as_float(d[i].x) + __uint_as_float(d[i + 2].x), __uint_as_float(d[i].z) + __uint_as_float(d[i + 2].z));
    STAMP(2);
  }
  BARRIER();
  STAMP(3);
  float sc[4], mrow;
  {
    LAS char* pb = L + L_ACT + b8 * 288 + 16 * g;
    LAS char* pk = L + L_R0 + (32 * w + (l & 15)) * 256;
    f32x4 a0 = {0.f, 0.f, 0.f, 0.f}, a1 = {0.f, 0.f, 0.f, 0.f};
#pragma unroll
    for (int ks = 0; ks < 4; ++ks) {
      const s16x8 bf = *(LAS s16x8*)(pb + ks * 64);
      const int pos = ((4 * ks + g) ^ (l & 15)) * 16;
      a0 = mfma(*(LAS s16x8*)(pk + pos), bf, a0);
      a1 = mfma(*(LAS s16x8*)(pk + 4096 + pos), bf, a1);
    }
    float mx = -INFINITY;
#pragma unroll
    for (int r = 0; r < 4; ++r) { sc[r] = rbf(rbf(d8 ? a1[r] : a0[r]) * 0.08838834764831845f); mx = fmaxf(mx, sc[r]); }
    mx = fmaxf(mx, dpp<DPP_R8>(mx));
    mx = max16_32(mx);
    if (l < 8) *(LAS float*)(L + L_ST + l * 32 + w * 4) = mx;
  }
  BARRIER();
  {
    const f32x4 m0 = *(LAS f32x4*)(L + L_ST + b8 * 32), m1 = *(LAS f32x4*)(L + L_ST + b8 * 32 + 16);
    mrow = fmaxf(fmaxf(fmaxf(m0[0], m0[1]), fmaxf(m0[2], m0[3])), fmaxf(fmaxf(m1[0], m1[1]), fmaxf(m1[2], m1[3])));
    float pr[4], sm = 0.f;
#pragma unroll
    for (int r = 0; r < 4; ++r) { pr[r] = __builtin_amdgcn_exp2f((sc[r] - mrow) * 1.4426950408889634f); sm += pr[r]; }
    *(LAS v2u*)(L + L_P + b8 * 544 + (32 * w + 16 * d8 + 4 * g) * 2) = v2u{pk2(pr[0], pr[1]), pk2(pr[2], pr[3])};
    sm += dpp<DPP_R8>(sm);
    sm = sum16_32(sm);
    if (l < 8) *(LAS float*)(L + L_ST + 256 + l * 32 + w * 4) = sm;
  }
  BARRIER();
  s16x8 pf[8];
  float invl;
  {
    const f32x4 s0 = *(LAS f32x4*)(L + L_ST + 256 + b8 * 32), s1 = *(LAS f32x4*)(L + L_ST + 256 + b8 * 32 + 16);
    const float tot = ((s0[0] + s0[1]) + (s0[2] + s0[3])) + ((s1[0] + s1[1]) + (s1[2] + s1[3]));
    if (w == 4 && l < 8)
      __builtin_amdgcn_raw_buffer_store_b32(__float_as_uint(mrow * 1.4426950408889634f + __builtin_amdgcn_logf(tot)), rsW, WS_LSE + (bk * 4 + s) * 128 + l * 4, 0, AUX_SC1);
    invl = __builtin_amdgcn_rcpf(tot);
#pragma unroll
    for (int ks = 0; ks < 8; ++ks) pf[ks] = *(LAS s16x8*)(L + L_P + b8 * 544 + ks * 64 + 16 * g);
  }
  STAMP(4);
  if (w < 4) VMWAIT(0);
  STAMP(5);
  BARRIER();
  {
    LAS char* pa = L + L_R1 + (8 * g + q4) * 256 + ((w ^ fsw) << 5) + 8 * p4;
    f32x4 acc = {0.f, 0.f, 0.f, 0.f};
#pragma unroll
    for (int ks = 0; ks < 8; ++ks) acc = mfma(trfrag(pa + ks * 8192, 1024), pf[ks], acc);
    v4u o;
#pragma unroll
    for (int i = 0; i < 4; ++i) o[i] = __float_as_uint(acc[i] * invl);
    if (!d8) st16(rsW, WS_OP + (((kvh * 8 + w) * 8 + b) * 4 + s) * 512 + b8 * 64 + g * 16, o);
  }
  STAMP(6);
  if (sa >= 14) { STAMP_FLUSH(); return; }
  BARRIER();
  {
    const rsrc_t r1 = __builtin_amdgcn_make_buffer_rsrc((void*)w1, 0, 1024 * 3584 * 2, 0x00020000);
    const u32 v1 = (l >> 2) * 7168 + ((((l >> 1) & 1) ^ (l >> 5)) << 5) + (l & 1) * 16;
    const u32 sb1 = 128 * w * 7168 + 64 * m;
#pragma unroll
    for (int i = 0; i < 8; ++i) dma(r1, v1, sb1 + i * 16 * 7168, lbase + L_R0 + (8 * w + i) * 1024);
    const rsrc_t r2 = __builtin_amdgcn_make_buffer_rsrc((void*)w2, 0, 3584 * 1024 * 2, 0x00020000);
    const u32 v2 = ((((l >> 1) ^ (((w >> 1) & 1) << 2)) << 1) | (l & 1)) * 16;
    const u32 sb2 = (32 * m + 4 * w) * 2048;
#pragma unroll
    for (int i = 0; i < 8; ++i) dma(r2, v2 ^ (32 * (i >> 1)), sb2 + i * 1024, lbase + L_R1 + (8 * w + i) * 1024);
  }
  STAMP(7);
  if (w == 7) {
    POLL_LOOP {
        RELOAD();
      const v4u f0 = ld16(rsW, WS_FLAG + l * 16);
      if (__all((f0.x == tagLo) & (f0.y == tagHi) & (f0.z == tagLo) & (f0.w == tagHi))) break;
      nap<2>();
    }
  }
  VMWAIT(0);
  STAMP(8);
  BARRIER();
  const int nt = w & 1, kq = w >> 1;
  s16x8 f1[8], f2[8];
  {
    LAS char* pa = L + L_R0 + (256 * kq + 8 * g + q4) * 64 + ((nt ^ (g & 1)) << 5) + 8 * p4;
#pragma unroll
    for (int ks = 0; ks < 8; ++ks) f1[ks] = trfrag(pa + ks * 2048, 256);
#pragma unroll
    for (int T = 0; T < 8; ++T) f2[T] = trfrag(L + L_R1 + (8 * g + q4) * 2048 + (((8 * w + T) ^ fsw) << 5) + 8 * p4, 8192);
  }
  {
    const u32 hb = WS_H + w * 2048 + l * 16;
    v4u ha = sent4, hc = sent4;
    POLL_LOOP {
        RELOAD();
      ha = ld16(rsW, hb); hc = ld16(rsW, hb + 1024);
      if (__all(nosent(ha) & nosent(hc))) break;
      nap<1>();
    }
    STAMP(9);
    v4u oa, ob;
    ln_row(ha, hc, oa, ob);
    *(LAS v4u*)(L + L_ACT + w * 2080 + l * 16) = oa;
    *(LAS v4u*)(L + L_ACT + w * 2080 + 1024 + l * 16) = ob;
  }
  BARRIER();
  {
    LAS char* pb = L + L_ACT + b8 * 2080 + kq * 512 + 16 * g;
    f32x4 acc = {0.f, 0.f, 0.f, 0.f};
#pragma unroll
    for (int ks = 0; ks < 8; ++ks) acc = mfma(f1[ks], *(LAS s16x8*)(pb + ks * 64), acc);
    if (!d8) *(LAS f32x4*)(L + L_P + (kq * 8 + b8) * 144 + (16 * nt + 4 * g) * 4) = acc;
  }
  BARRIER();
  {
    LAS char* pg = L + L_P + (2 * d8 * 8 + b8) * 144 + g * 32;
    const f32x4 x0 = *(LAS f32x4*)(pg) + *(LAS f32x4*)(pg + 8 * 144), x1 = *(LAS f32x4*)(pg + 16) + *(LAS f32x4*)(pg + 8 * 144 + 16);
    float gv[8];
#pragma unroll
    for (int i = 0; i < 4; ++i) { gv[i] = fmaxf(x0[i] + dpp<DPP_R8>(x0[i]), 0.f); gv[4 + i] = fmaxf(x1[i] + dpp<DPP_R8>(x1[i]), 0.f); }
    const v4u gp = {pk2(gv[0], gv[1]), pk2(gv[2], gv[3]), pk2(gv[4], gv[5]), pk2(gv[6], gv[7])};
    const s16x8 gf = __builtin_bit_cast(s16x8, gp);
    f32x4 o[8];
#pragma unroll
    for (int T = 0; T < 8; ++T) o[T] = mfma(f2[T], gf, f32x4{0.f, 0.f, 0.f, 0.f});
#pragma unroll
    for (int i = 0; i < 2; ++i) {
      f32x4 u0, u1;
#pragma unroll
      for (int r = 0; r < 4; ++r) { u0[r] = d8 ? o[4 * i + 2][r] : o[4 * i][r]; u1[r] = d8 ? o[4 * i + 3][r] : o[4 * i + 1][r]; }
      const v4u pv = {pk2(u0[0], u0[1]), pk2(u0[2], u0[3]), pk2(u1[0], u1[1]), pk2(u1[2], u1[3])};
      st16(rsW, WS_MP + (b8 * 16 + 2 * w + i) * 16384 + m * 128 + (d8 * 4 + g) * 16, pv);
    }
  }
  STAMP(10);
  STAMP_FLUSH();
}

extern "C" void kernel_launch(void* const* d_in, const int* in_sizes, int n_in, void* d_out, int out_size, void* d_ws, size_t ws_size,
                              hipStream_t stream) {
  (void)hipFuncSetAttribute((const void*)fused_decode_layer, hipFuncAttributeMaxDynamicSharedMemorySize, L_BYTES);
  fused_decode_layer<<<NWG, NTHR, L_BYTES, stream>>>((const char*)d_in[0], (const char*)d_in[1], (const char*)d_in[2], (const char*)d_in[3],
                                                     (const char*)d_in[4], (const char*)d_in[6], (const char*)d_in[7], (char*)d_out, (char*)d_ws);
}
```

```cpp
#define EXP_ROLE 0
#define EXP_IDX 0
#include <hip/hip_runtime.h>
#include <hip/hip_bf16.h>
#include <stdint.h>

#define DIAG 1
#define NTW 1
#define A_START_DELAY 0
#define A_Q_DELAY 60
#define B_WO_DELAY 0
#define B_R_DELAY 8
#define B_F_DELAY 40

#define LAS __attribute__((address_space(3)))
typedef unsigned u32;
typedef unsigned long long u64;
typedef unsigned v4u __attribute__((ext_vector_type(4)));
typedef unsigned v2u __attribute__((ext_vector_type(2)));
typedef float f32x4 __attribute__((ext_vector_type(4)));
typedef short s16x4 __attribute__((ext_vector_type(4)));
typedef short s16x8 __attribute__((ext_vector_type(8)));
typedef __bf16 bf16x2 __attribute__((ext_vector_type(2)));
typedef __bf16 bf16x8 __attribute__((ext_vector_type(8)));
typedef __amdgpu_buffer_rsrc_t rsrc_t;

namespace {

constexpr int NWG = 256, NTHR = 512;
constexpr u32 SENT = 0xFFFFFFFFu;
constexpr int AUX_SC1 = 16;
constexpr int SPIN_CAP = 20000;

constexpr int WS_EPOCH = 0;
constexpr int WS_FLAG = 1024;
constexpr int WS_LSE = 4096;
constexpr int WS_H = 32768;
constexpr int WS_QG = 65536;
constexpr int WS_OP = 1 << 20;
constexpr int WS_WP = 2 << 20;
constexpr int WS_MP = 4 << 20;
constexpr int WS_END = 6 << 20;
constexpr int WS_STAMP = 8 << 20;

constexpr int L_R0 = 0, L_R1 = 65536, L_MISC = 131072;
constexpr int L_ACT = L_MISC;
constexpr int L_P = L_ACT + 8 * 2080;
constexpr int L_ST = L_P + 4736;
constexpr int L_BYTES = L_ST + 2560;

__device__ __forceinline__ float bflo(u32 v) { return __uint_as_float(v << 16); }
__device__ __forceinline__ float bfhi(u32 v) { return __uint_as_float(v & 0xFFFF0000u); }
__device__ __forceinline__ u32 pk2(float lo, float hi) {
  bf16x2 r = {(__bf16)lo, (__bf16)hi};
  return __builtin_bit_cast(u32, r);
}
__device__ __forceinline__ float rbf(float x) { return (float)(__bf16)x; }

template <int CTRL> __device__ __forceinline__ float dpp(float x) {
  return __builtin_bit_cast(float, __builtin_amdgcn_mov_dpp(__builtin_bit_cast(int, x), CTRL, 0xf, 0xf, true));
}
constexpr int DPP_X1 = 0xB1, DPP_X2 = 0x4E, DPP_HM = 0x141, DPP_M = 0x140, DPP_R8 = 0x128;
__device__ __forceinline__ float sum16_32(float x) {
  auto s = __builtin_amdgcn_permlane16_swap(__float_as_uint(x), __float_as_uint(x), false, false);
  x = __uint_as_float(s[0]) + __uint_as_float(s[1]);
  auto t = __builtin_amdgcn_permlane32_swap(__float_as_uint(x), __float_as_uint(x), false, false);
  return __uint_as_float(t[0]) + __uint_as_float(t[1]);
}
__device__ __forceinline__ float max16_32(float x) {
  auto s = __builtin_amdgcn_permlane16_swap(__float_as_uint(x), __float_as_uint(x), false, false);
  x = fmaxf(__uint_as_float(s[0]), __uint_as_float(s[1]));
  auto t = __builtin_amdgcn_permlane32_swap(__float_as_uint(x), __float_as_uint(x), false, false);
  return fmaxf(__uint_as_float(t[0]), __uint_as_float(t[1]));
}
__device__ __forceinline__ float wave_sum(float x) {
  x += dpp<DPP_X1>(x); x += dpp<DPP_X2>(x); x += dpp<DPP_HM>(x); x += dpp<DPP_M>(x);
  return sum16_32(x);
}
__device__ __forceinline__ f32x4 mfma(s16x8 a, s16x8 b, f32x4 c) {
  return __builtin_amdgcn_mfma_f32_16x16x32_bf16(__builtin_bit_cast(bf16x8, a), __builtin_bit_cast(bf16x8, b), c, 0, 0, 0);
}
__device__ __forceinline__ s16x8 trfrag(LAS char* p, int half_off) {
  typedef short v4i16_t __attribute__((ext_vector_type(4)));
  s16x4 lo = __builtin_amdgcn_ds_read_tr16_b64_v4i16((LAS v4i16_t*)p);
  s16x4 hi = __builtin_amdgcn_ds_read_tr16_b64_v4i16((LAS v4i16_t*)(p + half_off));
  return __builtin_shufflevector(lo, hi, 0, 1, 2, 3, 4, 5, 6, 7);
}
__device__ __forceinline__ void dma(rsrc_t rs, u32 voff, u32 soff, u32 lds) {
  asm volatile("s_mov_b32 m0, %0\n\ts_nop 0\n\tbuffer_load_dwordx4 %1, %2, %3 offen nt lds" ::"s"(lds), "v"(voff), "s"(rs), "s"(soff) : "memory");
}
#define BARRIER() asm volatile("s_waitcnt lgkmcnt(0)\n\ts_barrier" ::: "memory")
#define POLL_LOOP _Pragma("nounroll") for (int it = 0; it < SPIN_CAP; ++it)
#define RELOAD() asm volatile("" ::: "memory")
#define VMWAIT(n) asm volatile("s_waitcnt vmcnt(" #n ")" ::: "memory")
template <int N> __device__ __forceinline__ void nap() {
  if constexpr (N > 0) { __builtin_amdgcn_s_sleep(N > 127 ? 127 : N); nap<(N > 127 ? N - 127 : 0)>(); }
}
__device__ __forceinline__ v4u ld16(rsrc_t rs, u32 off) { return __builtin_amdgcn_raw_buffer_load_b128(rs, off, 0, AUX_SC1); }
__device__ __forceinline__ u32 ld4(rsrc_t rs, u32 off) { return __builtin_amdgcn_raw_buffer_load_b32(rs, off, 0, AUX_SC1); }
__device__ __forceinline__ void st16(rsrc_t rs, u32 off, v4u v) { __builtin_amdgcn_raw_buffer_store_b128(v, rs, off, 0, AUX_SC1); }
__device__ __forceinline__ bool nosent(v4u v) { return (v.x != SENT) & (v.y != SENT) & (v.z != SENT) & (v.w != SENT); }

__device__ __forceinline__ void ln_row(const v4u a, const v4u b, v4u& oa, v4u& ob) {
  float v[16];
#pragma unroll
  for (int i = 0; i < 4; ++i) { v[2 * i] = bflo(a[i]); v[2 * i + 1] = bfhi(a[i]); v[8 + 2 * i] = bflo(b[i]); v[9 + 2 * i] = bfhi(b[i]); }
  float s = 0.f;
#pragma unroll
  for (int i = 0; i < 16; ++i) s += v[i];
  const float mean = wave_sum(s) * (1.f / 1024.f);
  float q = 0.f;
#pragma unroll
  for (int i = 0; i < 16; ++i) { const float d = v[i] - mean; q = fmaf(d, d, q); }
  const float var = wave_sum(q) * (1.f / 1024.f);
  const float mb = rbf(mean);
  const float den = rbf(__builtin_amdgcn_sqrtf(rbf(rbf(var) + 1e-5f)));
  const float inv = __builtin_amdgcn_rcpf(den);
#pragma unroll
  for (int i = 0; i < 4; ++i) {
    const u32 da = pk2(v[2 * i] - mb, v[2 * i + 1] - mb), db = pk2(v[8 + 2 * i] - mb, v[9 + 2 * i] - mb);
    oa[i] = pk2(bflo(da) * inv, bfhi(da) * inv);
    ob[i] = pk2(bflo(db) * inv, bfhi(db) * inv);
  }
}

#define STAMP(i) do { __builtin_amdgcn_sched_barrier(0); st_[i] = __builtin_amdgcn_s_memrealtime(); __builtin_amdgcn_sched_barrier(0); } while (0)
#define STAMP_DECL() u64 st_[16]; _Pragma("unroll") for (int i_ = 0; i_ < 16; ++i_) st_[i_] = 0; const u64 ck0_ = __builtin_amdgcn_s_memtime()
#define STAMP_FLUSH() do { u64 tgt_ = st_[0] + 6000; if (((c < 128) == (EXP_ROLE == 0)) && st_[EXP_IDX] != 0) tgt_ += 20 * (st_[EXP_IDX] - st_[0]); \
  for (int i_ = 0; i_ < 200000; ++i_) { if (__builtin_amdgcn_s_memrealtime() >= tgt_) break; __builtin_amdgcn_s_sleep(2); } } while (0)

}

__global__ void __launch_bounds__(NTHR, 2)
fused_decode_layer(const char* __restrict__ x, const char* __restrict__ kc, const char* __restrict__ vc, const char* __restrict__ wq,
                   const char* __restrict__ wo, const char* __restrict__ w1, const char* __restrict__ w2, char* __restrict__ out, char* ws) {
  extern __shared__ __attribute__((aligned(16))) char smem[];
  LAS char* L = (LAS char*)smem;
  const u32 lbase = (u32)(size_t)L;
  const int tid = threadIdx.x, l = tid & 63, w = __builtin_amdgcn_readfirstlane(tid >> 6), c = blockIdx.x;
  const int g = l >> 4, q4 = (l >> 2) & 3, p4 = l & 3, b8 = l & 7, d8 = (l >> 3) & 1;
  const int fsw = q4 | ((g & 1) << 2);
  const rsrc_t rsW = __builtin_amdgcn_make_buffer_rsrc((void*)ws, 0, WS_END, 0x00020000);
  const v4u sent4 = {SENT, SENT, SENT, SENT};
  STAMP_DECL();
  STAMP(0);

  if (c < 128) {
    const int j = c, xcd = j & 7, sj = j >> 3;
    const int cg = j >> 1, kh = j & 1;
    const int kvh = sj >> 2, et = xcd, nq = sj & 3;
    const int rb = j >> 4, cb = j & 15;
    u32 tagLo = 0, tagHi = 0;
    v2u xres = {0u, 0u};
    if (w < 4) {
      const rsrc_t rs = __builtin_amdgcn_make_buffer_rsrc((void*)wq, 0, 1024 * 4096 * 2, 0x00020000);
      const int r = l >> 3;
      const u32 v0 = r * 8192 + (((((l & 7) >> 1) ^ ((r >> 1) & 1)) << 1) | (l & 1)) * 16;
      const u32 v1 = v0 ^ 64;
      const u32 sb = (512 * kh + 128 * w) * 8192 + 128 * cg;
#pragma unroll
      for (int i = 0; i < 16; ++i) dma(rs, (i & 1) ? v1 : v0, sb + i * 8 * 8192, lbase + L_R0 + (16 * w + i) * 1024);
      const v2u e = __builtin_amdgcn_raw_buffer_load_b64(rsW, WS_EPOCH, 0, AUX_SC1);
      if (w == 0) {
        const rsrc_t rx = __builtin_amdgcn_make_buffer_rsrc((void*)x, 0, 8 * 1024 * 2, 0x00020000);
        xres = __builtin_amdgcn_raw_buffer_load_b64(rx, rb * 2048 + cb * 128 + (l & 15) * 8, 0, 0);
      }
      tagLo = e.x + 1u; tagHi = e.y + 1u;
      STAMP(1);
      VMWAIT(0);
      STAMP(2);
    } else {
      const rsrc_t rx = __builtin_amdgcn_make_buffer_rsrc((void*)x, 0, 8 * 1024 * 2, 0x00020000);
      const int r0 = 2 * (w - 4);
      v4u xa[2], xb[2];
#pragma unroll
      for (int i = 0; i < 2; ++i) {
        xa[i] = __builtin_amdgcn_raw_buffer_load_b128(rx, (r0 + i) * 2048 + l * 16, 0, 0);
        xb[i] = __builtin_amdgcn_raw_buffer_load_b128(rx, (r0 + i) * 2048 + 1024 + l * 16, 0, 0);
      }
      const v2u e = __builtin_amdgcn_raw_buffer_load_b64(rsW, WS_EPOCH, 0, AUX_SC1);
      tagLo = e.x + 1u; tagHi = e.y + 1u;
#pragma unroll
      for (int i = 0; i < 2; ++i) {
        v4u oa, ob;
        ln_row(xa[i], xb[i], oa, ob);
        *(LAS v4u*)(L + L_ACT + (r0 + i) * 1056 + l * 16) = kh ? ob : oa;
      }
      STAMP(2);
    }
    BARRIER();
    if (w < 4) {
      LAS char* pa = L + L_R0 + (8 * g + q4) * 128 + ((w ^ (((q4 >> 1) & 1) | ((g & 1) << 1))) << 5) + 8 * p4;
      LAS char* pb = L + L_ACT + b8 * 1056 + 16 * g;
      f32x4 acc = {0.f, 0.f, 0.f, 0.f};
#pragma unroll
      for (int ks = 0; ks < 16; ++ks) acc = mfma(trfrag(pa + ks * 4096, 512), *(LAS s16x8*)(pb + ks * 64), acc);
      const int col = 64 * cg + 16 * w + 4 * g + 2 * d8;
      v4u o;
      o.x = __float_as_uint(d8 ? acc[2] : acc[0]); o.y = tagLo; o.z = __float_as_uint(d8 ? acc[3] : acc[1]); o.w = tagHi;
      st16(rsW, WS_QG + (((b8 * 4 + (col >> 10)) * 2 + kh) * 1024 + (col & 1023)) * 8, o);
      STAMP(3);
    } else {
      nap<6>();
      const int u = w - 4;
#pragma unroll
      for (int i = 0; i < 2; ++i) st16(rsW, WS_WP + j * 8192 + (2 * u + i) * 1024 + l * 16, sent4);
#pragma unroll
      for (int i = 0; i < 4; ++i) if (4 * u + i < 14) st16(rsW, WS_MP + j * 16384 + (4 * u + i) * 1024 + l * 16, sent4);
      if (u == 3 && l < 8) st16(rsW, WS_H + rb * 2048 + cb * 128 + l * 16, sent4);
      VMWAIT(0);
      STAMP(3);
    }
    BARRIER();
    if (tid == 448) __builtin_amdgcn_raw_buffer_store_b64(v2u{tagLo, tagHi}, rsW, WS_FLAG + c * 8, 0, AUX_SC1);
    nap<B_WO_DELAY>();
    {
      const rsrc_t rs = __builtin_amdgcn_make_buffer_rsrc((void*)wo, 0, 4096 * 1024 * 2, 0x00020000);
      const u32 v0 = (l >> 5) * 2048 + (((((l & 31) >> 1) ^ (l >> 5)) << 1) | (l & 1)) * 16;
      const u32 sb = ((kvh * 8 + w) * 128 + 16 * et) * 2048 + 512 * nq;
#pragma unroll
      for (int i = 0; i < 8; ++i) dma(rs, v0 ^ (64 * (i & 1)) ^ (128 * (i >> 2)), sb + i * 4096, lbase + L_R1 + (8 * w + i) * 1024);
    }
    STAMP(4);
    if (w == 7) {
      POLL_LOOP {
        RELOAD();
        const v4u f0 = ld16(rsW, WS_FLAG + l * 16), f1 = ld16(rsW, WS_FLAG + 1024 + l * 16);
        const bool ok = (f0.x == tagLo) & (f0.y == tagHi) & (f0.z == tagLo) & (f0.w == tagHi) & (f1.x == tagLo) & (f1.y == tagHi) & (f1.z == tagLo) & (f1.w == tagHi);
        if (__all(ok)) break;
        nap<2>();
      }
    }
    VMWAIT(0);
    STAMP(5);
    BARRIER();
    s16x8 wf[2][4];
#pragma unroll
    for (int T = 0; T < 2; ++T) {
      LAS char* pa = L + L_R1 + (8 * g + q4) * 512 + (((2 * w + T) ^ fsw) << 5) + 8 * p4;
#pragma unroll
      for (int ks = 0; ks < 4; ++ks) wf[T][ks] = trfrag(pa + ks * 16384, 2048);
    }
    {
      const int hh = (l >> 2) & 7, sh = l >> 5;
      const u32 lo0 = WS_LSE + ((w * 4 + kvh) * 4 + sh) * 128 + hh * 4;
      u32 e0 = SENT, e1 = SENT;
      POLL_LOOP {
        RELOAD();
        e0 = ld4(rsW, lo0); e1 = ld4(rsW, lo0 + 256);
        if (__all((e0 != SENT) & (e1 != SENT))) break;
        nap<1>();
      }
      STAMP(6);
      const u32 ob = WS_OP + ((kvh * 8 + et) * 8 + w) * 2048 + l * 16;
      v4u d0 = sent4, d1 = sent4;
      POLL_LOOP {
        RELOAD();
        d0 = ld16(rsW, ob); d1 = ld16(rsW, ob + 1024);
        if (__all(nosent(d0) & nosent(d1))) break;
      }
      STAMP(7);
      const float l0 = __uint_as_float(e0), l1 = __uint_as_float(e1);
      float mx = fmaxf(l0, l1);
      { auto t = __builtin_amdgcn_permlane32_swap(__float_as_uint(mx), __float_as_uint(mx), false, false); mx = fmaxf(__uint_as_float(t[0]), __uint_as_float(t[1])); }
      const float w0 = __builtin_amdgcn_exp2f(l0 - mx), w1_ = __builtin_amdgcn_exp2f(l1 - mx);
      float tot = w0 + w1_;
      { auto t = __builtin_amdgcn_permlane32_swap(__float_as_uint(tot), __float_as_uint(tot), false, false); tot = __uint_as_float(t[0]) + __uint_as_float(t[1]); }
      const float it_ = __builtin_amdgcn_rcpf(tot);
      float o[4];
#pragma unroll
      for (int i = 0; i < 4; ++i) {
        const float v = w0 * __uint_as_float(d0[i]) + w1_ * __uint_as_float(d1[i]);
        auto t = __builtin_amdgcn_permlane32_swap(__float_as_uint(v), __float_as_uint(v), false, false);
        o[i] = (__uint_as_float(t[0]) + __uint_as_float(t[1])) * it_;
      }
      if (l < 32) *(LAS v2u*)(L + L_ACT + w * 288 + l * 8) = v2u{pk2(o[0], o[1]), pk2(o[2], o[3])};
    }
    BARRIER();
    {
      LAS char* pb = L + L_ACT + b8 * 288 + 16 * g;
      f32x4 a0 = {0.f, 0.f, 0.f, 0.f}, a1 = {0.f, 0.f, 0.f, 0.f};
#pragma unroll
      for (int ks = 0; ks < 4; ++ks) { const s16x8 bf = *(LAS s16x8*)(pb + ks * 64); a0 = mfma(wf[0][ks], bf, a0); a1 = mfma(wf[1][ks], bf, a1); }
      const int col = 256 * nq + 32 * w + 16 * d8 + 4 * g;
      v4u o;
#pragma unroll
      for (int i = 0; i < 4; ++i) o[i] = __float_as_uint(d8 ? a1[i] : a0[i]);
      st16(rsW, WS_WP + (b8 * 16 + (col >> 6)) * 8192 + (kvh * 8 + et) * 256 + (col & 63) * 4, o);
    }
    STAMP(8);
    if (w == 0) {
      nap<B_R_DELAY>();
      v4u d[8];
      POLL_LOOP {
        RELOAD();
        bool ok = true;
#pragma unroll
        for (int i = 0; i < 8; ++i) d[i] = ld16(rsW, WS_WP + j * 8192 + i * 1024 + l * 16);
#pragma unroll
        for (int i = 0; i < 8; ++i) ok &= nosent(d[i]);
        if (__all(ok)) break;
      }
      STAMP(9);
      float hv[4];
#pragma unroll
      for (int k = 0; k < 4; ++k) {
        float s = ((__uint_as_float(d[0][k]) + __uint_as_float(d[1][k])) + (__uint_as_float(d[2][k]) + __uint_as_float(d[3][k]))) +
                  ((__uint_as_float(d[4][k]) + __uint_as_float(d[5][k])) + (__uint_as_float(d[6][k]) + __uint_as_float(d[7][k])));
        s = sum16_32(s);
        const float xv = (k & 1) ? bfhi(xres[k >> 1]) : bflo(xres[k >> 1]);
        hv[k] = xv + rbf(s);
      }
      const v2u hp = {pk2(hv[0], hv[1]), pk2(hv[2], hv[3])};
      if (l < 16) {
        __builtin_amdgcn_raw_buffer_store_b64(hp, rsW, WS_H + rb * 2048 + cb * 128 + l * 8, 0, AUX_SC1);
        *(LAS f32x4*)(L + L_ST + 2048 + l * 16) = f32x4{bflo(hp.x), bfhi(hp.x), bflo(hp.y), bfhi(hp.y)};
      }
      STAMP(10);
    }
    BARRIER();
    if (w < 7) {
      nap<B_F_DELAY>();
      const u32 mb = WS_MP + j * 16384 + w * 2048 + l * 16;
      v4u d0 = sent4, d1 = sent4;
      POLL_LOOP {
        RELOAD();
        d0 = ld16(rsW, mb); d1 = ld16(rsW, mb + 1024);
        if (__all(nosent(d0) & nosent(d1))) break;
        nap<1>();
      }
      STAMP(11);
      float v[8];
#pragma unroll
      for (int i = 0; i < 4; ++i) { v[2 * i] = bflo(d0[i]) + bflo(d1[i]); v[2 * i + 1] = bfhi(d0[i]) + bfhi(d1[i]); }
#pragma unroll
      for (int i = 0; i < 4; ++i) { auto t = __builtin_amdgcn_permlane32_swap(__float_as_uint(v[i]), __float_as_uint(v[i + 4]), false, false); v[i] = __uint_as_float(t[0]) + __uint_as_float(t[1]); }
#pragma unroll
      for (int i = 0; i < 2; ++i) { auto t = __builtin_amdgcn_permlane16_swap(__float_as_uint(v[i]), __float_as_uint(v[i + 2]), false, false); v[i] = __uint_as_float(t[0]) + __uint_as_float(t[1]); }
      const float t0 = v[0] + dpp<DPP_R8>(v[0]), t1 = v[1] + dpp<DPP_R8>(v[1]);
      *(LAS float*)(L + L_ST + w * 256 + l * 4) = d8 ? t1 : t0;
    }
    BARRIER();
    if (w == 7) {
      float s = 0.f;
#pragma unroll
      for (int i = 0; i < 7; ++i) s += *(LAS float*)(L + L_ST + i * 256 + l * 4);
      const int e = l >> 3;
      const int col = 32 * (b8 >> 2) + 16 * (e >> 2) + 4 * (b8 & 3) + (e & 3);
      const float hv = *(LAS float*)(L + L_ST + 2048 + col * 4);
      const u32 r = pk2(hv + rbf(s), 0.f);
      const rsrc_t ro = __builtin_amdgcn_make_buffer_rsrc((void*)out, 0, 8 * 1024 * 2, 0x00020000);
      __builtin_amdgcn_raw_buffer_store_b16((unsigned short)r, ro, (rb * 1024 + cb * 64 + col) * 2, 0, 0);
      if (c == 0 && l == 0) __builtin_amdgcn_raw_buffer_store_b64(v2u{tagLo, tagHi}, rsW, WS_EPOCH, 0, AUX_SC1);
      STAMP(12);
    }
    STAMP_FLUSH();
    return;
  }

  const int a = c - 128, xcd = a & 7, sa = a >> 3;
  const int s = sa & 3, bk = (sa >> 2) * 8 + xcd, b = bk >> 2, kvh = bk & 3;
  const int m = ((sa >> 1) * 8 + xcd) * 2 + (sa & 1);
  u32 tagLo = 0, tagHi = 0;
  if (w < 4) {
    nap<A_START_DELAY>();
    const rsrc_t rk = __builtin_amdgcn_make_buffer_rsrc((void*)kc, 0, 8 * 4 * 4096 * 128 * 2, 0x00020000);
    const rsrc_t rv = __builtin_amdgcn_make_buffer_rsrc((void*)vc, 0, 8 * 4 * 4096 * 128 * 2, 0x00020000);
    const u32 sb = (bk * 4096 + 3072 + 256 * s + 64 * w) * 256;
    const u32 vk = g * 256 + ((l & 15) ^ g) * 16;
#pragma unroll
    for (int i = 0; i < 16; ++i) dma(rk, vk ^ (64 * (i & 3)), sb + i * 1024, lbase + L_R0 + (16 * w + i) * 1024);
    const u32 vv = g * 256 + (((((l & 15) >> 1) ^ g) << 1) | (l & 1)) * 16;
#pragma unroll
    for (int i = 0; i < 16; ++i) dma(rv, vv ^ (128 * ((i >> 1) & 1)), sb + i * 1024, lbase + L_R1 + (16 * w + i) * 1024);
    STAMP(1);
    VMWAIT(16);
    STAMP(2);
  } else {
    const int u = w - 4;
    const v2u e = __builtin_amdgcn_raw_buffer_load_b64(rsW, WS_EPOCH, 0, AUX_SC1);
    if (u == 0) {
#pragma unroll
      for (int i = 0; i < 4; ++i) st16(rsW, WS_OP + (((kvh * 8 + 2 * i + (l >> 5)) * 8 + b) * 4 + s) * 512 + (l & 31) * 16, sent4);
      if (l < 2) st16(rsW, WS_LSE + (bk * 4 + s) * 128 + l * 16, sent4);
    }
    tagLo = e.x + 1u; tagHi = e.y + 1u;
    if (u == 0) {
      VMWAIT(0);
      if (l == 0) __builtin_amdgcn_raw_buffer_store_b64(v2u{tagLo, tagHi}, rsW, WS_FLAG + c * 8, 0, AUX_SC1);
    }
    STAMP(1);
    nap<A_Q_DELAY>();
    const u32 qb = WS_QG + bk * 16384 + u * 2048 + l * 16;
    v4u d[4];
    POLL_LOOP {
        RELOAD();
      bool ok = true;
#pragma unroll
      for (int i = 0; i < 4; ++i) d[i] = ld16(rsW, qb + (i >> 1) * 8192 + (i & 1) * 1024);
#pragma unroll
      for (int i = 0; i < 4; ++i) ok &= (d[i].y == tagLo) & (d[i].w == tagHi);
      if (__all(ok)) break;
      nap<1>();
    }
#pragma unroll
    for (int i = 0; i < 2; ++i)
      *(LAS u32*)(L + L_ACT + (2 * u + i) * 288 + l * 4) = pk2(__uint_as_float(d[i].x) + __uint_as_float(d[i + 2].x), __uint_as_float(d[i].z) + __uint_as_float(d[i + 2].z));
    STAMP(2);
  }
  BARRIER();
  STAMP(3);
  float sc[4], mrow;
  {
    LAS char* pb = L + L_ACT + b8 * 288 + 16 * g;
    LAS char* pk = L + L_R0 + (32 * w + (l & 15)) * 256;
    f32x4 a0 = {0.f, 0.f, 0.f, 0.f}, a1 = {0.f, 0.f, 0.f, 0.f};
#pragma unroll
    for (int ks = 0; ks < 4; ++ks) {
      const s16x8 bf = *(LAS s16x8*)(pb + ks * 64);
      const int pos = ((4 * ks + g) ^ (l & 15)) * 16;
      a0 = mfma(*(LAS s16x8*)(pk + pos), bf, a0);
      a1 = mfma(*(LAS s16x8*)(pk + 4096 + pos), bf, a1);
    }
    float mx = -INFINITY;
#pragma unroll
    for (int r = 0; r < 4; ++r) { sc[r] = rbf(rbf(d8 ? a1[r] : a0[r]) * 0.08838834764831845f); mx = fmaxf(mx, sc[r]); }
    mx = fmaxf(mx, dpp<DPP_R8>(mx));
    mx = max16_32(mx);
    if (l < 8) *(LAS float*)(L + L_ST + l * 32 + w * 4) = mx;
  }
  BARRIER();
  {
    const f32x4 m0 = *(LAS f32x4*)(L + L_ST + b8 * 32), m1 = *(LAS f32x4*)(L + L_ST + b8 * 32 + 16);
    mrow = fmaxf(fmaxf(fmaxf(m0[0], m0[1]), fmaxf(m0[2], m0[3])), fmaxf(fmaxf(m1[0], m1[1]), fmaxf(m1[2], m1[3])));
    float pr[4], sm = 0.f;
#pragma unroll
    for (int r = 0; r < 4; ++r) { pr[r] = __builtin_amdgcn_exp2f((sc[r] - mrow) * 1.4426950408889634f); sm += pr[r]; }
    *(LAS v2u*)(L + L_P + b8 * 544 + (32 * w + 16 * d8 + 4 * g) * 2) = v2u{pk2(pr[0], pr[1]), pk2(pr[2], pr[3])};
    sm += dpp<DPP_R8>(sm);
    sm = sum16_32(sm);
    if (l < 8) *(LAS float*)(L + L_ST + 256 + l * 32 + w * 4) = sm;
  }
  BARRIER();
  s16x8 pf[8];
  float invl;
  {
    const f32x4 s0 = *(LAS f32x4*)(L + L_ST + 256 + b8 * 32), s1 = *(LAS f32x4*)(L + L_ST + 256 + b8 * 32 + 16);
    const float tot = ((s0[0] + s0[1]) + (s0[2] + s0[3])) + ((s1[0] + s1[1]) + (s1[2] + s1[3]));
    if (w == 4 && l < 8)
      __builtin_amdgcn_raw_buffer_store_b32(__float_as_uint(mrow * 1.4426950408889634f + __builtin_amdgcn_logf(tot)), rsW, WS_LSE + (bk * 4 + s) * 128 + l * 4, 0, AUX_SC1);
    invl = __builtin_amdgcn_rcpf(tot);
#pragma unroll
    for (int ks = 0; ks < 8; ++ks) pf[ks] = *(LAS s16x8*)(L + L_P + b8 * 544 + ks * 64 + 16 * g);
  }
  STAMP(4);
  if (w < 4) VMWAIT(0);
  STAMP(5);
  BARRIER();
  {
    LAS char* pa = L + L_R1 + (8 * g + q4) * 256 + ((w ^ fsw) << 5) + 8 * p4;
    f32x4 acc = {0.f, 0.f, 0.f, 0.f};
#pragma unroll
    for (int ks = 0; ks < 8; ++ks) acc = mfma(trfrag(pa + ks * 8192, 1024), pf[ks], acc);
    v4u o;
#pragma unroll
    for (int i = 0; i < 4; ++i) o[i] = __float_as_uint(acc[i] * invl);
    if (!d8) st16(rsW, WS_OP + (((kvh * 8 + w) * 8 + b) * 4 + s) * 512 + b8 * 64 + g * 16, o);
  }
  STAMP(6);
  if (sa >= 14) { STAMP_FLUSH(); return; }
  BARRIER();
  {
    const rsrc_t r1 = __builtin_amdgcn_make_buffer_rsrc((void*)w1, 0, 1024 * 3584 * 2, 0x00020000);
    const u32 v1 = (l >> 2) * 7168 + ((((l >> 1) & 1) ^ (l >> 5)) << 5) + (l & 1) * 16;
    const u32 sb1 = 128 * w * 7168 + 64 * m;
#pragma unroll
    for (int i = 0; i < 8; ++i) dma(r1, v1, sb1 + i * 16 * 7168, lbase + L_R0 + (8 * w + i) * 1024);
    const rsrc_t r2 = __builtin_amdgcn_make_buffer_rsrc((void*)w2, 0, 3584 * 1024 * 2, 0x00020000);
    const u32 v2 = ((((l >> 1) ^ (((w >> 1) & 1) << 2)) << 1) | (l & 1)) * 16;
    const u32 sb2 = (32 * m + 4 * w) * 2048;
#pragma unroll
    for (int i = 0; i < 8; ++i) dma(r2, v2 ^ (32 * (i >> 1)), sb2 + i * 1024, lbase + L_R1 + (8 * w + i) * 1024);
  }
  STAMP(7);
  if (w == 7) {
    POLL_LOOP {
        RELOAD();
      const v4u f0 = ld16(rsW, WS_FLAG + l * 16);
      if (__all((f0.x == tagLo) & (f0.y == tagHi) & (f0.z == tagLo) & (f0.w == tagHi))) break;
      nap<2>();
    }
  }
  VMWAIT(0);
  STAMP(8);
  BARRIER();
  const int nt = w & 1, kq = w >> 1;
  s16x8 f1[8], f2[8];
  {
    LAS char* pa = L + L_R0 + (256 * kq + 8 * g + q4) * 64 + ((nt ^ (g & 1)) << 5) + 8 * p4;
#pragma unroll
    for (int ks = 0; ks < 8; ++ks) f1[ks] = trfrag(pa + ks * 2048, 256);
#pragma unroll
    for (int T = 0; T < 8; ++T) f2[T] = trfrag(L + L_R1 + (8 * g + q4) * 2048 + (((8 * w + T) ^ fsw) << 5) + 8 * p4, 8192);
  }
  {
    const u32 hb = WS_H + w * 2048 + l * 16;
    v4u ha = sent4, hc = sent4;
    POLL_LOOP {
        RELOAD();
      ha = ld16(rsW, hb); hc = ld16(rsW, hb + 1024);
      if (__all(nosent(ha) & nosent(hc))) break;
      nap<1>();
    }
    STAMP(9);
    v4u oa, ob;
    ln_row(ha, hc, oa, ob);
    *(LAS v4u*)(L + L_ACT + w * 2080 + l * 16) = oa;
    *(LAS v4u*)(L + L_ACT + w * 2080 + 1024 + l * 16) = ob;
  }
  BARRIER();
  {
    LAS char* pb = L + L_ACT + b8 * 2080 + kq * 512 + 16 * g;
    f32x4 acc = {0.f, 0.f, 0.f, 0.f};
#pragma unroll
    for (int ks = 0; ks < 8; ++ks) acc = mfma(f1[ks], *(LAS s16x8*)(pb + ks * 64), acc);
    if (!d8) *(LAS f32x4*)(L + L_P + (kq * 8 + b8) * 144 + (16 * nt + 4 * g) * 4) = acc;
  }
  BARRIER();
  {
    LAS char* pg = L + L_P + (2 * d8 * 8 + b8) * 144 + g * 32;
    const f32x4 x0 = *(LAS f32x4*)(pg) + *(LAS f32x4*)(pg + 8 * 144), x1 = *(LAS f32x4*)(pg + 16) + *(LAS f32x4*)(pg + 8 * 144 + 16);
    float gv[8];
#pragma unroll
    for (int i = 0; i < 4; ++i) { gv[i] = fmaxf(x0[i] + dpp<DPP_R8>(x0[i]), 0.f); gv[4 + i] = fmaxf(x1[i] + dpp<DPP_R8>(x1[i]), 0.f); }
    const v4u gp = {pk2(gv[0], gv[1]), pk2(gv[2], gv[3]), pk2(gv[4], gv[5]), pk2(gv[6], gv[7])};
    const s16x8 gf = __builtin_bit_cast(s16x8, gp);
    f32x4 o[8];
#pragma unroll
    for (int T = 0; T < 8; ++T) o[T] = mfma(f2[T], gf, f32x4{0.f, 0.f, 0.f, 0.f});
#pragma unroll
    for (int i = 0; i < 2; ++i) {
      f32x4 u0, u1;
#pragma unroll
      for (int r = 0; r < 4; ++r) { u0[r] = d8 ? o[4 * i + 2][r] : o[4 * i][r]; u1[r] = d8 ? o[4 * i + 3][r] : o[4 * i + 1][r]; }
      const v4u pv = {pk2(u0[0], u0[1]), pk2(u0[2], u0[3]), pk2(u1[0], u1[1]), pk2(u1[2], u1[3])};
      st16(rsW, WS_MP + (b8 * 16 + 2 * w + i) * 16384 + m * 128 + (d8 * 4 + g) * 16, pv);
    }
  }
  STAMP(10);
  STAMP_FLUSH();
}

extern "C" void kernel_launch(void* const* d_in, const int* in_sizes, int n_in, void* d_out, int out_size, void* d_ws, size_t ws_size,
                              hipStream_t stream) {
  (void)hipFuncSetAttribute((const void*)fused_decode_layer, hipFuncAttributeMaxDynamicSharedMemorySize, L_BYTES);
  fused_decode_layer<<<NWG, NTHR, L_BYTES, stream>>>((const char*)d_in[0], (const char*)d_in[1], (const char*)d_in[2], (const char*)d_in[3],
                                                     (const char*)d_in[4], (const char*)d_in[6], (const char*)d_in[7], (char*)d_out, (char*)d_ws);
}
```

```cpp
#include <hip/hip_runtime.h>
#include <hip/hip_bf16.h>
#include <stdint.h>

#define DIAG 0
#define A_START_DELAY 4
#ifndef M_DELAY
#define M_DELAY 14
#endif
#define O_DELAY 12
#define R_DELAY 8
#define F_DELAY 60
#define O_NAP 0
#define Q_NAP 1
#define F_NAP 0
#define H_NAP 0
#define H_DELAY 120
#define NTOUCH_V 8
#define TOUCH_DELAY 10
#define C3FIX 1
#define WQ_TOUCH 1
#define CODE_LINES 15
#define CODE_STRIDE 64
#define V_OWN 0
#define W1C3 0
#define C3NT 0
#define QH_DELAY 2
#define WO_PRE 4
#define V_GATE 0
#define TOUCH_EARLY 0
#define W2_DELAY 0
#define WO_PRE0 2
#define ALLSYNC 0
#define MPC_N 14
#define MPC_DELAY 0
#define LNAP 0
#define WS_TAIL 0
#define WS_SHIFT(n) (WS_TAIL ? (((n) - (size_t)(WS_TAIL * (DIAG ? 9 : 4) << 20)) & ~(size_t)4095) : (size_t)0)
#define MP_FIRST 0
#define C3HEAD 1
#define MORD 2
#define HOPS 1

#define LAS __attribute__((address_space(3)))
typedef unsigned u32;
typedef unsigned long long u64;
typedef unsigned v4u __attribute__((ext_vector_type(4)));
typedef unsigned v2u __attribute__((ext_vector_type(2)));
typedef float f32x4 __attribute__((ext_vector_type(4)));
typedef float f32x2 __attribute__((ext_vector_type(2)));
typedef short s16x2 __attribute__((ext_vector_type(2)));
typedef short s16x4 __attribute__((ext_vector_type(4)));
typedef short s16x8 __attribute__((ext_vector_type(8)));
typedef __bf16 bf16x2 __attribute__((ext_vector_type(2)));
typedef __bf16 bf16x8 __attribute__((ext_vector_type(8)));
typedef __amdgpu_buffer_rsrc_t rsrc_t;

namespace {

constexpr int NWG = 256, NTHR = 512;
constexpr u32 SENT = 0xFFFFFFFFu;
constexpr int AUX_SC1 = 16;
constexpr int SPIN_CAP = 20000;
constexpr float LOG2E = 1.4426950408889634f;

constexpr int WS_EPOCH = 0;
constexpr int WS_LSE = 4096;
constexpr int WS_FLAG = 49152;
constexpr int WS_H = 32768;
constexpr int WS_QH = 0xA0000;
constexpr int WS_QG = 65536;
constexpr int WS_OP = 1 << 20;
constexpr int WS_WP = WS_OP + 524288;
constexpr int WS_MP = WS_WP + 524288;
constexpr int MPJ = 112 * 128;
constexpr int WS_END = WS_MP + 128 * MPJ;
constexpr int WS_STAMP = 8 << 20;

constexpr int L_R0 = 0, L_R1 = 65536, L_MISC = 131072;
constexpr int L_ACT = L_MISC;
constexpr int L_P = L_ACT + 8 * 1056;
constexpr int L_CS = L_P + 4736;
constexpr int L_ST = L_CS + 640;
constexpr int L_FL = L_ST + 512;
constexpr int F_KR = 0, F_KD = 16, F_Q = 32, F_P = 48, F_V = 64, F_O = 80;
constexpr int L_BYTES = L_ST + 2048;

__device__ __forceinline__ float bflo(u32 v) { return __uint_as_float(v << 16); }
__device__ __forceinline__ float bfhi(u32 v) { return __uint_as_float(v & 0xFFFF0000u); }
__device__ __forceinline__ u32 pk2(float lo, float hi) {
  bf16x2 r = {(__bf16)lo, (__bf16)hi};
  return __builtin_bit_cast(u32, r);
}
__device__ __forceinline__ float rbf(float x) { return (float)(__bf16)x; }

template <int CTRL> __device__ __forceinline__ float dpp(float x) {
  return __builtin_bit_cast(float, __builtin_amdgcn_mov_dpp(__builtin_bit_cast(int, x), CTRL, 0xf, 0xf, true));
}
constexpr int DPP_X1 = 0xB1, DPP_X2 = 0x4E, DPP_HM = 0x141, DPP_M = 0x140, DPP_R8 = 0x128, DPP_ID = 0xE4;
__device__ __forceinline__ float sum16_32(float x) {
  auto s = __builtin_amdgcn_permlane16_swap(__float_as_uint(x), __float_as_uint(x), false, false);
  x = __uint_as_float(s[0]) + __uint_as_float(s[1]);
  auto t = __builtin_amdgcn_permlane32_swap(__float_as_uint(x), __float_as_uint(x), false, false);
  return __uint_as_float(t[0]) + __uint_as_float(t[1]);
}
__device__ __forceinline__ float max16_32(float x) {
  auto s = __builtin_amdgcn_permlane16_swap(__float_as_uint(x), __float_as_uint(x), false, false);
  x = fmaxf(__uint_as_float(s[0]), __uint_as_float(s[1]));
  auto t = __builtin_amdgcn_permlane32_swap(__float_as_uint(x), __float_as_uint(x), false, false);
  return fmaxf(__uint_as_float(t[0]), __uint_as_float(t[1]));
}
__device__ __forceinline__ float wave_sum(float x) {
  x += dpp<DPP_X1>(x); x += dpp<DPP_X2>(x); x += dpp<DPP_HM>(x); x += dpp<DPP_M>(x);
  return sum16_32(x);
}
__device__ __forceinline__ f32x4 mfma(s16x8 a, s16x8 b, f32x4 c) {
  return __builtin_amdgcn_mfma_f32_16x16x32_bf16(__builtin_bit_cast(bf16x8, a), __builtin_bit_cast(bf16x8, b), c, 0, 0, 0);
}
__device__ __forceinline__ f32x4 mfma(v4u a, v4u b, f32x4 c) {
  return __builtin_amdgcn_mfma_f32_16x16x32_bf16(__builtin_bit_cast(bf16x8, a), __builtin_bit_cast(bf16x8, b), c, 0, 0, 0);
}
__device__ __forceinline__ s16x8 trfrag(LAS char* p, int half_off) {
  typedef short v4i16_t __attribute__((ext_vector_type(4)));
  s16x4 lo = __builtin_amdgcn_ds_read_tr16_b64_v4i16((LAS v4i16_t*)p);
  s16x4 hi = __builtin_amdgcn_ds_read_tr16_b64_v4i16((LAS v4i16_t*)(p + half_off));
  return __builtin_shufflevector(lo, hi, 0, 1, 2, 3, 4, 5, 6, 7);
}
#define DMA4_ASM(POL) \
  asm volatile("s_mov_b32 m0, %6\n\ts_nop 0\n\t" \
               "buffer_load_dwordx4 %1, %5, %0 offen " POL " lds\n\ts_add_u32 %0, %0, %7\n\t" \
               "buffer_load_dwordx4 %2, %5, %0 offen offset:1024 " POL " lds\n\ts_add_u32 %0, %0, %7\n\t" \
               "buffer_load_dwordx4 %3, %5, %0 offen offset:2048 " POL " lds\n\ts_add_u32 %0, %0, %7\n\t" \
               "buffer_load_dwordx4 %4, %5, %0 offen offset:3072 " POL " lds" \
               : "+s"(soff) : "v"(v0), "v"(v1), "v"(v2), "v"(v3), "s"(rs), "s"(lds), "s"(sdelta) : "memory", "scc")
template <int NT = 1>
__device__ __forceinline__ u32 dma4(rsrc_t rs, u32 v0, u32 v1, u32 v2, u32 v3, u32 soff, u32 sdelta, u32 lds) {
  if constexpr (NT) DMA4_ASM("nt"); else DMA4_ASM("");
  return soff;
}
__device__ __forceinline__ void dma4c(rsrc_t rs, u32 v0, u32 v1, u32 v2, u32 v3, u32 soff, u32 lds) {
  asm volatile("s_mov_b32 m0, %6\n\ts_nop 0\n\t"
               "buffer_load_dwordx4 %1, %5, %0 offen nt lds\n\t"
               "buffer_load_dwordx4 %2, %5, %0 offen offset:1024 nt lds\n\t"
               "buffer_load_dwordx4 %3, %5, %0 offen offset:2048 nt lds\n\t"
               "buffer_load_dwordx4 %4, %5, %0 offen offset:3072 nt lds"
               :: "s"(soff), "v"(v0), "v"(v1), "v"(v2), "v"(v3), "s"(rs), "s"(lds) : "memory");
}
template <int NT = 1>
__device__ __forceinline__ void touch(rsrc_t rs, u32 voff, u32 soff, u32 lds) {
  if constexpr (NT) asm volatile("s_mov_b32 m0, %0\n\ts_nop 0\n\tbuffer_load_dword %1, %2, %3 offen nt lds" ::"s"(lds), "v"(voff), "s"(rs), "s"(soff) : "memory");
  else asm volatile("s_mov_b32 m0, %0\n\ts_nop 0\n\tbuffer_load_dword %1, %2, %3 offen lds" ::"s"(lds), "v"(voff), "s"(rs), "s"(soff) : "memory");
}
#define LOAD_TAG() do { u64 e_; asm volatile("s_load_dwordx2 %0, %1, 0x0\n\ts_waitcnt lgkmcnt(0)" : "=s"(e_) : "s"(ws) : "memory"); tagLo = (u32)e_ + 1u; tagHi = (u32)(e_ >> 32) + 1u; } while (0)
#define PAD(n, nx) asm volatile("Lpad" #n ":\n\ts_cbranch_execz Lpad" #nx)
#define HOP(first, ret) asm volatile("s_mov_b64 exec, 0\n\ts_branch Lpad" #first "\nLpad" #ret ":\n\ts_mov_b64 exec, -1")
#define LSET(off) do { asm volatile("s_waitcnt lgkmcnt(0)" ::: "memory"); *(volatile LAS u32*)(L + L_FL + (off)) = 1u; } while (0)
#define LWAIT1(off) do { _Pragma("nounroll") for (int it_ = 0; it_ < 200000; ++it_) { \
    if (__builtin_amdgcn_readfirstlane(*(volatile LAS u32*)(L + L_FL + (off))) != 0u) break; nap<LNAP>(); } asm volatile("" ::: "memory"); } while (0)
#define LWAIT4(off) do { _Pragma("nounroll") for (int it_ = 0; it_ < 200000; ++it_) { const v4u f_ = *(volatile LAS v4u*)(L + L_FL + (off)); \
    if (__builtin_amdgcn_readfirstlane(f_.x & f_.y & f_.z & f_.w) != 0u) break; nap<LNAP>(); } asm volatile("" ::: "memory"); } while (0)
#define LWAIT8(off) do { _Pragma("nounroll") for (int it_ = 0; it_ < 200000; ++it_) { \
    const v4u f_ = *(volatile LAS v4u*)(L + L_FL + (off)), g_ = *(volatile LAS v4u*)(L + L_FL + (off) + 16); \
    if (__builtin_amdgcn_readfirstlane((f_.x & f_.y & f_.z & f_.w) & (g_.x & g_.y & g_.z & g_.w)) != 0u) break; nap<LNAP>(); } asm volatile("" ::: "memory"); } while (0)
__device__ __forceinline__ void hint_wait(const char* p, u32 tag, u32 ticks) {
  u64 v = 0;
#pragma nounroll
  for (int it = 0; it < 64; ++it) {
    asm volatile("s_load_dwordx2 %0, %1, 0x0 glc\n\ts_waitcnt lgkmcnt(0)" : "=s"(v) : "s"(p) : "memory");
    if ((u32)v == tag) break;
  }
#pragma nounroll
  for (int it = 0; it < 64; ++it) { if ((u32)__builtin_amdgcn_s_memrealtime() - (u32)(v >> 32) >= ticks) break; }
}
typedef unsigned s8u __attribute__((ext_vector_type(8)));
__device__ __forceinline__ void hint_wait8(const char* p, u32 tag) {
#pragma nounroll
  for (int it = 0; it < 64; ++it) {
    s8u v;
    asm volatile("s_load_dwordx8 %0, %1, 0x0 glc\n\ts_waitcnt lgkmcnt(0)" : "=s"(v) : "s"(p) : "memory");
    if ((((v[0] ^ tag) | (v[1] ^ tag)) | ((v[2] ^ tag) | (v[3] ^ tag)) | ((v[4] ^ tag) | (v[5] ^ tag)) | ((v[6] ^ tag) | (v[7] ^ tag))) == 0u) break;
  }
}
__device__ __forceinline__ u32 max4(v4u v) { return max(max(v.x, v.y), max(v.z, v.w)); }
#define POLL_LOOP _Pragma("nounroll") for (int it = 0; it < SPIN_CAP; ++it)
#define RELOAD() asm volatile("" ::: "memory")
#define BARRIER() asm volatile("s_waitcnt lgkmcnt(0)\n\ts_barrier" ::: "memory")
#define VMWAIT(n) asm volatile("s_waitcnt vmcnt(" #n ")" ::: "memory")
#define FENCE() __builtin_amdgcn_sched_barrier(0)
#define PIN(x) asm volatile("" : "+v"(x))
template <int N> __device__ __forceinline__ void nap() {
  if constexpr (N > 0) { __builtin_amdgcn_s_sleep(N > 127 ? 127 : N); nap<(N > 127 ? N - 127 : 0)>(); }
}
__device__ __forceinline__ v4u ld16(rsrc_t rs, u32 off) { return __builtin_amdgcn_raw_buffer_load_b128(rs, off, 0, AUX_SC1); }
__device__ __forceinline__ u32 ld4(rsrc_t rs, u32 off) { return __builtin_amdgcn_raw_buffer_load_b32(rs, off, 0, AUX_SC1); }
__device__ __forceinline__ void st16(rsrc_t rs, u32 off, v4u v) { __builtin_amdgcn_raw_buffer_store_b128(v, rs, off, 0, AUX_SC1); }
__device__ __forceinline__ bool wave_clean(float v) { return __builtin_amdgcn_ballot_w64(v != v) == 0ull; }
__device__ __forceinline__ rsrc_t mkrs(const void* p, int bytes) { return __builtin_amdgcn_make_buffer_rsrc((void*)p, 0, bytes, 0x00020000); }

__device__ __forceinline__ float dot2(u32 a, u32 b, float c) {
  return __builtin_amdgcn_fdot2_f32_bf16(__builtin_bit_cast(bf16x2, a), __builtin_bit_cast(bf16x2, b), c, false);
}
__device__ __forceinline__ v4u ln_half(const v4u c, const v4u o) {
  float s = 0.f, q = 0.f;
#pragma unroll
  for (int i = 0; i < 4; ++i) { s = dot2(c[i], 0x3F803F80u, s); q = dot2(c[i], c[i], q); s = dot2(o[i], 0x3F803F80u, s); q = dot2(o[i], o[i], q); }
  const float mean = wave_sum(s) * (1.f / 1024.f);
  const float var = fmaxf(fmaf(-mean, mean, wave_sum(q) * (1.f / 1024.f)), 0.f);
  const u32 mv = pk2(mean, var);
  const float mb = bflo(mv);
  const float den = rbf(__builtin_amdgcn_sqrtf(rbf(bfhi(mv) + 1e-5f)));
  const float inv = __builtin_amdgcn_rcpf(den);
  v4u r;
#pragma unroll
  for (int i = 0; i < 4; ++i) {
    const u32 d = pk2(bflo(c[i]) - mb, bfhi(c[i]) - mb);
    const float d0 = bflo(d), d1 = bfhi(d);
    float q0 = d0 * inv, q1 = d1 * inv;
    q0 = fmaf(fmaf(-q0, den, d0), inv, q0);
    q1 = fmaf(fmaf(-q1, den, d1), inv, q1);
    r[i] = pk2(q0, q1);
  }
  return r;
}

#define STAMP(i) do {} while (0)
#define STAMP_DECL() do {} while (0)
#define STAMP_FLUSH() do {} while (0)

}

__global__ void __launch_bounds__(NTHR, 2)
fused_decode_layer(const char* x, const char* kc, const char* vc, const char* wq, const char* wo, const char* w1, const char* w2, char* out, char* ws) {
  extern __shared__ __attribute__((aligned(16))) char smem[];
  STAMP_DECL();
  STAMP(0);
  {
    u32 t0, t1, t2, t3, t4;
    asm volatile(
        "s_load_dwordx8 s[64:71], %[ka], 0x0\n\t"
        "s_load_dwordx2 s[80:81], %[ka], 0x40\n\t"
        "s_getpc_b64 s[82:83]\n\t"
        "v_readfirstlane_b32 s72, %[tid]\n\t"
        "v_and_b32 %[t4], 63, %[tid]\n\t"
        "s_mov_b32 s79, 0x20000\n\t"
        "s_mov_b32 s78, 0x800000\n\t"
        "s_lshr_b32 s72, s72, 6\n\t"
        "s_and_b32 s73, s72, 3\n\t"
        "v_lshrrev_b32 %[t1], 4, %[t4]\n\t"
        "s_bfe_u32 s84, %[c], 0x20003\n\t"
        "s_bfe_u32 s74, %[c], 0x10006\n\t"
        "s_lshl1_add_u32 s84, s84, s74\n\t"
        "s_bfe_u32 s85, %[c], 0x10005\n\t"
        "s_lshl2_add_u32 s85, s85, s73\n\t"
        "s_and_b32 s74, %[c], 0x80\n\t"
        "s_cmp_gt_u32 s72, 3\n\t"
        "s_cbranch_scc1 Lpro_hi\n\t"
        "s_cmp_eq_u32 s74, 0\n\t"
        "s_cbranch_scc1 Lpro_bl\n\t"
        "s_cmp_lg_u32 s72, 3\n\t"
        "s_cbranch_scc1 Lpro_al0\n\t"
        "s_bfe_u32 s74, %[c], 0x40003\n\t"
        "s_mul_i32 s74, s74, %[cl]*%[cs]\n\t"
        "s_and_b32 s82, s82, 0xffffff00\n\t"
        "s_add_u32 s82, s82, s74\n\t"
        "s_addc_u32 s83, s83, 0\n\t"
        ".irp o,0,1,2,3,4,5,6,7,8,9,10,11,12,13,14,15\n\t"
        ".if \\o < %[cl]\n\t"
        "s_load_dword s75, s[82:83], \\o*%[cs]\n\t"
        ".endif\n\t"
        ".endr\n\t"
        "s_branch Lpro_end\n\t"
        "Lpro_al0:\n\t"
        "v_mov_b32 v4, -1\n\t"
        "v_mov_b32 v5, -1\n\t"
        "v_mov_b32 v6, -1\n\t"
        "v_mov_b32 v7, -1\n\t"
        "v_lshlrev_b32 %[t2], 4, %[t4]\n\t"
        "s_mul_i32 s82, %[c], %[mpj]\n\t"
        "s_add_u32 s82, s82, %[mpb]\n\t"
        "s_mul_i32 s75, s73, 5120\n\t"
        "s_add_u32 s75, s75, s82\n\t"
        "s_add_u32 s74, s75, 4096\n\t"
        "s_add_u32 s82, s82, %[mpj]\n\t"
        "s_mov_b32 s83, s79\n\t"
        "s_waitcnt lgkmcnt(0)\n\t"
        "s_and_b32 s81, s81, 0xffff\n\t"
        ".if %[mpf]\n\t"
        "buffer_store_dwordx4 v[4:7], %[t2], s[80:83], s75 offen sc1\n\t"
        "buffer_store_dwordx4 v[4:7], %[t2], s[80:83], s75 offen offset:1024 sc1\n\t"
        "buffer_store_dwordx4 v[4:7], %[t2], s[80:83], s75 offen offset:2048 sc1\n\t"
        "buffer_store_dwordx4 v[4:7], %[t2], s[80:83], s75 offen offset:3072 sc1\n\t"
        "buffer_store_dwordx4 v[4:7], %[t2], s[80:83], s74 offen sc1\n\t"
        ".else\n\t"
        "s_mov_b32 s86, s75\n\t"
        "s_mov_b32 s87, s74\n\t"
        ".endif\n\t"
        "Lpro_al:\n\t"
        "s_lshl1_add_u32 s74, s73, s84\n\t"
        "s_and_b32 s74, s74, 7\n\t"
        "s_add_u32 s75, s74, %[c]\n\t"
        "s_and_b32 s75, s75, 7\n\t"
        "s_lshl3_add_u32 s75, s74, s75\n\t"
        "s_lshl_b32 s75, s75, 7\n\t"
        "s_lshl1_add_u32 s74, s85, 1\n\t"
        "s_lshl_b32 s74, s74, 19\n\t"
        "s_or_b32 s74, s74, s75\n\t"
        "v_lshlrev_b32 %[t0], 13, %[t4]\n\t"
        "s_mov_b32 m0, %[junk]\n\t"
        "s_mov_b32 s78, %[wqb]\n\t"
        "s_waitcnt lgkmcnt(0)\n\t"
        "s_mov_b32 s76, s70\n\t"
        "s_and_b32 s77, s71, 0xffff\n\t"
        "buffer_load_dword %[t0], s[76:79], s74 offen nt lds\n\t"
        "s_add_u32 s85, s85, 1\n\t"
        "s_add_u32 s73, s73, 1\n\t"
        "s_cmp_eq_u32 s73, 3\n\t"
        "s_cbranch_scc1 Lpro_al\n\t"
        ".if !%[mpf]\n\t"
        "buffer_store_dwordx4 v[4:7], %[t2], s[80:83], s86 offen sc1\n\t"
        "buffer_store_dwordx4 v[4:7], %[t2], s[80:83], s86 offen offset:1024 sc1\n\t"
        "buffer_store_dwordx4 v[4:7], %[t2], s[80:83], s86 offen offset:2048 sc1\n\t"
        "buffer_store_dwordx4 v[4:7], %[t2], s[80:83], s86 offen offset:3072 sc1\n\t"
        "buffer_store_dwordx4 v[4:7], %[t2], s[80:83], s87 offen sc1\n\t"
        ".endif\n\t"
        "s_branch Lpro_end\n\t"
        "Lpro_hi:\n\t"
        "s_lshl_b32 m0, s73, 14\n\t"
        "s_cmp_lg_u32 s74, 0\n\t"
        "s_cbranch_scc1 Lpro_ah\n\t"
        "v_and_b32 %[t1], 1, %[t1]\n\t"
        "v_lshlrev_b32 %[t1], 1, %[t1]\n\t"
        "v_and_b32 %[t0], 7, %[t4]\n\t"
        "v_xor_b32 %[t0], %[t0], %[t1]\n\t"
        "v_lshrrev_b32 %[t2], 3, %[t4]\n\t"
        "v_lshlrev_b32 %[t2], 13, %[t2]\n\t"
        "v_lshl_or_b32 %[t0], %[t0], 4, %[t2]\n\t"
        "v_xor_b32 %[t1], 64, %[t0]\n\t"
        "s_add_u32 s75, s84, %[c]\n\t"
        "s_and_b32 s75, s75, 7\n\t"
        "s_lshl3_add_u32 s75, s84, s75\n\t"
        "s_lshl_b32 s75, s75, 7\n\t"
        "s_lshl_b32 s74, s85, 20\n\t"
        "s_or_b32 s74, s74, s75\n\t"
        "s_mov_b32 s75, 0xfc00\n\t"
        "s_mov_b32 s73, 4\n\t"
        "s_waitcnt lgkmcnt(0)\n\t"
        "s_mov_b32 s76, s70\n\t"
        "s_and_b32 s77, s71, 0xffff\n\t"
        "Lpro_bh_loop:\n\t"
        "buffer_load_dwordx4 %[t0], s[76:79], s74 offen nt lds\n\t"
        "s_add_u32 s74, s74, s75\n\t"
        "buffer_load_dwordx4 %[t1], s[76:79], s74 offen offset:1024 nt lds\n\t"
        "s_add_u32 s74, s74, s75\n\t"
        "buffer_load_dwordx4 %[t0], s[76:79], s74 offen offset:2048 nt lds\n\t"
        "s_add_u32 s74, s74, s75\n\t"
        "buffer_load_dwordx4 %[t1], s[76:79], s74 offen offset:3072 nt lds\n\t"
        "s_add_u32 m0, m0, 0x1000\n\t"
        "s_add_u32 s74, s74, 0x10c00\n\t"
        "s_sub_u32 s73, s73, 1\n\t"
        "s_cmp_lg_u32 s73, 0\n\t"
        "s_cbranch_scc1 Lpro_bh_loop\n\t"
        "s_branch Lpro_end\n\t"
        "Lpro_bl:\n\t"
        "v_lshlrev_b32 %[t0], 4, %[t4]\n\t"
        "v_and_b32 %[t1], 31, %[t4]\n\t"
        "v_lshlrev_b32 %[t1], 13, %[t1]\n\t"
        "s_lshl_b32 s74, s73, 12\n\t"
        "s_add_u32 m0, s74, %[r1]\n\t"
        "s_waitcnt lgkmcnt(0)\n\t"
        "s_and_b32 s65, s65, 0xffff\n\t"
        "s_movk_i32 s66, 0x4000\n\t"
        "s_mov_b32 s67, s79\n\t"
        "buffer_load_dwordx4 %[t0], s[64:67], s74 offen lds\n\t"
        "buffer_load_dwordx4 %[t0], s[64:67], s74 offen offset:1024 lds\n\t"
        "buffer_load_dwordx4 %[t0], s[64:67], s74 offen offset:2048 lds\n\t"
        "buffer_load_dwordx4 %[t0], s[64:67], s74 offen offset:3072 lds\n\t"
        "s_cmp_lg_u32 s72, %[c3w]\n\t"
        "s_cbranch_scc1 Lpro_end\n\t"
        "s_lshr_b32 s74, s70, 7\n\t"
        "s_sub_u32 s74, 3, s74\n\t"
        "s_and_b32 s74, s74, 7\n\t"
        "s_sub_u32 s75, s74, %[c]\n\t"
        "s_and_b32 s75, s75, 7\n\t"
        "s_lshl3_add_u32 s75, s75, s74\n\t"
        "s_lshl_b32 s75, s75, 7\n\t"
        "s_bfe_u32 s82, %[c], 0x30003\n\t"
        "s_bfe_u32 s83, %[c], 0x10006\n\t"
        "s_lshl2_add_u32 s82, s82, s83\n\t"
        "s_lshl_b32 s82, s82, 18\n\t"
        "s_or_b32 s74, s82, s75\n\t"
        "s_mov_b32 m0, %[junk]\n\t"
        "s_mov_b32 s76, s70\n\t"
        "s_and_b32 s77, s71, 0xffff\n\t"
        "buffer_load_dword %[t1], s[76:79], s74 offen nt lds\n\t"
        "s_branch Lpro_end\n\t"
        "Lpro_ah:\n\t"
        "v_and_b32 %[t4], 15, %[t4]\n\t"
        "v_xor_b32 %[t0], %[t4], %[t1]\n\t"
        "v_lshlrev_b32 %[t1], 8, %[t1]\n\t"
        "v_lshl_or_b32 %[t0], %[t0], 4, %[t1]\n\t"
        "v_xor_b32 %[t1], 64, %[t0]\n\t"
        "v_xor_b32 %[t2], 0x80, %[t0]\n\t"
        "v_xor_b32 %[t3], 0x80, %[t1]\n\t"
        "s_bfe_u32 s74, %[c], 0x20005\n\t"
        "s_and_b32 s75, %[c], 7\n\t"
        "s_lshl3_add_u32 s74, s74, s75\n\t"
        "s_bfe_u32 s75, %[c], 0x20003\n\t"
        "s_lshl4_add_u32 s74, s74, s75\n\t"
        "s_or_b32 s74, s74, 12\n\t"
        "s_lshl2_add_u32 s74, s74, s73\n\t"
        "s_lshl_b32 s74, s74, 14\n\t"
        "s_brev_b32 s78, 64\n\t"
        "s_mov_b32 s73, 4\n\t"
        "s_waitcnt lgkmcnt(0)\n\t"
        "s_sleep %[dly]\n\t"
        "s_mov_b32 s76, s66\n\t"
        "s_and_b32 s77, s67, 0xffff\n\t"
        "Lpro_k_loop:\n\t"
        "buffer_load_dwordx4 %[t0], s[76:79], s74 offen nt lds\n\t"
        "buffer_load_dwordx4 %[t1], s[76:79], s74 offen offset:1024 nt lds\n\t"
        "buffer_load_dwordx4 %[t2], s[76:79], s74 offen offset:2048 nt lds\n\t"
        "buffer_load_dwordx4 %[t3], s[76:79], s74 offen offset:3072 nt lds\n\t"
        "s_add_u32 m0, m0, 0x1000\n\t"
        "s_addk_i32 s74, 0x1000\n\t"
        "s_sub_u32 s73, s73, 1\n\t"
        "s_cmp_lg_u32 s73, 0\n\t"
        "s_cbranch_scc1 Lpro_k_loop\n\t"
        "Lpro_end:\n\t"
        ".pushsection .text,1\n\t"
        ".fill 512, 4, 0xBF800000\n\t"
        ".popsection\n\t"
        "s_waitcnt lgkmcnt(0)"
        : [t0] "=&v"(t0), [t1] "=&v"(t1), [t2] "=&v"(t2), [t3] "=&v"(t3), [t4] "=&v"(t4)
        : [ka] "s"(__builtin_amdgcn_kernarg_segment_ptr()), [tid] "v"(threadIdx.x), [c] "s"(blockIdx.x), [cl] "i"(CODE_LINES), [cs] "i"(CODE_STRIDE),
          [junk] "i"(L_ST + 1024), [r1] "i"(L_R1), [dly] "i"(A_START_DELAY),
          [mpj] "i"(MPJ), [mpb] "i"(WS_MP - 128 * MPJ), [mpf] "i"(MP_FIRST), [c3w] "i"(C3FIX ? 0 : 9), [wqb] "i"(WQ_TOUCH ? 0x800000 : 0)
        : "s64", "s65", "s66", "s67", "s68", "s69", "s70", "s71", "s72", "s73", "s74", "s75", "s76", "s77", "s78", "s79", "s80", "s81",
          "s82", "s83", "s84", "s85", "s86", "s87", "v4", "v5", "v6", "v7", "scc", "memory");
  }
  STAMP(1);
  LAS char* L = (LAS char*)smem;
  constexpr u32 lbase = 0;
  const int tid = threadIdx.x, l = tid & 63, w = __builtin_amdgcn_readfirstlane(tid >> 6), c = blockIdx.x;
  const int g = l >> 4, xcd = c & 7, sl = (c >> 3) & 15, u = w & 3;
  const bool hiw = w >= 4;
  const u32 vv = g * 256 + (((((l & 15) >> 1) ^ g) << 1) | (l & 1)) * 16;
  const int band = 2 * (sl & 3) + (sl >> 3), cg = 8 * band + ((xcd + band) & 7), kh = (sl >> 2) & 1;
  const int bk = (sl >> 2) * 8 + xcd;
  const u32 kvb = (bk * 4096 + 3072 + 256 * (sl & 3)) * 256;
  const int q4 = (l >> 2) & 3, p4 = l & 3, b8 = l & 7, d8 = (l >> 3) & 1;
  const int fsw = q4 | ((g & 1) << 2);
  const rsrc_t rsW = mkrs(ws, WS_END);
  const v4u sent4 = {SENT, SENT, SENT, SENT};
  const f32x4 zero4 = {0.f, 0.f, 0.f, 0.f};
  u32 tagLo = 0, tagHi = 0;

  const u32 fo = WS_FLAG + (16 * w + (l & 15)) * 128;
  if (c < 128) {
    if (!hiw) {
      LAS char* px = L + L_R1 + w * 4096 + l * 16;
      if (C3FIX && w == 0) VMWAIT(1); else VMWAIT(0);
      STAMP(13);
      const v4u xc0 = *(LAS v4u*)(px + kh * 1024), xo0 = *(LAS v4u*)(px + (kh ^ 1) * 1024);
      const v4u xc1 = *(LAS v4u*)(px + 2048 + kh * 1024), xo1 = *(LAS v4u*)(px + 2048 + (kh ^ 1) * 1024);
      *(LAS v4u*)(L + L_ACT + (2 * w) * 1056 + l * 16) = ln_half(xc0, xo0);
      *(LAS v4u*)(L + L_ACT + (2 * w + 1) * 1056 + l * 16) = ln_half(xc1, xo1);
      STAMP(12);
      if (w == 0) HOP(B0, BR);
    }
    LOAD_TAG();
    PAD(B0, B1);
    BARRIER();
    const rsrc_t rv = mkrs(vc, 8 * 4 * 4096 * 128 * 2);
    if (w == 7) {
#pragma unroll
      for (int i = 0; i < 4; ++i) st16(rsW, WS_WP + c * 4096 + i * 1024 + l * 16, sent4);
      st16(rsW, l < 8 ? WS_H + c * 128 + l * 16 : WS_END, sent4);
    }
    if (w < TOUCH_EARLY) { nap<2>(); touch(rv, g * 16384 + (l & 15) * 128, kvb + (2 * V_OWN + w) * 2048, lbase + L_ST + 1024); }
    const int bq = 2 * u + (l >> 5), cp = l & 31;
    LAS char* pq = L + L_P + bq * 272 + cp * 8;
    u32 qo = WS_QG + (((bq * 4 + (cg >> 4)) * 2 + kh) * 1024 + 64 * (cg & 15) + 2 * cp) * 8;
    {
      const int kh2 = w >> 2;
      s16x8 bf[8];
      LAS char* pb = L + L_ACT + b8 * 1056 + kh2 * 512 + 16 * g;
#pragma unroll
      for (int ks = 0; ks < 8; ++ks) bf[ks] = *(LAS s16x8*)(pb + ks * 64);
      LAS char* pa = L + L_R0 + kh2 * 32768 + (8 * g + q4) * 128 + ((u ^ (((q4 >> 1) & 1) | ((g & 1) << 1))) << 5) + 8 * p4;
      LAS char* pw = L + L_P + (kh2 * 8 + b8) * 272 + (16 * u + 4 * g) * 4;
      PIN(pa); PIN(pw); PIN(pq); PIN(qo);
      if (w == 7) VMWAIT(5); else if (hiw) VMWAIT(0);
      STAMP(2);
      BARRIER();
      PAD(B1, B2);
      if (w == 6 && l < 8) __builtin_amdgcn_raw_buffer_store_b32(tagLo, rsW, WS_QH + (l * 16 + (cg >> 2)) * 128 + ((cg & 3) * 2 + kh) * 4, 0, AUX_SC1);
      s16x8 af[8];
#pragma unroll
      for (int ks = 0; ks < 8; ++ks) af[ks] = trfrag(pa + ks * 4096, 512);
      FENCE();
      f32x4 acc = zero4;
#pragma unroll
      for (int ks = 0; ks < 8; ++ks) acc = mfma(af[ks], bf[ks], acc);
      if (!d8) *(LAS f32x4*)pw = acc;
    }
    BARRIER();
    const rsrc_t r1 = mkrs(w1, 1024 * 3584 * 2);
    const int lc = (3 - (int)(((u32)(size_t)w1) >> 7) - 7 * xcd) & 7;
    const bool slow = C3HEAD && lc < 7 && (sl >> 1) == lc;
    if (!hiw) {
      PAD(B2, B6);
      const f32x2 s = *(LAS f32x2*)pq + *(LAS f32x2*)(pq + 8 * 272);
      st16(rsW, qo, v4u{__float_as_uint(s[0]), tagLo, __float_as_uint(s[1]), tagHi});
      STAMP(3);
      if (!slow) nap<M_DELAY>();
    } else if (w == 4 && !slow) {
      nap<TOUCH_DELAY>();
#pragma unroll
      for (int i = 2 * V_OWN + TOUCH_EARLY; i < NTOUCH_V; ++i) touch(rv, g * 16384 + (l & 15) * 128, kvb + i * 2048, lbase + L_ST + 1024);
    } else if (w == 5 && C3HEAD && lc < 7 && sl >= 14) {
      nap<TOUCH_DELAY>();
      const int s2 = 2 * lc + (sl & 1);
      const u32 kv2 = (((s2 >> 2) * 8 + xcd) * 4096 + 3072 + 256 * (s2 & 3)) * 256;
#pragma unroll
      for (int i = 2 * V_OWN + TOUCH_EARLY; i < NTOUCH_V; ++i) touch(rv, g * 16384 + (l & 15) * 128, kv2 + i * 2048, lbase + L_ST + 1024);
    } else if (w == 7) {
      VMWAIT(0);
      if (l == 0) __builtin_amdgcn_raw_buffer_store_b64(v2u{tagLo, tagHi}, rsW, WS_FLAG + c * 128, 0, AUX_SC1);
      STAMP(3);
    }
    if (sl >= 14) { STAMP_FLUSH(); return; }
    BARRIER();
    const int m = xcd * 14 + sl;
    PAD(B6, BR);
    STAMP(4);
    {
      const rsrc_t r2 = mkrs(w2, 3584 * 1024 * 2);
      const u32 v2o = vv + g * (2048 - 256);
      const u32 v1 = (l >> 2) * 7168 + ((((l >> 1) & 1) ^ ((l >> 4) & 1)) << 5) + (l & 1) * 16;
      const int h0 = 64 * w, h1 = h0 + 512;
      dma4(r1, v1, v1, v1, v1, h0 * 7168 + 64 * m, 16 * 7168 - 1024, lbase + L_R1 + h0 * 64);
      if (MORD == 0) dma4(r2, v2o, v2o, v2o ^ 128, v2o ^ 128, 32 * m * 2048 + 256 * w, 4 * 2048 - 1024, lbase + L_R0 + 8 * w * 1024);
      if (MORD == 2) BARRIER();
      STAMP(5);
      dma4(r1, v1, v1, v1, v1, h1 * 7168 + 64 * m, 16 * 7168 - 1024, lbase + L_R1 + h1 * 64);
      if (MORD >= 1) { BARRIER(); STAMP(6); dma4(r2, v2o, v2o, v2o ^ 128, v2o ^ 128, 32 * m * 2048 + 256 * w, 4 * 2048 - 1024, lbase + L_R0 + 8 * w * 1024); }
      dma4(r2, v2o, v2o, v2o ^ 128, v2o ^ 128, (32 * m + 16) * 2048 + 256 * w, 4 * 2048 - 1024, lbase + L_R0 + (8 * w + 4) * 1024);
    }
    STAMP(7);
    if (w == 0) HOP(B7, BS);
    PAD(B7, B8);
    const v4u ones = {0x3F803F80u, 0x3F803F80u, 0x3F803F80u, 0x3F803F80u};
    s16x8 f2[8];
    LAS char* pz = L + L_P + (u * 8 + b8) * 144 + (16 * d8 + 4 * g) * 4;
    LAS char* pr = L + L_P + b8 * 144 + g * 32 + d8 * 16;
    LAS char* pc = L + L_CS + g * 32 + d8 * 16;
    LAS char* pa1 = L + L_R1 + (256 * u + 4 * g + q4) * 64 + ((g & 1) << 5) + 8 * p4;
    u32 hb = WS_H + u * 4096 + (4 * d8 + g) * 128 + b8 * 16;
    u32 mo = WS_MP + (4 * (4 * w + d8) + g) * MPJ + m * 128 + b8 * 16;
    LAS char* pst = L + L_ST + b8 * 32 + (u * 2 + d8) * 4;
    LAS char* psr = L + L_ST + d8 * 256 + b8 * 32;
    PIN(hb); PIN(mo); PIN(pst); PIN(psr); PIN(pz); PIN(pr); PIN(pc); PIN(pa1);
    POLL_LOOP {
      RELOAD();
      const v2u fl = __builtin_amdgcn_raw_buffer_load_b64(rsW, fo, 0, AUX_SC1);
      if (__builtin_amdgcn_ballot_w64(((fl.x ^ tagLo) | (fl.y ^ tagHi)) != 0u) == 0ull) break;
    }
    VMWAIT(0);
    STAMP(8);
    PAD(B8, B9);
#pragma unroll
    for (int T = 0; T < 8; ++T) f2[T] = trfrag(L + L_R0 + w * 8192 + (8 * g + q4) * 256 + ((T ^ fsw) << 5) + 8 * p4, 1024);
    BARRIER();
#pragma unroll
    for (int T = 0; T < 8; ++T) PIN(f2[T]);
    PAD(B9, B10);
    if (!hiw) {
      s16x8 f1[2][8];
#pragma unroll
      for (int nt = 0; nt < 2; ++nt)
#pragma unroll
        for (int ks = 0; ks < 8; ++ks) f1[nt][ks] = trfrag((LAS char*)((u32)(size_t)pa1 ^ (32 * nt)) + ks * 2048, 1024);
      hint_wait(ws + WS_FLAG + c * 128 + 64, tagLo, H_DELAY);
      STAMP(11);
      v4u hf[4];
#pragma unroll
      for (int i = 0; i < 4; ++i) hf[i] = ld16(rsW, hb + i * 1024);
      FENCE();
      {
        f32x4 c0 = zero4, c1 = zero4;
#pragma unroll
        for (int ks = 0; ks < 8; ++ks) { c0 = mfma(f1[0][ks], __builtin_bit_cast(s16x8, ones), c0); c1 = mfma(f1[1][ks], __builtin_bit_cast(s16x8, ones), c1); }
        if ((l & 15) == 0) { *(LAS f32x4*)(L + L_CS + u * 144 + 16 * g) = c0; *(LAS f32x4*)(L + L_CS + u * 144 + 64 + 16 * g) = c1; }
      }
      FENCE();
      PAD(B10, B11);
      POLL_LOOP {
        if (__builtin_amdgcn_ballot_w64(max(max(max4(hf[0]), max4(hf[1])), max(max4(hf[2]), max4(hf[3]))) == SENT) == 0ull) break;
        nap<H_NAP>();
        RELOAD();
#pragma unroll
        for (int i = 0; i < 4; ++i) hf[i] = ld16(rsW, hb + i * 1024);
      }
      STAMP(9);
      PAD(B11, B12);
      f32x4 aE[2] = {zero4, zero4}, aO[2] = {zero4, zero4}, aS = zero4, aQ = zero4;
#pragma unroll
      for (int i = 0; i < 4; ++i) { aS = mfma(ones, hf[i], aS); aQ = mfma(hf[i], hf[i], aQ); }
#pragma unroll
      for (int i = 0; i < 4; ++i)
#pragma unroll
        for (int nt = 0; nt < 2; ++nt) {
          aE[nt] = mfma(f1[nt][2 * i], __builtin_bit_cast(s16x8, hf[i]), aE[nt]);
          aO[nt] = mfma(f1[nt][2 * i + 1], __builtin_bit_cast(s16x8, hf[i]), aO[nt]);
        }
      const float sv = (p4 & 2) ? ((p4 & 1) ? aQ[3] : aQ[2]) : ((p4 & 1) ? aQ[1] : aQ[0]);
      if (g == ((l & 15) >> 2)) { *(LAS float*)pst = aS[0]; *(LAS float*)(pst + 256) = sv; }
      f32x4 z;
#pragma unroll
      for (int i = 0; i < 4; ++i) z[i] = (d8 ? aO[1][i] : aE[0][i]) + dpp<DPP_R8>(d8 ? aO[0][i] : aE[1][i]);
      *(LAS f32x4*)pz = z;
    }
    BARRIER();
    PAD(B12, B13);
    {
      const f32x4 t0 = *(LAS f32x4*)psr, t1 = *(LAS f32x4*)(psr + 16);
      const f32x4 z0 = *(LAS f32x4*)pr, z1 = *(LAS f32x4*)(pr + 8 * 144), z2 = *(LAS f32x4*)(pr + 16 * 144), z3 = *(LAS f32x4*)(pr + 24 * 144);
      const f32x4 c0 = *(LAS f32x4*)pc, c1 = *(LAS f32x4*)(pc + 144), c2 = *(LAS f32x4*)(pc + 2 * 144), c3 = *(LAS f32x4*)(pc + 3 * 144);
      FENCE();
      const float mine = (((t0[0] + t0[1]) + (t0[2] + t0[3])) + ((t1[0] + t1[1]) + (t1[2] + t1[3]))) * (1.f / 1024.f);
      const float other = dpp<DPP_R8>(mine);
      const float mean = d8 ? other : mine, ex2 = d8 ? mine : other;
      const float var = fmaxf(fmaf(-mean, mean, ex2), 0.f);
      const u32 mv = pk2(mean, var);
      const float den = rbf(__builtin_amdgcn_sqrtf(rbf(bfhi(mv) + 1e-5f)));
      const float inv = __builtin_amdgcn_rcpf(den);
      const float mb = bflo(mv);
      float gv[4];
#pragma unroll
      for (int i = 0; i < 4; ++i) gv[i] = fmaf(-mb, (c0[i] + c1[i]) + (c2[i] + c3[i]), (z0[i] + z1[i]) + (z2[i] + z3[i])) * inv;
      const s16x2 zz = {0, 0};
      const int g0 = __builtin_bit_cast(int, __builtin_elementwise_max(__builtin_bit_cast(s16x2, pk2(gv[0], gv[1])), zz));
      const int g1 = __builtin_bit_cast(int, __builtin_elementwise_max(__builtin_bit_cast(s16x2, pk2(gv[2], gv[3])), zz));
      PAD(B13, BS);
      v4u B0, B1;
      B0[0] = __builtin_amdgcn_update_dpp(0, g0, DPP_ID, 0xf, 0x3, false);
      B0[1] = __builtin_amdgcn_update_dpp(0, g1, DPP_ID, 0xf, 0x3, false);
      B0[2] = __builtin_amdgcn_update_dpp(0, g0, DPP_R8, 0xf, 0x3, false);
      B0[3] = __builtin_amdgcn_update_dpp(0, g1, DPP_R8, 0xf, 0x3, false);
      B1[0] = __builtin_amdgcn_update_dpp(0, g0, DPP_R8, 0xf, 0xc, false);
      B1[1] = __builtin_amdgcn_update_dpp(0, g1, DPP_R8, 0xf, 0xc, false);
      B1[2] = __builtin_amdgcn_update_dpp(0, g0, DPP_ID, 0xf, 0xc, false);
      B1[3] = __builtin_amdgcn_update_dpp(0, g1, DPP_ID, 0xf, 0xc, false);
#pragma unroll
      for (int i = 0; i < 2; ++i) {
        f32x4 u0 = mfma(f2[4 * i], __builtin_bit_cast(s16x8, B0), zero4), u1 = mfma(f2[4 * i + 1], __builtin_bit_cast(s16x8, B0), zero4);
        u0 = mfma(f2[4 * i + 2], __builtin_bit_cast(s16x8, B1), u0);
        u1 = mfma(f2[4 * i + 3], __builtin_bit_cast(s16x8, B1), u1);
        __builtin_amdgcn_raw_buffer_store_b128(v4u{pk2(u0[0], u0[1]), pk2(u0[2], u0[3]), pk2(u1[0], u1[1]), pk2(u1[2], u1[3])}, rsW, mo, i * 8 * MPJ, AUX_SC1);
      }
    }
    STAMP(10);
    STAMP_FLUSH();
    return;
  }

  const int j = c - 128;
  const int s = sl & 3, b = bk >> 2, kvh = bk & 3;
  const int kvo = sl >> 2, nq = sl & 3, ksl = kvo * 8 + xcd;
  const int jcol = 32 * (j >> 2) + 4 * (j & 3) + 16 * (g & 1);
  const rsrc_t rv = mkrs(vc, 8 * 4 * 4096 * 128 * 2);
  const rsrc_t ro_ = mkrs(wo, 4096 * 1024 * 2);
  const u32 vo = (l >> 5) * 2048 + (((((l & 31) >> 1) ^ (l >> 5)) << 1) | (l & 1)) * 16;
  auto load_wo = [&](int h, int half) {
    const u32 vx = vo ^ (128 * half);
    dma4(ro_, vx, vx ^ 64, vx, vx ^ 64, ((kvo * 8 + h) * 128 + 16 * xcd + 8 * half) * 2048 + 512 * nq, 4096 - 1024, lbase + L_R0 + (8 * h + 4 * half) * 1024);
  };
  v2u xres = {0u, 0u};
  if (hiw) {
#pragma unroll
    for (int i = 0; i < V_OWN; ++i) dma4c(rv, vv, vv, vv ^ 128, vv ^ 128, kvb + u * 16384 + i * 4096, lbase + L_R1 + u * 16384 + i * 4096);
  } else {
    if (tid < 32) *(LAS u32*)(L + L_FL + tid * 4) = 0u;
    if (w == 3) HOP(A0, AR);
  }
  LOAD_TAG();
  BARRIER();
  if (hiw) {
    PAD(A0, A1);
    asm volatile("s_waitcnt vmcnt(%0)" :: "i"(4 * V_OWN) : "memory");
    LSET(F_KR + u * 4);
    STAMP(2);
    LWAIT4(V_GATE ? F_Q : F_KD);
#pragma unroll
    for (int i = V_OWN; i < 4; ++i) dma4c(rv, vv, vv, vv ^ 128, vv ^ 128, kvb + u * 16384 + i * 4096, lbase + L_R1 + u * 16384 + i * 4096);
    STAMP(3);
    if (!V_GATE) LWAIT4(F_Q);
#pragma unroll
    for (int i = 0; i < WO_PRE0; ++i) load_wo(2 * u + (i & 1), i >> 1);
    asm volatile("s_waitcnt vmcnt(%0)" :: "i"(4 * WO_PRE0) : "memory");
    LSET(F_V + u * 4);
    STAMP(5);
#pragma unroll
    for (int i = WO_PRE0; i < WO_PRE; ++i) load_wo(2 * u + (i & 1), i >> 1);
    STAMP(4);
    LWAIT4(F_O);
#pragma unroll
    for (int i = WO_PRE; i < 4; ++i) load_wo(2 * u + (i & 1), i >> 1);
    STAMP(6);
  } else {
    PAD(A1, A2);
    LAS char* pk = L + L_R0 + (64 * u + (l & 15)) * 256;
    LAS char* pb = L + L_ACT + b8 * 288 + 16 * g;
    LAS char* pp = L + L_P + b8 * 544 + (64 * u + 16 * d8 + 4 * g) * 2;
    LAS char* ps = L + L_ST + b8 * 16;
    LAS char* pa = L + L_R1 + (8 * g + q4) * 256 + (((2 * u) ^ fsw) << 5) + 8 * p4;
    LAS char* pf_ = L + L_P + b8 * 544 + 16 * g;
    u32 oo = WS_OP + (((kvh * 8 + 2 * u + d8) * 8 + b) * 4 + s) * 512 + g * 128 + b8 * 16;
    const u32 qb = WS_QG + bk * 16384 + w * 2048 + l * 16;
    PIN(pb); PIN(pp); PIN(ps); PIN(pa); PIN(pf_); PIN(oo);
    hint_wait8(ws + WS_QH + (b * 16 + kvh * 4 + u) * 128, tagLo);
    nap<QH_DELAY>();
    STAMP(12);
    v4u d[4];
#pragma unroll
    for (int i = 0; i < 4; ++i) d[i] = ld16(rsW, qb + (i >> 1) * 8192 + (i & 1) * 1024);
    if (w == 0) xres = __builtin_amdgcn_raw_buffer_load_b64(mkrs(x, 8 * 1024 * 2), b8 * 2048 + jcol * 2, 0, 0);
    LSET(F_KD + u * 4);
    LWAIT1(F_KR + u * 4);
    s16x8 kf[4][4];
#pragma unroll
    for (int ks = 0; ks < 4; ++ks) {
      const int pos = ((4 * ks + g) ^ (l & 15)) * 16;
#pragma unroll
      for (int T = 0; T < 4; ++T) kf[T][ks] = *(LAS s16x8*)(pk + T * 4096 + pos);
    }
    FENCE();
    PAD(A2, A3);
    POLL_LOOP {
      u32 bad = 0;
#pragma unroll
      for (int i = 0; i < 4; ++i) bad |= (d[i].y ^ tagLo) | (d[i].w ^ tagHi);
      if (__builtin_amdgcn_ballot_w64(bad != 0u) == 0ull) break;
      nap<Q_NAP>();
      RELOAD();
#pragma unroll
      for (int i = 0; i < 4; ++i) d[i] = ld16(rsW, qb + (i >> 1) * 8192 + (i & 1) * 1024);
    }
#pragma unroll
    for (int i = 0; i < 2; ++i)
      *(LAS u32*)(L + L_ACT + (2 * w + i) * 288 + l * 4) = pk2(__uint_as_float(d[i].x) + __uint_as_float(d[i + 2].x), __uint_as_float(d[i].z) + __uint_as_float(d[i + 2].z));
    VMWAIT(0);
    LSET(F_Q + u * 4);
    PIN(xres);
    STAMP(2);
    LWAIT4(F_Q);
    STAMP(3);
    PAD(A3, A4);
    s16x8 bf[4];
#pragma unroll
    for (int ks = 0; ks < 4; ++ks) bf[ks] = *(LAS s16x8*)(pb + ks * 64);
    FENCE();
    f32x4 ac[4] = {zero4, zero4, zero4, zero4};
#pragma unroll
    for (int T = 0; T < 4; T += 2)
#pragma unroll
      for (int ks = 0; ks < 4; ++ks) { ac[T] = mfma(kf[T][ks], bf[ks], ac[T]); ac[T + 1] = mfma(kf[T + 1][ks], bf[ks], ac[T + 1]); }
    float sc[8];
#pragma unroll
    for (int pr = 0; pr < 2; ++pr)
#pragma unroll
      for (int r = 0; r < 4; r += 2) {
        const u32 t = pk2(d8 ? ac[2 * pr + 1][r] : ac[2 * pr][r], d8 ? ac[2 * pr + 1][r + 1] : ac[2 * pr][r + 1]);
        const u32 t2 = pk2(bflo(t) * 0.08838834764831845f, bfhi(t) * 0.08838834764831845f);
        sc[4 * pr + r] = bflo(t2); sc[4 * pr + r + 1] = bfhi(t2);
      }
    float mx = fmaxf(fmaxf(fmaxf(sc[0], sc[1]), fmaxf(sc[2], sc[3])), fmaxf(fmaxf(sc[4], sc[5]), fmaxf(sc[6], sc[7])));
    mx = fmaxf(mx, dpp<DPP_R8>(mx));
    mx = max16_32(mx) * LOG2E;
    PAD(A4, A5);
    float pr_[8];
#pragma unroll
    for (int r = 0; r < 8; ++r) pr_[r] = __builtin_amdgcn_exp2f(fmaf(sc[r], LOG2E, -mx));
    *(LAS v2u*)pp = v2u{pk2(pr_[0], pr_[1]), pk2(pr_[2], pr_[3])};
    *(LAS v2u*)(pp + 64) = v2u{pk2(pr_[4], pr_[5]), pk2(pr_[6], pr_[7])};
    float sm = ((pr_[0] + pr_[1]) + (pr_[2] + pr_[3])) + ((pr_[4] + pr_[5]) + (pr_[6] + pr_[7]));
    sm += dpp<DPP_R8>(sm);
    sm = sum16_32(sm);
    if (l < 8) { *(LAS float*)(ps + u * 4) = mx; *(LAS float*)(ps + 128 + u * 4) = sm; }
    LSET(F_P + u * 4);
    STAMP(4);
    LWAIT4(F_P);
    PAD(A5, A6);
    const f32x4 m4 = *(LAS f32x4*)ps, s4 = *(LAS f32x4*)(ps + 128);
    s16x8 pf[8], vf[2][8];
#pragma unroll
    for (int ks = 0; ks < 8; ++ks) pf[ks] = *(LAS s16x8*)(pf_ + ks * 64);
    v4u vfl = *(volatile LAS v4u*)(L + L_FL + F_V);
    FENCE();
    const float mm = fmaxf(fmaxf(m4[0], m4[1]), fmaxf(m4[2], m4[3]));
    f32x4 f;
#pragma unroll
    for (int i = 0; i < 4; ++i) f[i] = __builtin_amdgcn_exp2f(m4[i] - mm);
    const float tot = fmaf(f[0], s4[0], f[1] * s4[1]) + fmaf(f[2], s4[2], f[3] * s4[3]);
    if (w == 3 && l < 8)
      __builtin_amdgcn_raw_buffer_store_b64(v2u{__float_as_uint(mm + __builtin_amdgcn_logf(tot)), (l & 1) ? tagHi : tagLo}, rsW, WS_LSE + (bk * 4 + s) * 128 + l * 8, 0, AUX_SC1);
    if (__builtin_amdgcn_readfirstlane(vfl.x & vfl.y & vfl.z & vfl.w) == 0u) LWAIT4(F_V);
    asm volatile("" ::: "memory");
    STAMP(5);
    PAD(A6, A7);
#pragma unroll
    for (int ks = 0; ks < 8; ++ks) {
      vf[0][ks] = trfrag(pa + ks * 8192, 1024);
      vf[1][ks] = trfrag((LAS char*)((u32)(size_t)pa ^ 32) + ks * 8192, 1024);
    }
    FENCE();
    f *= __builtin_amdgcn_rcpf(tot);
    f32x4 o = zero4;
#pragma unroll
    for (int i = 0; i < 4; ++i) {
      const f32x4 a0 = mfma(vf[0][2 * i + 1], pf[2 * i + 1], mfma(vf[0][2 * i], pf[2 * i], zero4));
      const f32x4 a1 = mfma(vf[1][2 * i + 1], pf[2 * i + 1], mfma(vf[1][2 * i], pf[2 * i], zero4));
#pragma unroll
      for (int r = 0; r < 4; ++r) o[r] = fmaf(d8 ? a1[r] : a0[r], f[i], o[r]);
    }
    st16(rsW, oo, v4u{pk2(o[0], o[1]), tagLo, pk2(o[2], o[3]), tagHi});
    LSET(F_O + u * 4);
    STAMP(6);
  }
  const u32 one = (l & 8) ? 0u : ((l & 1) ? 0x3F800000u : 0x00003F80u);
  const int si = b8 >> 1;
  v4u sel = {si == 0 ? one : 0u, si == 1 ? one : 0u, si == 2 ? one : 0u, si == 3 ? one : 0u};
  u32 wb = WS_WP + j * 4096 + l * 16, hb = WS_H + j * 128 + l * 16, mb = WS_MP + j * MPJ + w * 2048 + l * 16, oo = b8 * 2048 + jcol * 2;
  {
    const int sh = l >> 5;
    const u32 lo0 = WS_LSE + ((w * 4 + kvo) * 4 + sh) * 128 + b8 * 8, ltag = (l & 1) ? tagHi : tagLo;
    const u32 la = (!ALLSYNC || l < 16) ? fo : WS_LSE + w * 2048 + (l & 15) * 128;
    u32 ob = WS_OP + (ksl * 8 + w) * 2048 + l * 16;
    LAS char* po = L + L_ACT + w * 288 + b8 * 32 + ((l >> 3) & 3) * 8 + sh * 4;
    u32 wpo = WS_WP + (4 * (8 * nq + w) + g) * 4096 + ksl * 128 + b8 * 16;
    LAS char* pb = L + L_ACT + b8 * 288 + 16 * g;
    LAS char* pa0 = L + L_R0 + (8 * g + q4) * 512 + (((2 * w) ^ fsw) << 5) + 8 * p4;
    PIN(ob); PIN(po); PIN(wpo); PIN(pb); PIN(pa0); PIN(sel); PIN(wb); PIN(hb); PIN(mb); PIN(oo);
    v4u ea = ld16(rsW, la);
    nap<O_DELAY>();
    v2u e0 = __builtin_amdgcn_raw_buffer_load_b64(rsW, lo0, 0, AUX_SC1), e1 = __builtin_amdgcn_raw_buffer_load_b64(rsW, lo0 + 256, 0, AUX_SC1);
    v4u d0 = ld16(rsW, ob), d1 = ld16(rsW, ob + 1024);
    if (hiw) VMWAIT(5);
    STAMP(7);
    asm volatile("s_barrier" ::: "memory");
    PAD(A7, A8);
    s16x8 wf[2][4];
#pragma unroll
    for (int T = 0; T < 2; ++T)
#pragma unroll
      for (int ks = 0; ks < 4; ++ks) wf[T][ks] = trfrag((LAS char*)((u32)(size_t)pa0 ^ (32 * T)) + ks * 16384, 2048);
    FENCE();
    POLL_LOOP {
      const u32 ta = (!ALLSYNC || l < 16) ? ((ea.x ^ tagLo) | (ea.y ^ tagHi)) : ((ea.y ^ tagLo) | (ea.w ^ tagHi));
      if (__builtin_amdgcn_ballot_w64(ta != 0u) == 0ull) break;
      RELOAD();
      ea = ld16(rsW, la);
    }
    POLL_LOOP {
      const u32 bad = ((e0.y ^ ltag) | (e1.y ^ ltag)) | ((d0.y ^ tagLo) | (d0.w ^ tagHi)) | ((d1.y ^ tagLo) | (d1.w ^ tagHi));
      if (__builtin_amdgcn_ballot_w64(bad != 0u) == 0ull) break;
      nap<O_NAP>();
      RELOAD();
      e0 = __builtin_amdgcn_raw_buffer_load_b64(rsW, lo0, 0, AUX_SC1); e1 = __builtin_amdgcn_raw_buffer_load_b64(rsW, lo0 + 256, 0, AUX_SC1);
      d0 = ld16(rsW, ob); d1 = ld16(rsW, ob + 1024);
    }
    PAD(A8, A9);
    const float l0 = __uint_as_float(e0.x), l1 = __uint_as_float(e1.x);
    float mx = fmaxf(l0, l1);
    { auto t = __builtin_amdgcn_permlane32_swap(__float_as_uint(mx), __float_as_uint(mx), false, false); mx = fmaxf(__uint_as_float(t[0]), __uint_as_float(t[1])); }
    const float w0 = __builtin_amdgcn_exp2f(l0 - mx), w1_ = __builtin_amdgcn_exp2f(l1 - mx);
    float tot = w0 + w1_;
    { auto t = __builtin_amdgcn_permlane32_swap(__float_as_uint(tot), __float_as_uint(tot), false, false); tot = __uint_as_float(t[0]) + __uint_as_float(t[1]); }
    const float it_ = __builtin_amdgcn_rcpf(tot);
    const float v0 = fmaf(w0, bflo(d0.x), w1_ * bflo(d1.x)), v1 = fmaf(w0, bfhi(d0.x), w1_ * bfhi(d1.x));
    const float v2 = fmaf(w0, bflo(d0.z), w1_ * bflo(d1.z)), v3 = fmaf(w0, bfhi(d0.z), w1_ * bfhi(d1.z));
    auto t0 = __builtin_amdgcn_permlane32_swap(__float_as_uint(v0), __float_as_uint(v2), false, false);
    auto t1 = __builtin_amdgcn_permlane32_swap(__float_as_uint(v1), __float_as_uint(v3), false, false);
    *(LAS u32*)po = pk2((__uint_as_float(t0[0]) + __uint_as_float(t0[1])) * it_, (__uint_as_float(t1[0]) + __uint_as_float(t1[1])) * it_);
    STAMP(8);
    BARRIER();
    s16x8 bf[4];
#pragma unroll
    for (int ks = 0; ks < 4; ++ks) bf[ks] = *(LAS s16x8*)(pb + ks * 64);
    FENCE();
    f32x4 a0 = zero4, a1 = zero4;
#pragma unroll
    for (int ks = 0; ks < 4; ++ks) { a0 = mfma(wf[0][ks], bf[ks], a0); a1 = mfma(wf[1][ks], bf[ks], a1); }
    if (!d8) st16(rsW, wpo, v4u{pk2(a0[0], a0[1]), pk2(a0[2], a0[3]), pk2(a1[0], a1[1]), pk2(a1[2], a1[3])});
    STAMP(9);
  }
  PAD(A9, A10);
  u32 h01 = 0, h23 = 0;
  if (w == 0) {
    nap<R_DELAY>();
    f32x4 acc = zero4;
    POLL_LOOP {
      RELOAD();
      v4u d[4];
#pragma unroll
      for (int i = 0; i < 4; ++i) d[i] = ld16(rsW, wb + i * 1024);
      FENCE();
      acc = zero4;
#pragma unroll
      for (int i = 0; i < 4; ++i) acc = mfma(sel, d[i], acc);
      if (wave_clean(acc[0])) break;
    }
    PAD(A10, A11);
    const u32 a01 = pk2(acc[0] + dpp<DPP_R8>(acc[0]), acc[1] + dpp<DPP_R8>(acc[1]));
    const u32 a23 = pk2(acc[2] + dpp<DPP_R8>(acc[2]), acc[3] + dpp<DPP_R8>(acc[3]));
    h01 = pk2(bflo(xres.x) + bflo(a01), bfhi(xres.x) + bfhi(a01));
    h23 = pk2(bflo(xres.y) + bflo(a23), bfhi(xres.y) + bfhi(a23));
    auto s0 = __builtin_amdgcn_permlane16_swap(h01, h01, false, false);
    auto s1 = __builtin_amdgcn_permlane16_swap(h23, h23, false, false);
    if (l < 8) st16(rsW, hb, v4u{s0[0], s1[0], s0[1], s1[1]});
    STAMP(10);
  } else if (w == 7) {
    if (l == 0) __builtin_amdgcn_raw_buffer_store_b64(v2u{tagLo, (u32)__builtin_amdgcn_s_memrealtime()}, rsW, WS_FLAG + j * 128 + 64, 0, AUX_SC1);
  }
  asm volatile("s_barrier" ::: "memory");
  PAD(A11, A12);
  LAS char* pfs = L + L_P + (l & 31) * 16;
  if (w < 7) {
    nap<F_DELAY>();
    f32x4 acc = zero4;
    POLL_LOOP {
      RELOAD();
      const v4u d0 = ld16(rsW, mb), d1 = ld16(rsW, mb + 1024);
      FENCE();
      acc = mfma(sel, d1, mfma(sel, d0, zero4));
      if (wave_clean(acc[0])) break;
      nap<F_NAP>();
    }
    STAMP(13);
    if (l < 32) *(LAS f32x4*)(pfs + w * 512) = acc;
  }
  BARRIER();
  PAD(A12, AR);
  if (w == 0) {
    f32x4 p[7];
#pragma unroll
    for (int i = 0; i < 7; ++i) p[i] = *(LAS f32x4*)(pfs + i * 512);
    FENCE();
    const f32x4 acc = ((p[0] + p[1]) + (p[2] + p[3])) + ((p[4] + p[5]) + p[6]);
    const u32 y01 = pk2(acc[0] + dpp<DPP_R8>(acc[0]), acc[1] + dpp<DPP_R8>(acc[1]));
    const u32 y23 = pk2(acc[2] + dpp<DPP_R8>(acc[2]), acc[3] + dpp<DPP_R8>(acc[3]));
    const v2u r = {pk2(bflo(h01) + bflo(y01), bfhi(h01) + bfhi(y01)), pk2(bflo(h23) + bflo(y23), bfhi(h23) + bfhi(y23))};
    if ((l & 40) == 0) __builtin_amdgcn_raw_buffer_store_b64(r, mkrs(out, 8 * 1024 * 2), oo, 0, 0);
    if (j == 0 && l == 0) __builtin_amdgcn_raw_buffer_store_b64(v2u{tagLo, tagHi}, rsW, WS_EPOCH, 0, AUX_SC1);
    STAMP(11);
  }
  STAMP_FLUSH();
}

extern "C" void kernel_launch(void* const* d_in, const int* in_sizes, int n_in, void* d_out, int out_size, void* d_ws, size_t ws_size,
                              hipStream_t stream) {
  (void)hipFuncSetAttribute((const void*)fused_decode_layer, hipFuncAttributeMaxDynamicSharedMemorySize, L_BYTES);
  fused_decode_layer<<<NWG, NTHR, L_BYTES, stream>>>((const char*)d_in[0], (const char*)d_in[1], (const char*)d_in[2], (const char*)d_in[3],
                                                     (const char*)d_in[4], (const char*)d_in[6], (const char*)d_in[7], (char*)d_out, (char*)d_ws + WS_SHIFT(ws_size));
}
```

```cpp
#include <hip/hip_runtime.h>
#include <hip/hip_bf16.h>
#include <stdint.h>

#define DIAG 0
#define A_START_DELAY 4
#ifndef M_DELAY
#define M_DELAY 14
#endif
#define O_DELAY 12
#define R_DELAY 8
#define F_DELAY 75
#define O_NAP 0
#define Q_NAP 1
#define F_NAP 0
#define H_NAP 0
#define H_DELAY 120
#define NTOUCH_V 8
#define TOUCH_DELAY 10
#define C3FIX 1
#define WQ_TOUCH 1
#define CODE_LINES 15
#define CODE_STRIDE 64
#define V_OWN 0
#define W1C3 0
#define C3NT 0
#define QH_DELAY 2
#define WO_PRE 4
#define V_GATE 0
#define TOUCH_EARLY 0
#define W2_DELAY 0
#define WO_PRE0 2
#define ALLSYNC 0
#define MPC_N 14
#define MPC_DELAY 0
#define LNAP 0
#define WS_TAIL 0
#define WS_SHIFT(n) (WS_TAIL ? (((n) - (size_t)(WS_TAIL * (DIAG ? 9 : 4) << 20)) & ~(size_t)4095) : (size_t)0)
#define MP_FIRST 0
#define C3HEAD 1
#define MORD 2
#define HOPS 1

#define LAS __attribute__((address_space(3)))
typedef unsigned u32;
typedef unsigned long long u64;
typedef unsigned v4u __attribute__((ext_vector_type(4)));
typedef unsigned v2u __attribute__((ext_vector_type(2)));
typedef float f32x4 __attribute__((ext_vector_type(4)));
typedef float f32x2 __attribute__((ext_vector_type(2)));
typedef short s16x2 __attribute__((ext_vector_type(2)));
typedef short s16x4 __attribute__((ext_vector_type(4)));
typedef short s16x8 __attribute__((ext_vector_type(8)));
typedef __bf16 bf16x2 __attribute__((ext_vector_type(2)));
typedef __bf16 bf16x8 __attribute__((ext_vector_type(8)));
typedef __amdgpu_buffer_rsrc_t rsrc_t;

namespace {

constexpr int NWG = 256, NTHR = 512;
constexpr u32 SENT = 0xFFFFFFFFu;
constexpr int AUX_SC1 = 16;
constexpr int SPIN_CAP = 20000;
constexpr float LOG2E = 1.4426950408889634f;

constexpr int WS_EPOCH = 0;
constexpr int WS_LSE = 4096;
constexpr int WS_FLAG = 49152;
constexpr int WS_H = 32768;
constexpr int WS_QH = 0xA0000;
constexpr int WS_QG = 65536;
constexpr int WS_OP = 1 << 20;
constexpr int WS_WP = WS_OP + 524288;
constexpr int WS_MP = WS_WP + 524288;
constexpr int MPJ = 112 * 128;
constexpr int WS_END = WS_MP + 128 * MPJ;
constexpr int WS_STAMP = 8 << 20;

constexpr int L_R0 = 0, L_R1 = 65536, L_MISC = 131072;
constexpr int L_ACT = L_MISC;
constexpr int L_P = L_ACT + 8 * 1056;
constexpr int L_CS = L_P + 4736;
constexpr int L_ST = L_CS + 640;
constexpr int L_FL = L_ST + 512;
constexpr int F_KR = 0, F_KD = 16, F_Q = 32, F_P = 48, F_V = 64, F_O = 80;
constexpr int L_BYTES = L_ST + 2048;

__device__ __forceinline__ float bflo(u32 v) { return __uint_as_float(v << 16); }
__device__ __forceinline__ float bfhi(u32 v) { return __uint_as_float(v & 0xFFFF0000u); }
__device__ __forceinline__ u32 pk2(float lo, float hi) {
  bf16x2 r = {(__bf16)lo, (__bf16)hi};
  return __builtin_bit_cast(u32, r);
}
__device__ __forceinline__ float rbf(float x) { return (float)(__bf16)x; }

template <int CTRL> __device__ __forceinline__ float dpp(float x) {
  return __builtin_bit_cast(float, __builtin_amdgcn_mov_dpp(__builtin_bit_cast(int, x), CTRL, 0xf, 0xf, true));
}
constexpr int DPP_X1 = 0xB1, DPP_X2 = 0x4E, DPP_HM = 0x141, DPP_M = 0x140, DPP_R8 = 0x128, DPP_ID = 0xE4;
__device__ __forceinline__ float sum16_32(float x) {
  auto s = __builtin_amdgcn_permlane16_swap(__float_as_uint(x), __float_as_uint(x), false, false);
  x = __uint_as_float(s[0]) + __uint_as_float(s[1]);
  auto t = __builtin_amdgcn_permlane32_swap(__float_as_uint(x), __float_as_uint(x), false, false);
  return __uint_as_float(t[0]) + __uint_as_float(t[1]);
}
__device__ __forceinline__ float max16_32(float x) {
  auto s = __builtin_amdgcn_permlane16_swap(__float_as_uint(x), __float_as_uint(x), false, false);
  x = fmaxf(__uint_as_float(s[0]), __uint_as_float(s[1]));
  auto t = __builtin_amdgcn_permlane32_swap(__float_as_uint(x), __float_as_uint(x), false, false);
  return fmaxf(__uint_as_float(t[0]), __uint_as_float(t[1]));
}
__device__ __forceinline__ float wave_sum(float x) {
  x += dpp<DPP_X1>(x); x += dpp<DPP_X2>(x); x += dpp<DPP_HM>(x); x += dpp<DPP_M>(x);
  return sum16_32(x);
}
__device__ __forceinline__ f32x4 mfma(s16x8 a, s16x8 b, f32x4 c) {
  return __builtin_amdgcn_mfma_f32_16x16x32_bf16(__builtin_bit_cast(bf16x8, a), __builtin_bit_cast(bf16x8, b), c, 0, 0, 0);
}
__device__ __forceinline__ f32x4 mfma(v4u a, v4u b, f32x4 c) {
  return __builtin_amdgcn_mfma_f32_16x16x32_bf16(__builtin_bit_cast(bf16x8, a), __builtin_bit_cast(bf16x8, b), c, 0, 0, 0);
}
__device__ __forceinline__ s16x8 trfrag(LAS char* p, int half_off) {
  typedef short v4i16_t __attribute__((ext_vector_type(4)));
  s16x4 lo = __builtin_amdgcn_ds_read_tr16_b64_v4i16((LAS v4i16_t*)p);
  s16x4 hi = __builtin_amdgcn_ds_read_tr16_b64_v4i16((LAS v4i16_t*)(p + half_off));
  return __builtin_shufflevector(lo, hi, 0, 1, 2, 3, 4, 5, 6, 7);
}
#define DMA4_ASM(POL) \
  asm volatile("s_mov_b32 m0, %6\n\ts_nop 0\n\t" \
               "buffer_load_dwordx4 %1, %5, %0 offen " POL " lds\n\ts_add_u32 %0, %0, %7\n\t" \
               "buffer_load_dwordx4 %2, %5, %0 offen offset:1024 " POL " lds\n\ts_add_u32 %0, %0, %7\n\t" \
               "buffer_load_dwordx4 %3, %5, %0 offen offset:2048 " POL " lds\n\ts_add_u32 %0, %0, %7\n\t" \
               "buffer_load_dwordx4 %4, %5, %0 offen offset:3072 " POL " lds" \
               : "+s"(soff) : "v"(v0), "v"(v1), "v"(v2), "v"(v3), "s"(rs), "s"(lds), "s"(sdelta) : "memory", "scc")
template <int NT = 1>
__device__ __forceinline__ u32 dma4(rsrc_t rs, u32 v0, u32 v1, u32 v2, u32 v3, u32 soff, u32 sdelta, u32 lds) {
  if constexpr (NT) DMA4_ASM(""); else DMA4_ASM("");
  return soff;
}
__device__ __forceinline__ void dma4c(rsrc_t rs, u32 v0, u32 v1, u32 v2, u32 v3, u32 soff, u32 lds) {
  asm volatile("s_mov_b32 m0, %6\n\ts_nop 0\n\t"
               "buffer_load_dwordx4 %1, %5, %0 offen lds\n\t"
               "buffer_load_dwordx4 %2, %5, %0 offen offset:1024 lds\n\t"
               "buffer_load_dwordx4 %3, %5, %0 offen offset:2048 lds\n\t"
               "buffer_load_dwordx4 %4, %5, %0 offen offset:3072 lds"
               :: "s"(soff), "v"(v0), "v"(v1), "v"(v2), "v"(v3), "s"(rs), "s"(lds) : "memory");
}
template <int NT = 1>
__device__ __forceinline__ void touch(rsrc_t rs, u32 voff, u32 soff, u32 lds) {
  if constexpr (NT) asm volatile("s_mov_b32 m0, %0\n\ts_nop 0\n\tbuffer_load_dword %1, %2, %3 offen lds" ::"s"(lds), "v"(voff), "s"(rs), "s"(soff) : "memory");
  else asm volatile("s_mov_b32 m0, %0\n\ts_nop 0\n\tbuffer_load_dword %1, %2, %3 offen lds" ::"s"(lds), "v"(voff), "s"(rs), "s"(soff) : "memory");
}
#define LOAD_TAG() do { u64 e_; asm volatile("s_load_dwordx2 %0, %1, 0x0\n\ts_waitcnt lgkmcnt(0)" : "=s"(e_) : "s"(ws) : "memory"); tagLo = (u32)e_ + 1u; tagHi = (u32)(e_ >> 32) + 1u; } while (0)
#define PAD(n, nx) asm volatile("Lpad" #n ":\n\ts_cbranch_execz Lpad" #nx)
#define HOP(first, ret) asm volatile("s_mov_b64 exec, 0\n\ts_branch Lpad" #first "\nLpad" #ret ":\n\ts_mov_b64 exec, -1")
#define LSET(off) do { asm volatile("s_waitcnt lgkmcnt(0)" ::: "memory"); *(volatile LAS u32*)(L + L_FL + (off)) = 1u; } while (0)
#define LWAIT1(off) do { _Pragma("nounroll") for (int it_ = 0; it_ < 200000; ++it_) { \
    if (__builtin_amdgcn_readfirstlane(*(volatile LAS u32*)(L + L_FL + (off))) != 0u) break; nap<LNAP>(); } asm volatile("" ::: "memory"); } while (0)
#define LWAIT4(off) do { _Pragma("nounroll") for (int it_ = 0; it_ < 200000; ++it_) { const v4u f_ = *(volatile LAS v4u*)(L + L_FL + (off)); \
    if (__builtin_amdgcn_readfirstlane(f_.x & f_.y & f_.z & f_.w) != 0u) break; nap<LNAP>(); } asm volatile("" ::: "memory"); } while (0)
#define LWAIT8(off) do { _Pragma("nounroll") for (int it_ = 0; it_ < 200000; ++it_) { \
    const v4u f_ = *(volatile LAS v4u*)(L + L_FL + (off)), g_ = *(volatile LAS v4u*)(L + L_FL + (off) + 16); \
    if (__builtin_amdgcn_readfirstlane((f_.x & f_.y & f_.z & f_.w) & (g_.x & g_.y & g_.z & g_.w)) != 0u) break; nap<LNAP>(); } asm volatile("" ::: "memory"); } while (0)
__device__ __forceinline__ void hint_wait(const char* p, u32 tag, u32 ticks) {
  u64 v = 0;
#pragma nounroll
  for (int it = 0; it < 64; ++it) {
    asm volatile("s_load_dwordx2 %0, %1, 0x0 glc\n\ts_waitcnt lgkmcnt(0)" : "=s"(v) : "s"(p) : "memory");
    if ((u32)v == tag) break;
  }
#pragma nounroll
  for (int it = 0; it < 64; ++it) { if ((u32)__builtin_amdgcn_s_memrealtime() - (u32)(v >> 32) >= ticks) break; }
}
typedef unsigned s8u __attribute__((ext_vector_type(8)));
__device__ __forceinline__ void hint_wait8(const char* p, u32 tag) {
#pragma nounroll
  for (int it = 0; it < 64; ++it) {
    s8u v;
    asm volatile("s_load_dwordx8 %0, %1, 0x0 glc\n\ts_waitcnt lgkmcnt(0)" : "=s"(v) : "s"(p) : "memory");
    if ((((v[0] ^ tag) | (v[1] ^ tag)) | ((v[2] ^ tag) | (v[3] ^ tag)) | ((v[4] ^ tag) | (v[5] ^ tag)) | ((v[6] ^ tag) | (v[7] ^ tag))) == 0u) break;
  }
}
__device__ __forceinline__ u32 max4(v4u v) { return max(max(v.x, v.y), max(v.z, v.w)); }
#define POLL_LOOP _Pragma("nounroll") for (int it = 0; it < SPIN_CAP; ++it)
#define RELOAD() asm volatile("" ::: "memory")
#define BARRIER() asm volatile("s_waitcnt lgkmcnt(0)\n\ts_barrier" ::: "memory")
#define VMWAIT(n) asm volatile("s_waitcnt vmcnt(" #n ")" ::: "memory")
#define FENCE() __builtin_amdgcn_sched_barrier(0)
#define PIN(x) asm volatile("" : "+v"(x))
template <int N> __device__ __forceinline__ void nap() {
  if constexpr (N > 0) { __builtin_amdgcn_s_sleep(N > 127 ? 127 : N); nap<(N > 127 ? N - 127 : 0)>(); }
}
__device__ __forceinline__ v4u ld16(rsrc_t rs, u32 off) { return __builtin_amdgcn_raw_buffer_load_b128(rs, off, 0, AUX_SC1); }
__device__ __forceinline__ u32 ld4(rsrc_t rs, u32 off) { return __builtin_amdgcn_raw_buffer_load_b32(rs, off, 0, AUX_SC1); }
__device__ __forceinline__ void st16(rsrc_t rs, u32 off, v4u v) { __builtin_amdgcn_raw_buffer_store_b128(v, rs, off, 0, AUX_SC1); }
__device__ __forceinline__ bool wave_clean(float v) { return __builtin_amdgcn_ballot_w64(v != v) == 0ull; }
__device__ __forceinline__ rsrc_t mkrs(const void* p, int bytes) { return __builtin_amdgcn_make_buffer_rsrc((void*)p, 0, bytes, 0x00020000); }

__device__ __forceinline__ float dot2(u32 a, u32 b, float c) {
  return __builtin_amdgcn_fdot2_f32_bf16(__builtin_bit_cast(bf16x2, a), __builtin_bit_cast(bf16x2, b), c, false);
}
__device__ __forceinline__ v4u ln_half(const v4u c, const v4u o) {
  float s = 0.f, q = 0.f;
#pragma unroll
  for (int i = 0; i < 4; ++i) { s = dot2(c[i], 0x3F803F80u, s); q = dot2(c[i], c[i], q); s = dot2(o[i], 0x3F803F80u, s); q = dot2(o[i], o[i], q); }
  const float mean = wave_sum(s) * (1.f / 1024.f);
  const float var = fmaxf(fmaf(-mean, mean, wave_sum(q) * (1.f / 1024.f)), 0.f);
  const u32 mv = pk2(mean, var);
  const float mb = bflo(mv);
  const float den = rbf(__builtin_amdgcn_sqrtf(rbf(bfhi(mv) + 1e-5f)));
  const float inv = __builtin_amdgcn_rcpf(den);
  v4u r;
#pragma unroll
  for (int i = 0; i < 4; ++i) {
    const u32 d = pk2(bflo(c[i]) - mb, bfhi(c[i]) - mb);
    const float d0 = bflo(d), d1 = bfhi(d);
    float q0 = d0 * inv, q1 = d1 * inv;
    q0 = fmaf(fmaf(-q0, den, d0), inv, q0);
    q1 = fmaf(fmaf(-q1, den, d1), inv, q1);
    r[i] = pk2(q0, q1);
  }
  return r;
}

#define STAMP(i) do {} while (0)
#define STAMP_DECL() do {} while (0)
#define STAMP_FLUSH() do {} while (0)

}

__global__ void __launch_bounds__(NTHR, 2)
fused_decode_layer(const char* x, const char* kc, const char* vc, const char* wq, const char* wo, const char* w1, const char* w2, char* out, char* ws) {
  extern __shared__ __attribute__((aligned(16))) char smem[];
  STAMP_DECL();
  STAMP(0);
  {
    u32 t0, t1, t2, t3, t4;
    asm volatile(
        "s_load_dwordx8 s[64:71], %[ka], 0x0\n\t"
        "s_load_dwordx2 s[80:81], %[ka], 0x40\n\t"
        "s_getpc_b64 s[82:83]\n\t"
        "v_readfirstlane_b32 s72, %[tid]\n\t"
        "v_and_b32 %[t4], 63, %[tid]\n\t"
        "s_mov_b32 s79, 0x20000\n\t"
        "s_mov_b32 s78, 0x800000\n\t"
        "s_lshr_b32 s72, s72, 6\n\t"
        "s_and_b32 s73, s72, 3\n\t"
        "v_lshrrev_b32 %[t1], 4, %[t4]\n\t"
        "s_bfe_u32 s84, %[c], 0x20003\n\t"
        "s_bfe_u32 s74, %[c], 0x10006\n\t"
        "s_lshl1_add_u32 s84, s84, s74\n\t"
        "s_bfe_u32 s85, %[c], 0x10005\n\t"
        "s_lshl2_add_u32 s85, s85, s73\n\t"
        "s_and_b32 s74, %[c], 0x80\n\t"
        "s_cmp_gt_u32 s72, 3\n\t"
        "s_cbranch_scc1 Lpro_hi\n\t"
        "s_cmp_eq_u32 s74, 0\n\t"
        "s_cbranch_scc1 Lpro_bl\n\t"
        "s_cmp_lg_u32 s72, 3\n\t"
        "s_cbranch_scc1 Lpro_al0\n\t"
        "s_bfe_u32 s74, %[c], 0x40003\n\t"
        "s_mul_i32 s74, s74, %[cl]*%[cs]\n\t"
        "s_and_b32 s82, s82, 0xffffff00\n\t"
        "s_add_u32 s82, s82, s74\n\t"
        "s_addc_u32 s83, s83, 0\n\t"
        ".irp o,0,1,2,3,4,5,6,7,8,9,10,11,12,13,14,15\n\t"
        ".if \\o < %[cl]\n\t"
        "s_load_dword s75, s[82:83], \\o*%[cs]\n\t"
        ".endif\n\t"
        ".endr\n\t"
        "s_branch Lpro_end\n\t"
        "Lpro_al0:\n\t"
        "v_mov_b32 v4, -1\n\t"
        "v_mov_b32 v5, -1\n\t"
        "v_mov_b32 v6, -1\n\t"
        "v_mov_b32 v7, -1\n\t"
        "v_lshlrev_b32 %[t2], 4, %[t4]\n\t"
        "s_mul_i32 s82, %[c], %[mpj]\n\t"
        "s_add_u32 s82, s82, %[mpb]\n\t"
        "s_mul_i32 s75, s73, 5120\n\t"
        "s_add_u32 s75, s75, s82\n\t"
        "s_add_u32 s74, s75, 4096\n\t"
        "s_add_u32 s82, s82, %[mpj]\n\t"
        "s_mov_b32 s83, s79\n\t"
        "s_waitcnt lgkmcnt(0)\n\t"
        "s_and_b32 s81, s81, 0xffff\n\t"
        ".if %[mpf]\n\t"
        "buffer_store_dwordx4 v[4:7], %[t2], s[80:83], s75 offen sc1\n\t"
        "buffer_store_dwordx4 v[4:7], %[t2], s[80:83], s75 offen offset:1024 sc1\n\t"
        "buffer_store_dwordx4 v[4:7], %[t2], s[80:83], s75 offen offset:2048 sc1\n\t"
        "buffer_store_dwordx4 v[4:7], %[t2], s[80:83], s75 offen offset:3072 sc1\n\t"
        "buffer_store_dwordx4 v[4:7], %[t2], s[80:83], s74 offen sc1\n\t"
        ".else\n\t"
        "s_mov_b32 s86, s75\n\t"
        "s_mov_b32 s87, s74\n\t"
        ".endif\n\t"
        "Lpro_al:\n\t"
        "s_lshl1_add_u32 s74, s73, s84\n\t"
        "s_and_b32 s74, s74, 7\n\t"
        "s_add_u32 s75, s74, %[c]\n\t"
        "s_and_b32 s75, s75, 7\n\t"
        "s_lshl3_add_u32 s75, s74, s75\n\t"
        "s_lshl_b32 s75, s75, 7\n\t"
        "s_lshl1_add_u32 s74, s85, 1\n\t"
        "s_lshl_b32 s74, s74, 19\n\t"
        "s_or_b32 s74, s74, s75\n\t"
        "v_lshlrev_b32 %[t0], 13, %[t4]\n\t"
        "s_mov_b32 m0, %[junk]\n\t"
        "s_mov_b32 s78, %[wqb]\n\t"
        "s_waitcnt lgkmcnt(0)\n\t"
        "s_mov_b32 s76, s70\n\t"
        "s_and_b32 s77, s71, 0xffff\n\t"
        "buffer_load_dword %[t0], s[76:79], s74 offen lds\n\t"
        "s_add_u32 s85, s85, 1\n\t"
        "s_add_u32 s73, s73, 1\n\t"
        "s_cmp_eq_u32 s73, 3\n\t"
        "s_cbranch_scc1 Lpro_al\n\t"
        ".if !%[mpf]\n\t"
        "buffer_store_dwordx4 v[4:7], %[t2], s[80:83], s86 offen sc1\n\t"
        "buffer_store_dwordx4 v[4:7], %[t2], s[80:83], s86 offen offset:1024 sc1\n\t"
        "buffer_store_dwordx4 v[4:7], %[t2], s[80:83], s86 offen offset:2048 sc1\n\t"
        "buffer_store_dwordx4 v[4:7], %[t2], s[80:83], s86 offen offset:3072 sc1\n\t"
        "buffer_store_dwordx4 v[4:7], %[t2], s[80:83], s87 offen sc1\n\t"
        ".endif\n\t"
        "s_branch Lpro_end\n\t"
        "Lpro_hi:\n\t"
        "s_lshl_b32 m0, s73, 14\n\t"
        "s_cmp_lg_u32 s74, 0\n\t"
        "s_cbranch_scc1 Lpro_ah\n\t"
        "v_and_b32 %[t1], 1, %[t1]\n\t"
        "v_lshlrev_b32 %[t1], 1, %[t1]\n\t"
        "v_and_b32 %[t0], 7, %[t4]\n\t"
        "v_xor_b32 %[t0], %[t0], %[t1]\n\t"
        "v_lshrrev_b32 %[t2], 3, %[t4]\n\t"
        "v_lshlrev_b32 %[t2], 13, %[t2]\n\t"
        "v_lshl_or_b32 %[t0], %[t0], 4, %[t2]\n\t"
        "v_xor_b32 %[t1], 64, %[t0]\n\t"
        "s_add_u32 s75, s84, %[c]\n\t"
        "s_and_b32 s75, s75, 7\n\t"
        "s_lshl3_add_u32 s75, s84, s75\n\t"
        "s_lshl_b32 s75, s75, 7\n\t"
        "s_lshl_b32 s74, s85, 20\n\t"
        "s_or_b32 s74, s74, s75\n\t"
        "s_mov_b32 s75, 0xfc00\n\t"
        "s_mov_b32 s73, 4\n\t"
        "s_waitcnt lgkmcnt(0)\n\t"
        "s_mov_b32 s76, s70\n\t"
        "s_and_b32 s77, s71, 0xffff\n\t"
        "Lpro_bh_loop:\n\t"
        "buffer_load_dwordx4 %[t0], s[76:79], s74 offen lds\n\t"
        "s_add_u32 s74, s74, s75\n\t"
        "buffer_load_dwordx4 %[t1], s[76:79], s74 offen offset:1024 lds\n\t"
        "s_add_u32 s74, s74, s75\n\t"
        "buffer_load_dwordx4 %[t0], s[76:79], s74 offen offset:2048 lds\n\t"
        "s_add_u32 s74, s74, s75\n\t"
        "buffer_load_dwordx4 %[t1], s[76:79], s74 offen offset:3072 lds\n\t"
        "s_add_u32 m0, m0, 0x1000\n\t"
        "s_add_u32 s74, s74, 0x10c00\n\t"
        "s_sub_u32 s73, s73, 1\n\t"
        "s_cmp_lg_u32 s73, 0\n\t"
        "s_cbranch_scc1 Lpro_bh_loop\n\t"
        "s_branch Lpro_end\n\t"
        "Lpro_bl:\n\t"
        "v_lshlrev_b32 %[t0], 4, %[t4]\n\t"
        "v_and_b32 %[t1], 31, %[t4]\n\t"
        "v_lshlrev_b32 %[t1], 13, %[t1]\n\t"
        "s_lshl_b32 s74, s73, 12\n\t"
        "s_add_u32 m0, s74, %[r1]\n\t"
        "s_waitcnt lgkmcnt(0)\n\t"
        "s_and_b32 s65, s65, 0xffff\n\t"
        "s_movk_i32 s66, 0x4000\n\t"
        "s_mov_b32 s67, s79\n\t"
        "buffer_load_dwordx4 %[t0], s[64:67], s74 offen lds\n\t"
        "buffer_load_dwordx4 %[t0], s[64:67], s74 offen offset:1024 lds\n\t"
        "buffer_load_dwordx4 %[t0], s[64:67], s74 offen offset:2048 lds\n\t"
        "buffer_load_dwordx4 %[t0], s[64:67], s74 offen offset:3072 lds\n\t"
        "s_cmp_lg_u32 s72, %[c3w]\n\t"
        "s_cbranch_scc1 Lpro_end\n\t"
        "s_lshr_b32 s74, s70, 7\n\t"
        "s_sub_u32 s74, 3, s74\n\t"
        "s_and_b32 s74, s74, 7\n\t"
        "s_sub_u32 s75, s74, %[c]\n\t"
        "s_and_b32 s75, s75, 7\n\t"
        "s_lshl3_add_u32 s75, s75, s74\n\t"
        "s_lshl_b32 s75, s75, 7\n\t"
        "s_bfe_u32 s82, %[c], 0x30003\n\t"
        "s_bfe_u32 s83, %[c], 0x10006\n\t"
        "s_lshl2_add_u32 s82, s82, s83\n\t"
        "s_lshl_b32 s82, s82, 18\n\t"
        "s_or_b32 s74, s82, s75\n\t"
        "s_mov_b32 m0, %[junk]\n\t"
        "s_mov_b32 s76, s70\n\t"
        "s_and_b32 s77, s71, 0xffff\n\t"
        "buffer_load_dword %[t1], s[76:79], s74 offen lds\n\t"
        "s_branch Lpro_end\n\t"
        "Lpro_ah:\n\t"
        "v_and_b32 %[t4], 15, %[t4]\n\t"
        "v_xor_b32 %[t0], %[t4], %[t1]\n\t"
        "v_lshlrev_b32 %[t1], 8, %[t1]\n\t"
        "v_lshl_or_b32 %[t0], %[t0], 4, %[t1]\n\t"
        "v_xor_b32 %[t1], 64, %[t0]\n\t"
        "v_xor_b32 %[t2], 0x80, %[t0]\n\t"
        "v_xor_b32 %[t3], 0x80, %[t1]\n\t"
        "s_bfe_u32 s74, %[c], 0x20005\n\t"
        "s_and_b32 s75, %[c], 7\n\t"
        "s_lshl3_add_u32 s74, s74, s75\n\t"
        "s_bfe_u32 s75, %[c], 0x20003\n\t"
        "s_lshl4_add_u32 s74, s74, s75\n\t"
        "s_or_b32 s74, s74, 12\n\t"
        "s_lshl2_add_u32 s74, s74, s73\n\t"
        "s_lshl_b32 s74, s74, 14\n\t"
        "s_brev_b32 s78, 64\n\t"
        "s_mov_b32 s73, 4\n\t"
        "s_waitcnt lgkmcnt(0)\n\t"
        "s_sleep %[dly]\n\t"
        "s_mov_b32 s76, s66\n\t"
        "s_and_b32 s77, s67, 0xffff\n\t"
        "Lpro_k_loop:\n\t"
        "buffer_load_dwordx4 %[t0], s[76:79], s74 offen lds\n\t"
        "buffer_load_dwordx4 %[t1], s[76:79], s74 offen offset:1024 lds\n\t"
        "buffer_load_dwordx4 %[t2], s[76:79], s74 offen offset:2048 lds\n\t"
        "buffer_load_dwordx4 %[t3], s[76:79], s74 offen offset:3072 lds\n\t"
        "s_add_u32 m0, m0, 0x1000\n\t"
        "s_addk_i32 s74, 0x1000\n\t"
        "s_sub_u32 s73, s73, 1\n\t"
        "s_cmp_lg_u32 s73, 0\n\t"
        "s_cbranch_scc1 Lpro_k_loop\n\t"
        "Lpro_end:\n\t"
        ".pushsection .text,1\n\t"
        ".fill 512, 4, 0xBF800000\n\t"
        ".popsection\n\t"
        "s_waitcnt lgkmcnt(0)"
        : [t0] "=&v"(t0), [t1] "=&v"(t1), [t2] "=&v"(t2), [t3] "=&v"(t3), [t4] "=&v"(t4)
        : [ka] "s"(__builtin_amdgcn_kernarg_segment_ptr()), [tid] "v"(threadIdx.x), [c] "s"(blockIdx.x), [cl] "i"(CODE_LINES), [cs] "i"(CODE_STRIDE),
          [junk] "i"(L_ST + 1024), [r1] "i"(L_R1), [dly] "i"(A_START_DELAY),
          [mpj] "i"(MPJ), [mpb] "i"(WS_MP - 128 * MPJ), [mpf] "i"(MP_FIRST), [c3w] "i"(C3FIX ? 0 : 9), [wqb] "i"(WQ_TOUCH ? 0x800000 : 0)
        : "s64", "s65", "s66", "s67", "s68", "s69", "s70", "s71", "s72", "s73", "s74", "s75", "s76", "s77", "s78", "s79", "s80", "s81",
          "s82", "s83", "s84", "s85", "s86", "s87", "v4", "v5", "v6", "v7", "scc", "memory");
  }
  STAMP(1);
  LAS char* L = (LAS char*)smem;
  constexpr u32 lbase = 0;
  const int tid = threadIdx.x, l = tid & 63, w = __builtin_amdgcn_readfirstlane(tid >> 6), c = blockIdx.x;
  const int g = l >> 4, xcd = c & 7, sl = (c >> 3) & 15, u = w & 3;
  const bool hiw = w >= 4;
  const u32 vv = g * 256 + (((((l & 15) >> 1) ^ g) << 1) | (l & 1)) * 16;
  const int band = 2 * (sl & 3) + (sl >> 3), cg = 8 * band + ((xcd + band) & 7), kh = (sl >> 2) & 1;
  const int bk = (sl >> 2) * 8 + xcd;
  const u32 kvb = (bk * 4096 + 3072 + 256 * (sl & 3)) * 256;
  const int q4 = (l >> 2) & 3, p4 = l & 3, b8 = l & 7, d8 = (l >> 3) & 1;
  const int fsw = q4 | ((g & 1) << 2);
  const rsrc_t rsW = mkrs(ws, WS_END);
  const v4u sent4 = {SENT, SENT, SENT, SENT};
  const f32x4 zero4 = {0.f, 0.f, 0.f, 0.f};
  u32 tagLo = 0, tagHi = 0;

  const u32 fo = WS_FLAG + (16 * w + (l & 15)) * 128;
  if (c < 128) {
    if (!hiw) {
      LAS char* px = L + L_R1 + w * 4096 + l * 16;
      if (C3FIX && w == 0) VMWAIT(1); else VMWAIT(0);
      STAMP(13);
      const v4u xc0 = *(LAS v4u*)(px + kh * 1024), xo0 = *(LAS v4u*)(px + (kh ^ 1) * 1024);
      const v4u xc1 = *(LAS v4u*)(px + 2048 + kh * 1024), xo1 = *(LAS v4u*)(px + 2048 + (kh ^ 1) * 1024);
      *(LAS v4u*)(L + L_ACT + (2 * w) * 1056 + l * 16) = ln_half(xc0, xo0);
      *(LAS v4u*)(L + L_ACT + (2 * w + 1) * 1056 + l * 16) = ln_half(xc1, xo1);
      STAMP(12);
      if (w == 0) HOP(B0, BR);
    }
    LOAD_TAG();
    PAD(B0, B1);
    BARRIER();
    const rsrc_t rv = mkrs(vc, 8 * 4 * 4096 * 128 * 2);
    if (w == 7) {
#pragma unroll
      for (int i = 0; i < 4; ++i) st16(rsW, WS_WP + c * 4096 + i * 1024 + l * 16, sent4);
      st16(rsW, l < 8 ? WS_H + c * 128 + l * 16 : WS_END, sent4);
    }
    if (w < TOUCH_EARLY) { nap<2>(); touch(rv, g * 16384 + (l & 15) * 128, kvb + (2 * V_OWN + w) * 2048, lbase + L_ST + 1024); }
    const int bq = 2 * u + (l >> 5), cp = l & 31;
    LAS char* pq = L + L_P + bq * 272 + cp * 8;
    u32 qo = WS_QG + (((bq * 4 + (cg >> 4)) * 2 + kh) * 1024 + 64 * (cg & 15) + 2 * cp) * 8;
    {
      const int kh2 = w >> 2;
      s16x8 bf[8];
      LAS char* pb = L + L_ACT + b8 * 1056 + kh2 * 512 + 16 * g;
#pragma unroll
      for (int ks = 0; ks < 8; ++ks) bf[ks] = *(LAS s16x8*)(pb + ks * 64);
      LAS char* pa = L + L_R0 + kh2 * 32768 + (8 * g + q4) * 128 + ((u ^ (((q4 >> 1) & 1) | ((g & 1) << 1))) << 5) + 8 * p4;
      LAS char* pw = L + L_P + (kh2 * 8 + b8) * 272 + (16 * u + 4 * g) * 4;
      PIN(pa); PIN(pw); PIN(pq); PIN(qo);
      if (w == 7) VMWAIT(5); else if (hiw) VMWAIT(0);
      STAMP(2);
      BARRIER();
      PAD(B1, B2);
      if (w == 6 && l < 8) __builtin_amdgcn_raw_buffer_store_b32(tagLo, rsW, WS_QH + (l * 16 + (cg >> 2)) * 128 + ((cg & 3) * 2 + kh) * 4, 0, AUX_SC1);
      s16x8 af[8];
#pragma unroll
      for (int ks = 0; ks < 8; ++ks) af[ks] = trfrag(pa + ks * 4096, 512);
      FENCE();
      f32x4 acc = zero4;
#pragma unroll
      for (int ks = 0; ks < 8; ++ks) acc = mfma(af[ks], bf[ks], acc);
      if (!d8) *(LAS f32x4*)pw = acc;
    }
    BARRIER();
    const rsrc_t r1 = mkrs(w1, 1024 * 3584 * 2);
    const int lc = (3 - (int)(((u32)(size_t)w1) >> 7) - 7 * xcd) & 7;
    const bool slow = C3HEAD && lc < 7 && (sl >> 1) == lc;
    if (!hiw) {
      PAD(B2, B6);
      const f32x2 s = *(LAS f32x2*)pq + *(LAS f32x2*)(pq + 8 * 272);
      st16(rsW, qo, v4u{__float_as_uint(s[0]), tagLo, __float_as_uint(s[1]), tagHi});
      STAMP(3);
      if (!slow) nap<M_DELAY>();
    } else if (w == 4 && !slow) {
      nap<TOUCH_DELAY>();
#pragma unroll
      for (int i = 2 * V_OWN + TOUCH_EARLY; i < NTOUCH_V; ++i) touch(rv, g * 16384 + (l & 15) * 128, kvb + i * 2048, lbase + L_ST + 1024);
    } else if (w == 5 && C3HEAD && lc < 7 && sl >= 14) {
      nap<TOUCH_DELAY>();
      const int s2 = 2 * lc + (sl & 1);
      const u32 kv2 = (((s2 >> 2) * 8 + xcd) * 4096 + 3072 + 256 * (s2 & 3)) * 256;
#pragma unroll
      for (int i = 2 * V_OWN + TOUCH_EARLY; i < NTOUCH_V; ++i) touch(rv, g * 16384 + (l & 15) * 128, kv2 + i * 2048, lbase + L_ST + 1024);
    } else if (w == 7) {
      VMWAIT(0);
      if (l == 0) __builtin_amdgcn_raw_buffer_store_b64(v2u{tagLo, tagHi}, rsW, WS_FLAG + c * 128, 0, AUX_SC1);
      STAMP(3);
    }
    if (sl >= 14) { STAMP_FLUSH(); return; }
    BARRIER();
    const int m = xcd * 14 + sl;
    PAD(B6, BR);
    STAMP(4);
    {
      const rsrc_t r2 = mkrs(w2, 3584 * 1024 * 2);
      const u32 v2o = vv + g * (2048 - 256);
      const u32 v1 = (l >> 2) * 7168 + ((((l >> 1) & 1) ^ ((l >> 4) & 1)) << 5) + (l & 1) * 16;
      const int h0 = 64 * w, h1 = h0 + 512;
      dma4(r1, v1, v1, v1, v1, h0 * 7168 + 64 * m, 16 * 7168 - 1024, lbase + L_R1 + h0 * 64);
      if (MORD == 0) dma4(r2, v2o, v2o, v2o ^ 128, v2o ^ 128, 32 * m * 2048 + 256 * w, 4 * 2048 - 1024, lbase + L_R0 + 8 * w * 1024);
      if (MORD == 2) BARRIER();
      STAMP(5);
      dma4(r1, v1, v1, v1, v1, h1 * 7168 + 64 * m, 16 * 7168 - 1024, lbase + L_R1 + h1 * 64);
      if (MORD >= 1) { BARRIER(); STAMP(6); dma4(r2, v2o, v2o, v2o ^ 128, v2o ^ 128, 32 * m * 2048 + 256 * w, 4 * 2048 - 1024, lbase + L_R0 + 8 * w * 1024); }
      dma4(r2, v2o, v2o, v2o ^ 128, v2o ^ 128, (32 * m + 16) * 2048 + 256 * w, 4 * 2048 - 1024, lbase + L_R0 + (8 * w + 4) * 1024);
    }
    STAMP(7);
    if (w == 0) HOP(B7, BS);
    PAD(B7, B8);
    const v4u ones = {0x3F803F80u, 0x3F803F80u, 0x3F803F80u, 0x3F803F80u};
    s16x8 f2[8];
    LAS char* pz = L + L_P + (u * 8 + b8) * 144 + (16 * d8 + 4 * g) * 4;
    LAS char* pr = L + L_P + b8 * 144 + g * 32 + d8 * 16;
    LAS char* pc = L + L_CS + g * 32 + d8 * 16;
    LAS char* pa1 = L + L_R1 + (256 * u + 4 * g + q4) * 64 + ((g & 1) << 5) + 8 * p4;
    u32 hb = WS_H + u * 4096 + (4 * d8 + g) * 128 + b8 * 16;
    u32 mo = WS_MP + (4 * (4 * w + d8) + g) * MPJ + m * 128 + b8 * 16;
    LAS char* pst = L + L_ST + b8 * 32 + (u * 2 + d8) * 4;
    LAS char* psr = L + L_ST + d8 * 256 + b8 * 32;
    PIN(hb); PIN(mo); PIN(pst); PIN(psr); PIN(pz); PIN(pr); PIN(pc); PIN(pa1);
    POLL_LOOP {
      RELOAD();
      const v2u fl = __builtin_amdgcn_raw_buffer_load_b64(rsW, fo, 0, AUX_SC1);
      if (__builtin_amdgcn_ballot_w64(((fl.x ^ tagLo) | (fl.y ^ tagHi)) != 0u) == 0ull) break;
    }
    VMWAIT(0);
    STAMP(8);
    PAD(B8, B9);
#pragma unroll
    for (int T = 0; T < 8; ++T) f2[T] = trfrag(L + L_R0 + w * 8192 + (8 * g + q4) * 256 + ((T ^ fsw) << 5) + 8 * p4, 1024);
    BARRIER();
#pragma unroll
    for (int T = 0; T < 8; ++T) PIN(f2[T]);
    PAD(B9, B10);
    if (!hiw) {
      s16x8 f1[2][8];
#pragma unroll
      for (int nt = 0; nt < 2; ++nt)
#pragma unroll
        for (int ks = 0; ks < 8; ++ks) f1[nt][ks] = trfrag((LAS char*)((u32)(size_t)pa1 ^ (32 * nt)) + ks * 2048, 1024);
      hint_wait(ws + WS_FLAG + c * 128 + 64, tagLo, H_DELAY);
      STAMP(11);
      v4u hf[4];
#pragma unroll
      for (int i = 0; i < 4; ++i) hf[i] = ld16(rsW, hb + i * 1024);
      FENCE();
      {
        f32x4 c0 = zero4, c1 = zero4;
#pragma unroll
        for (int ks = 0; ks < 8; ++ks) { c0 = mfma(f1[0][ks], __builtin_bit_cast(s16x8, ones), c0); c1 = mfma(f1[1][ks], __builtin_bit_cast(s16x8, ones), c1); }
        if ((l & 15) == 0) { *(LAS f32x4*)(L + L_CS + u * 144 + 16 * g) = c0; *(LAS f32x4*)(L + L_CS + u * 144 + 64 + 16 * g) = c1; }
      }
      FENCE();
      PAD(B10, B11);
      POLL_LOOP {
        if (__builtin_amdgcn_ballot_w64(max(max(max4(hf[0]), max4(hf[1])), max(max4(hf[2]), max4(hf[3]))) == SENT) == 0ull) break;
        nap<H_NAP>();
        RELOAD();
#pragma unroll
        for (int i = 0; i < 4; ++i) hf[i] = ld16(rsW, hb + i * 1024);
      }
      STAMP(9);
      PAD(B11, B12);
      f32x4 aE[2] = {zero4, zero4}, aO[2] = {zero4, zero4}, aS = zero4, aQ = zero4;
#pragma unroll
      for (int i = 0; i < 4; ++i) { aS = mfma(ones, hf[i], aS); aQ = mfma(hf[i], hf[i], aQ); }
#pragma unroll
      for (int i = 0; i < 4; ++i)
#pragma unroll
        for (int nt = 0; nt < 2; ++nt) {
          aE[nt] = mfma(f1[nt][2 * i], __builtin_bit_cast(s16x8, hf[i]), aE[nt]);
          aO[nt] = mfma(f1[nt][2 * i + 1], __builtin_bit_cast(s16x8, hf[i]), aO[nt]);
        }
      const float sv = (p4 & 2) ? ((p4 & 1) ? aQ[3] : aQ[2]) : ((p4 & 1) ? aQ[1] : aQ[0]);
      if (g == ((l & 15) >> 2)) { *(LAS float*)pst = aS[0]; *(LAS float*)(pst + 256) = sv; }
      f32x4 z;
#pragma unroll
      for (int i = 0; i < 4; ++i) z[i] = (d8 ? aO[1][i] : aE[0][i]) + dpp<DPP_R8>(d8 ? aO[0][i] : aE[1][i]);
      *(LAS f32x4*)pz = z;
    }
    BARRIER();
    PAD(B12, B13);
    {
      const f32x4 t0 = *(LAS f32x4*)psr, t1 = *(LAS f32x4*)(psr + 16);
      const f32x4 z0 = *(LAS f32x4*)pr, z1 = *(LAS f32x4*)(pr + 8 * 144), z2 = *(LAS f32x4*)(pr + 16 * 144), z3 = *(LAS f32x4*)(pr + 24 * 144);
      const f32x4 c0 = *(LAS f32x4*)pc, c1 = *(LAS f32x4*)(pc + 144), c2 = *(LAS f32x4*)(pc + 2 * 144), c3 = *(LAS f32x4*)(pc + 3 * 144);
      FENCE();
      const float mine = (((t0[0] + t0[1]) + (t0[2] + t0[3])) + ((t1[0] + t1[1]) + (t1[2] + t1[3]))) * (1.f / 1024.f);
      const float other = dpp<DPP_R8>(mine);
      const float mean = d8 ? other : mine, ex2 = d8 ? mine : other;
      const float var = fmaxf(fmaf(-mean, mean, ex2), 0.f);
      const u32 mv = pk2(mean, var);
      const float den = rbf(__builtin_amdgcn_sqrtf(rbf(bfhi(mv) + 1e-5f)));
      const float inv = __builtin_amdgcn_rcpf(den);
      const float mb = bflo(mv);
      float gv[4];
#pragma unroll
      for (int i = 0; i < 4; ++i) gv[i] = fmaf(-mb, (c0[i] + c1[i]) + (c2[i] + c3[i]), (z0[i] + z1[i]) + (z2[i] + z3[i])) * inv;
      const s16x2 zz = {0, 0};
      const int g0 = __builtin_bit_cast(int, __builtin_elementwise_max(__builtin_bit_cast(s16x2, pk2(gv[0], gv[1])), zz));
      const int g1 = __builtin_bit_cast(int, __builtin_elementwise_max(__builtin_bit_cast(s16x2, pk2(gv[2], gv[3])), zz));
      PAD(B13, BS);
      v4u B0, B1;
      B0[0] = __builtin_amdgcn_update_dpp(0, g0, DPP_ID, 0xf, 0x3, false);
      B0[1] = __builtin_amdgcn_update_dpp(0, g1, DPP_ID, 0xf, 0x3, false);
      B0[2] = __builtin_amdgcn_update_dpp(0, g0, DPP_R8, 0xf, 0x3, false);
      B0[3] = __builtin_amdgcn_update_dpp(0, g1, DPP_R8, 0xf, 0x3, false);
      B1[0] = __builtin_amdgcn_update_dpp(0, g0, DPP_R8, 0xf, 0xc, false);
      B1[1] = __builtin_amdgcn_update_dpp(0, g1, DPP_R8, 0xf, 0xc, false);
      B1[2] = __builtin_amdgcn_update_dpp(0, g0, DPP_ID, 0xf, 0xc, false);
      B1[3] = __builtin_amdgcn_update_dpp(0, g1, DPP_ID, 0xf, 0xc, false);
#pragma unroll
      for (int i = 0; i < 2; ++i) {
        f32x4 u0 = mfma(f2[4 * i], __builtin_bit_cast(s16x8, B0), zero4), u1 = mfma(f2[4 * i + 1], __builtin_bit_cast(s16x8, B0), zero4);
        u0 = mfma(f2[4 * i + 2], __builtin_bit_cast(s16x8, B1), u0);
        u1 = mfma(f2[4 * i + 3], __builtin_bit_cast(s16x8, B1), u1);
        __builtin_amdgcn_raw_buffer_store_b128(v4u{pk2(u0[0], u0[1]), pk2(u0[2], u0[3]), pk2(u1[0], u1[1]), pk2(u1[2], u1[3])}, rsW, mo, i * 8 * MPJ, AUX_SC1);
      }
    }
    STAMP(10);
    STAMP_FLUSH();
    return;
  }

  const int j = c - 128;
  const int s = sl & 3, b = bk >> 2, kvh = bk & 3;
  const int kvo = sl >> 2, nq = sl & 3, ksl = kvo * 8 + xcd;
  const int jcol = 32 * (j >> 2) + 4 * (j & 3) + 16 * (g & 1);
  const rsrc_t rv = mkrs(vc, 8 * 4 * 4096 * 128 * 2);
  const rsrc_t ro_ = mkrs(wo, 4096 * 1024 * 2);
  const u32 vo = (l >> 5) * 2048 + (((((l & 31) >> 1) ^ (l >> 5)) << 1) | (l & 1)) * 16;
  auto load_wo = [&](int h, int half) {
    const u32 vx = vo ^ (128 * half);
    dma4(ro_, vx, vx ^ 64, vx, vx ^ 64, ((kvo * 8 + h) * 128 + 16 * xcd + 8 * half) * 2048 + 512 * nq, 4096 - 1024, lbase + L_R0 + (8 * h + 4 * half) * 1024);
  };
  v2u xres = {0u, 0u};
  if (hiw) {
#pragma unroll
    for (int i = 0; i < V_OWN; ++i) dma4c(rv, vv, vv, vv ^ 128, vv ^ 128, kvb + u * 16384 + i * 4096, lbase + L_R1 + u * 16384 + i * 4096);
  } else {
    if (tid < 32) *(LAS u32*)(L + L_FL + tid * 4) = 0u;
    if (w == 3) HOP(A0, AR);
  }
  LOAD_TAG();
  BARRIER();
  if (hiw) {
    PAD(A0, A1);
    asm volatile("s_waitcnt vmcnt(%0)" :: "i"(4 * V_OWN) : "memory");
    LSET(F_KR + u * 4);
    STAMP(2);
    LWAIT4(V_GATE ? F_Q : F_KD);
#pragma unroll
    for (int i = V_OWN; i < 4; ++i) dma4c(rv, vv, vv, vv ^ 128, vv ^ 128, kvb + u * 16384 + i * 4096, lbase + L_R1 + u * 16384 + i * 4096);
    STAMP(3);
    if (!V_GATE) LWAIT4(F_Q);
#pragma unroll
    for (int i = 0; i < WO_PRE0; ++i) load_wo(2 * u + (i & 1), i >> 1);
    asm volatile("s_waitcnt vmcnt(%0)" :: "i"(4 * WO_PRE0) : "memory");
    LSET(F_V + u * 4);
    STAMP(5);
#pragma unroll
    for (int i = WO_PRE0; i < WO_PRE; ++i) load_wo(2 * u + (i & 1), i >> 1);
    STAMP(4);
    LWAIT4(F_O);
#pragma unroll
    for (int i = WO_PRE; i < 4; ++i) load_wo(2 * u + (i & 1), i >> 1);
    STAMP(6);
  } else {
    PAD(A1, A2);
    LAS char* pk = L + L_R0 + (64 * u + (l & 15)) * 256;
    LAS char* pb = L + L_ACT + b8 * 288 + 16 * g;
    LAS char* pp = L + L_P + b8 * 544 + (64 * u + 16 * d8 + 4 * g) * 2;
    LAS char* ps = L + L_ST + b8 * 16;
    LAS char* pa = L + L_R1 + (8 * g + q4) * 256 + (((2 * u) ^ fsw) << 5) + 8 * p4;
    LAS char* pf_ = L + L_P + b8 * 544 + 16 * g;
    u32 oo = WS_OP + (((kvh * 8 + 2 * u + d8) * 8 + b) * 4 + s) * 512 + g * 128 + b8 * 16;
    const u32 qb = WS_QG + bk * 16384 + w * 2048 + l * 16;
    PIN(pb); PIN(pp); PIN(ps); PIN(pa); PIN(pf_); PIN(oo);
    hint_wait8(ws + WS_QH + (b * 16 + kvh * 4 + u) * 128, tagLo);
    nap<QH_DELAY>();
    STAMP(12);
    v4u d[4];
#pragma unroll
    for (int i = 0; i < 4; ++i) d[i] = ld16(rsW, qb + (i >> 1) * 8192 + (i & 1) * 1024);
    if (w == 0) xres = __builtin_amdgcn_raw_buffer_load_b64(mkrs(x, 8 * 1024 * 2), b8 * 2048 + jcol * 2, 0, 0);
    LSET(F_KD + u * 4);
    LWAIT1(F_KR + u * 4);
    s16x8 kf[4][4];
#pragma unroll
    for (int ks = 0; ks < 4; ++ks) {
      const int pos = ((4 * ks + g) ^ (l & 15)) * 16;
#pragma unroll
      for (int T = 0; T < 4; ++T) kf[T][ks] = *(LAS s16x8*)(pk + T * 4096 + pos);
    }
    FENCE();
    PAD(A2, A3);
    POLL_LOOP {
      u32 bad = 0;
#pragma unroll
      for (int i = 0; i < 4; ++i) bad |= (d[i].y ^ tagLo) | (d[i].w ^ tagHi);
      if (__builtin_amdgcn_ballot_w64(bad != 0u) == 0ull) break;
      nap<Q_NAP>();
      RELOAD();
#pragma unroll
      for (int i = 0; i < 4; ++i) d[i] = ld16(rsW, qb + (i >> 1) * 8192 + (i & 1) * 1024);
    }
#pragma unroll
    for (int i = 0; i < 2; ++i)
      *(LAS u32*)(L + L_ACT + (2 * w + i) * 288 + l * 4) = pk2(__uint_as_float(d[i].x) + __uint_as_float(d[i + 2].x), __uint_as_float(d[i].z) + __uint_as_float(d[i + 2].z));
    VMWAIT(0);
    LSET(F_Q + u * 4);
    PIN(xres);
    STAMP(2);
    LWAIT4(F_Q);
    STAMP(3);
    PAD(A3, A4);
    s16x8 bf[4];
#pragma unroll
    for (int ks = 0; ks < 4; ++ks) bf[ks] = *(LAS s16x8*)(pb + ks * 64);
    FENCE();
    f32x4 ac[4] = {zero4, zero4, zero4, zero4};
#pragma unroll
    for (int T = 0; T < 4; T += 2)
#pragma unroll
      for (int ks = 0; ks < 4; ++ks) { ac[T] = mfma(kf[T][ks], bf[ks], ac[T]); ac[T + 1] = mfma(kf[T + 1][ks], bf[ks], ac[T + 1]); }
    float sc[8];
#pragma unroll
    for (int pr = 0; pr < 2; ++pr)
#pragma unroll
      for (int r = 0; r < 4; r += 2) {
        const u32 t = pk2(d8 ? ac[2 * pr + 1][r] : ac[2 * pr][r], d8 ? ac[2 * pr + 1][r + 1] : ac[2 * pr][r + 1]);
        const u32 t2 = pk2(bflo(t) * 0.08838834764831845f, bfhi(t) * 0.08838834764831845f);
        sc[4 * pr + r] = bflo(t2); sc[4 * pr + r + 1] = bfhi(t2);
      }
    float mx = fmaxf(fmaxf(fmaxf(sc[0], sc[1]), fmaxf(sc[2], sc[3])), fmaxf(fmaxf(sc[4], sc[5]), fmaxf(sc[6], sc[7])));
    mx = fmaxf(mx, dpp<DPP_R8>(mx));
    mx = max16_32(mx) * LOG2E;
    PAD(A4, A5);
    float pr_[8];
#pragma unroll
    for (int r = 0; r < 8; ++r) pr_[r] = __builtin_amdgcn_exp2f(fmaf(sc[r], LOG2E, -mx));
    *(LAS v2u*)pp = v2u{pk2(pr_[0], pr_[1]), pk2(pr_[2], pr_[3])};
    *(LAS v2u*)(pp + 64) = v2u{pk2(pr_[4], pr_[5]), pk2(pr_[6], pr_[7])};
    float sm = ((pr_[0] + pr_[1]) + (pr_[2] + pr_[3])) + ((pr_[4] + pr_[5]) + (pr_[6] + pr_[7]));
    sm += dpp<DPP_R8>(sm);
    sm = sum16_32(sm);
    if (l < 8) { *(LAS float*)(ps + u * 4) = mx; *(LAS float*)(ps + 128 + u * 4) = sm; }
    LSET(F_P + u * 4);
    STAMP(4);
    LWAIT4(F_P);
    PAD(A5, A6);
    const f32x4 m4 = *(LAS f32x4*)ps, s4 = *(LAS f32x4*)(ps + 128);
    s16x8 pf[8], vf[2][8];
#pragma unroll
    for (int ks = 0; ks < 8; ++ks) pf[ks] = *(LAS s16x8*)(pf_ + ks * 64);
    v4u vfl = *(volatile LAS v4u*)(L + L_FL + F_V);
    FENCE();
    const float mm = fmaxf(fmaxf(m4[0], m4[1]), fmaxf(m4[2], m4[3]));
    f32x4 f;
#pragma unroll
    for (int i = 0; i < 4; ++i) f[i] = __builtin_amdgcn_exp2f(m4[i] - mm);
    const float tot = fmaf(f[0], s4[0], f[1] * s4[1]) + fmaf(f[2], s4[2], f[3] * s4[3]);
    if (w == 3 && l < 8)
      __builtin_amdgcn_raw_buffer_store_b64(v2u{__float_as_uint(mm + __builtin_amdgcn_logf(tot)), (l & 1) ? tagHi : tagLo}, rsW, WS_LSE + (bk * 4 + s) * 128 + l * 8, 0, AUX_SC1);
    if (__builtin_amdgcn_readfirstlane(vfl.x & vfl.y & vfl.z & vfl.w) == 0u) LWAIT4(F_V);
    asm volatile("" ::: "memory");
    STAMP(5);
    PAD(A6, A7);
#pragma unroll
    for (int ks = 0; ks < 8; ++ks) {
      vf[0][ks] = trfrag(pa + ks * 8192, 1024);
      vf[1][ks] = trfrag((LAS char*)((u32)(size_t)pa ^ 32) + ks * 8192, 1024);
    }
    FENCE();
    f *= __builtin_amdgcn_rcpf(tot);
    f32x4 o = zero4;
#pragma unroll
    for (int i = 0; i < 4; ++i) {
      const f32x4 a0 = mfma(vf[0][2 * i + 1], pf[2 * i + 1], mfma(vf[0][2 * i], pf[2 * i], zero4));
      const f32x4 a1 = mfma(vf[1][2 * i + 1], pf[2 * i + 1], mfma(vf[1][2 * i], pf[2 * i], zero4));
#pragma unroll
      for (int r = 0; r < 4; ++r) o[r] = fmaf(d8 ? a1[r] : a0[r], f[i], o[r]);
    }
    st16(rsW, oo, v4u{pk2(o[0], o[1]), tagLo, pk2(o[2], o[3]), tagHi});
    LSET(F_O + u * 4);
    STAMP(6);
  }
  const u32 one = (l & 8) ? 0u : ((l & 1) ? 0x3F800000u : 0x00003F80u);
  const int si = b8 >> 1;
  v4u sel = {si == 0 ? one : 0u, si == 1 ? one : 0u, si == 2 ? one : 0u, si == 3 ? one : 0u};
  u32 wb = WS_WP + j * 4096 + l * 16, hb = WS_H + j * 128 + l * 16, mb = WS_MP + j * MPJ + w * 2048 + l * 16, oo = b8 * 2048 + jcol * 2;
  {
    const int sh = l >> 5;
    const u32 lo0 = WS_LSE + ((w * 4 + kvo) * 4 + sh) * 128 + b8 * 8, ltag = (l & 1) ? tagHi : tagLo;
    const u32 la = (!ALLSYNC || l < 16) ? fo : WS_LSE + w * 2048 + (l & 15) * 128;
    u32 ob = WS_OP + (ksl * 8 + w) * 2048 + l * 16;
    LAS char* po = L + L_ACT + w * 288 + b8 * 32 + ((l >> 3) & 3) * 8 + sh * 4;
    u32 wpo = WS_WP + (4 * (8 * nq + w) + g) * 4096 + ksl * 128 + b8 * 16;
    LAS char* pb = L + L_ACT + b8 * 288 + 16 * g;
    LAS char* pa0 = L + L_R0 + (8 * g + q4) * 512 + (((2 * w) ^ fsw) << 5) + 8 * p4;
    PIN(ob); PIN(po); PIN(wpo); PIN(pb); PIN(pa0); PIN(sel); PIN(wb); PIN(hb); PIN(mb); PIN(oo);
    v4u ea = ld16(rsW, la);
    nap<O_DELAY>();
    v2u e0 = __builtin_amdgcn_raw_buffer_load_b64(rsW, lo0, 0, AUX_SC1), e1 = __builtin_amdgcn_raw_buffer_load_b64(rsW, lo0 + 256, 0, AUX_SC1);
    v4u d0 = ld16(rsW, ob), d1 = ld16(rsW, ob + 1024);
    if (hiw) VMWAIT(5);
    STAMP(7);
    asm volatile("s_barrier" ::: "memory");
    PAD(A7, A8);
    s16x8 wf[2][4];
#pragma unroll
    for (int T = 0; T < 2; ++T)
#pragma unroll
      for (int ks = 0; ks < 4; ++ks) wf[T][ks] = trfrag((LAS char*)((u32)(size_t)pa0 ^ (32 * T)) + ks * 16384, 2048);
    FENCE();
    POLL_LOOP {
      const u32 ta = (!ALLSYNC || l < 16) ? ((ea.x ^ tagLo) | (ea.y ^ tagHi)) : ((ea.y ^ tagLo) | (ea.w ^ tagHi));
      if (__builtin_amdgcn_ballot_w64(ta != 0u) == 0ull) break;
      RELOAD();
      ea = ld16(rsW, la);
    }
    POLL_LOOP {
      const u32 bad = ((e0.y ^ ltag) | (e1.y ^ ltag)) | ((d0.y ^ tagLo) | (d0.w ^ tagHi)) | ((d1.y ^ tagLo) | (d1.w ^ tagHi));
      if (__builtin_amdgcn_ballot_w64(bad != 0u) == 0ull) break;
      nap<O_NAP>();
      RELOAD();
      e0 = __builtin_amdgcn_raw_buffer_load_b64(rsW, lo0, 0, AUX_SC1); e1 = __builtin_amdgcn_raw_buffer_load_b64(rsW, lo0 + 256, 0, AUX_SC1);
      d0 = ld16(rsW, ob); d1 = ld16(rsW, ob + 1024);
    }
    PAD(A8, A9);
    const float l0 = __uint_as_float(e0.x), l1 = __uint_as_float(e1.x);
    float mx = fmaxf(l0, l1);
    { auto t = __builtin_amdgcn_permlane32_swap(__float_as_uint(mx), __float_as_uint(mx), false, false); mx = fmaxf(__uint_as_float(t[0]), __uint_as_float(t[1])); }
    const float w0 = __builtin_amdgcn_exp2f(l0 - mx), w1_ = __builtin_amdgcn_exp2f(l1 - mx);
    float tot = w0 + w1_;
    { auto t = __builtin_amdgcn_permlane32_swap(__float_as_uint(tot), __float_as_uint(tot), false, false); tot = __uint_as_float(t[0]) + __uint_as_float(t[1]); }
    const float it_ = __builtin_amdgcn_rcpf(tot);
    const float v0 = fmaf(w0, bflo(d0.x), w1_ * bflo(d1.x)), v1 = fmaf(w0, bfhi(d0.x), w1_ * bfhi(d1.x));
    const float v2 = fmaf(w0, bflo(d0.z), w1_ * bflo(d1.z)), v3 = fmaf(w0, bfhi(d0.z), w1_ * bfhi(d1.z));
    auto t0 = __builtin_amdgcn_permlane32_swap(__float_as_uint(v0), __float_as_uint(v2), false, false);
    auto t1 = __builtin_amdgcn_permlane32_swap(__float_as_uint(v1), __float_as_uint(v3), false, false);
    *(LAS u32*)po = pk2((__uint_as_float(t0[0]) + __uint_as_float(t0[1])) * it_, (__uint_as_float(t1[0]) + __uint_as_float(t1[1])) * it_);
    STAMP(8);
    BARRIER();
    s16x8 bf[4];
#pragma unroll
    for (int ks = 0; ks < 4; ++ks) bf[ks] = *(LAS s16x8*)(pb + ks * 64);
    FENCE();
    f32x4 a0 = zero4, a1 = zero4;
#pragma unroll
    for (int ks = 0; ks < 4; ++ks) { a0 = mfma(wf[0][ks], bf[ks], a0); a1 = mfma(wf[1][ks], bf[ks], a1); }
    if (!d8) st16(rsW, wpo, v4u{pk2(a0[0], a0[1]), pk2(a0[2], a0[3]), pk2(a1[0], a1[1]), pk2(a1[2], a1[3])});
    STAMP(9);
  }
  PAD(A9, A10);
  u32 h01 = 0, h23 = 0;
  if (w == 0) {
    nap<R_DELAY>();
    f32x4 acc = zero4;
    POLL_LOOP {
      RELOAD();
      v4u d[4];
#pragma unroll
      for (int i = 0; i < 4; ++i) d[i] = ld16(rsW, wb + i * 1024);
      FENCE();
      acc = zero4;
#pragma unroll
      for (int i = 0; i < 4; ++i) acc = mfma(sel, d[i], acc);
      if (wave_clean(acc[0])) break;
    }
    PAD(A10, A11);
    const u32 a01 = pk2(acc[0] + dpp<DPP_R8>(acc[0]), acc[1] + dpp<DPP_R8>(acc[1]));
    const u32 a23 = pk2(acc[2] + dpp<DPP_R8>(acc[2]), acc[3] + dpp<DPP_R8>(acc[3]));
    h01 = pk2(bflo(xres.x) + bflo(a01), bfhi(xres.x) + bfhi(a01));
    h23 = pk2(bflo(xres.y) + bflo(a23), bfhi(xres.y) + bfhi(a23));
    auto s0 = __builtin_amdgcn_permlane16_swap(h01, h01, false, false);
    auto s1 = __builtin_amdgcn_permlane16_swap(h23, h23, false, false);
    if (l < 8) st16(rsW, hb, v4u{s0[0], s1[0], s0[1], s1[1]});
    STAMP(10);
  } else if (w == 7) {
    if (l == 0) __builtin_amdgcn_raw_buffer_store_b64(v2u{tagLo, (u32)__builtin_amdgcn_s_memrealtime()}, rsW, WS_FLAG + j * 128 + 64, 0, AUX_SC1);
  }
  asm volatile("s_barrier" ::: "memory");
  PAD(A11, A12);
  LAS char* pfs = L + L_P + (l & 31) * 16;
  if (w < 7) {
    nap<F_DELAY>();
    f32x4 acc = zero4;
    POLL_LOOP {
      RELOAD();
      const v4u d0 = ld16(rsW, mb), d1 = ld16(rsW, mb + 1024);
      FENCE();
      acc = mfma(sel, d1, mfma(sel, d0, zero4));
      if (wave_clean(acc[0])) break;
      nap<F_NAP>();
    }
    STAMP(13);
    if (l < 32) *(LAS f32x4*)(pfs + w * 512) = acc;
  }
  BARRIER();
  PAD(A12, AR);
  if (w == 0) {
    f32x4 p[7];
#pragma unroll
    for (int i = 0; i < 7; ++i) p[i] = *(LAS f32x4*)(pfs + i * 512);
    FENCE();
    const f32x4 acc = ((p[0] + p[1]) + (p[2] + p[3])) + ((p[4] + p[5]) + p[6]);
    const u32 y01 = pk2(acc[0] + dpp<DPP_R8>(acc[0]), acc[1] + dpp<DPP_R8>(acc[1]));
    const u32 y23 = pk2(acc[2] + dpp<DPP_R8>(acc[2]), acc[3] + dpp<DPP_R8>(acc[3]));
    const v2u r = {pk2(bflo(h01) + bflo(y01), bfhi(h01) + bfhi(y01)), pk2(bflo(h23) + bflo(y23), bfhi(h23) + bfhi(y23))};
    if ((l & 40) == 0) __builtin_amdgcn_raw_buffer_store_b64(r, mkrs(out, 8 * 1024 * 2), oo, 0, 0);
    if (j == 0 && l == 0) __builtin_amdgcn_raw_buffer_store_b64(v2u{tagLo, tagHi}, rsW, WS_EPOCH, 0, AUX_SC1);
    STAMP(11);
  }
  STAMP_FLUSH();
}

extern "C" void kernel_launch(void* const* d_in, const int* in_sizes, int n_in, void* d_out, int out_size, void* d_ws, size_t ws_size,
                              hipStream_t stream) {
  (void)hipFuncSetAttribute((const void*)fused_decode_layer, hipFuncAttributeMaxDynamicSharedMemorySize, L_BYTES);
  fused_decode_layer<<<NWG, NTHR, L_BYTES, stream>>>((const char*)d_in[0], (const char*)d_in[1], (const char*)d_in[2], (const char*)d_in[3],
                                                     (const char*)d_in[4], (const char*)d_in[6], (const char*)d_in[7], (char*)d_out, (char*)d_ws + WS_SHIFT(ws_size));
}
```

```cpp
#include <hip/hip_runtime.h>
#include <hip/hip_bf16.h>
#include <stdint.h>

#define DIAG 0
#define A_START_DELAY 4
#define WO_DELAY 2
#define R_DELAY 8
#define F_DELAY 60
#define L_NAP 1
#define Q_NAP 1
#define F_NAP 0
#define H_NAP 0
#define H_DELAY 125
#define NTOUCH_V 8
#define TOUCH_DELAY 2
#define C3FIX 1
#define WQ_TOUCH 1
#define CODE_LINES 15
#define CODE_STRIDE 64
#define V_OWN 1
#define W1C3 1
#define QH_DELAY 2
#define W2_PRE 2
#define V_GATE 0
#define ALLSYNC 1
#define MP_AT 1
#define NT_W1B 1
#define LNAP 0
#define TOUCH_EARLY 0
#define AM_DELAY 0
#define SPEC_O 1
#define FCHK_DELAY 40
#define W2_PRE0 1
#define HOPS 1

#define LAS __attribute__((address_space(3)))
typedef unsigned u32;
typedef unsigned long long u64;
typedef unsigned v4u __attribute__((ext_vector_type(4)));
typedef unsigned v2u __attribute__((ext_vector_type(2)));
typedef float f32x4 __attribute__((ext_vector_type(4)));
typedef float f32x2 __attribute__((ext_vector_type(2)));
typedef short s16x2 __attribute__((ext_vector_type(2)));
typedef short s16x4 __attribute__((ext_vector_type(4)));
typedef short s16x8 __attribute__((ext_vector_type(8)));
typedef __bf16 bf16x2 __attribute__((ext_vector_type(2)));
typedef __bf16 bf16x8 __attribute__((ext_vector_type(8)));
typedef __amdgpu_buffer_rsrc_t rsrc_t;

namespace {

constexpr int NWG = 256, NTHR = 512;
constexpr u32 SENT = 0xFFFFFFFFu;
constexpr int AUX_SC1 = 16;
constexpr int SPIN_CAP = 20000;
constexpr float LOG2E = 1.4426950408889634f;

constexpr int WS_EPOCH = 0;
constexpr int WS_LSE = 4096;
constexpr int WS_FLAG = 49152;
constexpr int WS_H = 32768;
constexpr int WS_QH = 0xA0000;
constexpr int WS_QG = 65536;
constexpr int WS_OP = 1 << 20;
constexpr int WS_WP = WS_OP + 524288;
constexpr int WS_MP = WS_WP + 524288;
constexpr int MPJ = 112 * 128;
constexpr int WS_END = WS_MP + 128 * MPJ;
constexpr int WS_STAMP = 8 << 20;

constexpr int L_R0 = 0, L_R1 = 65536, L_MISC = 131072;
constexpr int L_ACT = L_MISC;
constexpr int L_P = L_ACT + 8 * 1056;
constexpr int L_ST = L_P + 5376;
constexpr int L_FL = L_ST + 512;
constexpr int F_KR = 0, F_KD = 16, F_Q = 32, F_P = 48, F_V = 64, F_O = 80, F_H = 96, F_W = 112;
constexpr int L_BYTES = L_ST + 2048;

__device__ __forceinline__ float bflo(u32 v) { return __uint_as_float(v << 16); }
__device__ __forceinline__ float bfhi(u32 v) { return __uint_as_float(v & 0xFFFF0000u); }
__device__ __forceinline__ u32 pk2(float lo, float hi) {
  bf16x2 r = {(__bf16)lo, (__bf16)hi};
  return __builtin_bit_cast(u32, r);
}
__device__ __forceinline__ float rbf(float x) { return (float)(__bf16)x; }

template <int CTRL> __device__ __forceinline__ float dpp(float x) {
  return __builtin_bit_cast(float, __builtin_amdgcn_mov_dpp(__builtin_bit_cast(int, x), CTRL, 0xf, 0xf, true));
}
constexpr int DPP_X1 = 0xB1, DPP_X2 = 0x4E, DPP_HM = 0x141, DPP_M = 0x140, DPP_R8 = 0x128, DPP_ID = 0xE4;
__device__ __forceinline__ float sum16_32(float x) {
  auto s = __builtin_amdgcn_permlane16_swap(__float_as_uint(x), __float_as_uint(x), false, false);
  x = __uint_as_float(s[0]) + __uint_as_float(s[1]);
  auto t = __builtin_amdgcn_permlane32_swap(__float_as_uint(x), __float_as_uint(x), false, false);
  return __uint_as_float(t[0]) + __uint_as_float(t[1]);
}
__device__ __forceinline__ float max16_32(float x) {
  auto s = __builtin_amdgcn_permlane16_swap(__float_as_uint(x), __float_as_uint(x), false, false);
  x = fmaxf(__uint_as_float(s[0]), __uint_as_float(s[1]));
  auto t = __builtin_amdgcn_permlane32_swap(__float_as_uint(x), __float_as_uint(x), false, false);
  return fmaxf(__uint_as_float(t[0]), __uint_as_float(t[1]));
}
__device__ __forceinline__ float wave_sum(float x) {
  x += dpp<DPP_X1>(x); x += dpp<DPP_X2>(x); x += dpp<DPP_HM>(x); x += dpp<DPP_M>(x);
  return sum16_32(x);
}
__device__ __forceinline__ f32x4 mfma(s16x8 a, s16x8 b, f32x4 c) {
  return __builtin_amdgcn_mfma_f32_16x16x32_bf16(__builtin_bit_cast(bf16x8, a), __builtin_bit_cast(bf16x8, b), c, 0, 0, 0);
}
__device__ __forceinline__ f32x4 mfma(v4u a, v4u b, f32x4 c) {
  return __builtin_amdgcn_mfma_f32_16x16x32_bf16(__builtin_bit_cast(bf16x8, a), __builtin_bit_cast(bf16x8, b), c, 0, 0, 0);
}
__device__ __forceinline__ s16x8 trfrag(LAS char* p, int half_off) {
  typedef short v4i16_t __attribute__((ext_vector_type(4)));
  s16x4 lo = __builtin_amdgcn_ds_read_tr16_b64_v4i16((LAS v4i16_t*)p);
  s16x4 hi = __builtin_amdgcn_ds_read_tr16_b64_v4i16((LAS v4i16_t*)(p + half_off));
  return __builtin_shufflevector(lo, hi, 0, 1, 2, 3, 4, 5, 6, 7);
}
#define DMA4_ASM(POL) \
  asm volatile("s_mov_b32 m0, %6\n\ts_nop 0\n\t" \
               "buffer_load_dwordx4 %1, %5, %0 offen " POL " lds\n\ts_add_u32 %0, %0, %7\n\t" \
               "buffer_load_dwordx4 %2, %5, %0 offen offset:1024 " POL " lds\n\ts_add_u32 %0, %0, %7\n\t" \
               "buffer_load_dwordx4 %3, %5, %0 offen offset:2048 " POL " lds\n\ts_add_u32 %0, %0, %7\n\t" \
               "buffer_load_dwordx4 %4, %5, %0 offen offset:3072 " POL " lds" \
               : "+s"(soff) : "v"(v0), "v"(v1), "v"(v2), "v"(v3), "s"(rs), "s"(lds), "s"(sdelta) : "memory", "scc")
template <int NT = 1>
__device__ __forceinline__ u32 dma4(rsrc_t rs, u32 v0, u32 v1, u32 v2, u32 v3, u32 soff, u32 sdelta, u32 lds) {
  if constexpr (NT) DMA4_ASM("nt"); else DMA4_ASM("");
  return soff;
}
__device__ __forceinline__ void dma4c(rsrc_t rs, u32 v0, u32 v1, u32 v2, u32 v3, u32 soff, u32 lds) {
  asm volatile("s_mov_b32 m0, %6\n\ts_nop 0\n\t"
               "buffer_load_dwordx4 %1, %5, %0 offen nt lds\n\t"
               "buffer_load_dwordx4 %2, %5, %0 offen offset:1024 nt lds\n\t"
               "buffer_load_dwordx4 %3, %5, %0 offen offset:2048 nt lds\n\t"
               "buffer_load_dwordx4 %4, %5, %0 offen offset:3072 nt lds"
               :: "s"(soff), "v"(v0), "v"(v1), "v"(v2), "v"(v3), "s"(rs), "s"(lds) : "memory");
}
template <int NT = 1>
__device__ __forceinline__ void touch(rsrc_t rs, u32 voff, u32 soff, u32 lds) {
  if constexpr (NT) asm volatile("s_mov_b32 m0, %0\n\ts_nop 0\n\tbuffer_load_dword %1, %2, %3 offen nt lds" ::"s"(lds), "v"(voff), "s"(rs), "s"(soff) : "memory");
  else asm volatile("s_mov_b32 m0, %0\n\ts_nop 0\n\tbuffer_load_dword %1, %2, %3 offen lds" ::"s"(lds), "v"(voff), "s"(rs), "s"(soff) : "memory");
}
#define LOAD_TAG() do { u64 e_; asm volatile("s_load_dwordx2 %0, %1, 0x0\n\ts_waitcnt lgkmcnt(0)" : "=s"(e_) : "s"(ws) : "memory"); tagLo = (u32)e_ + 1u; tagHi = (u32)(e_ >> 32) + 1u; } while (0)
#define PAD(n, nx) asm volatile("Lpad" #n ":\n\ts_cbranch_execz Lpad" #nx)
#define HOP(first, ret) asm volatile("s_mov_b64 exec, 0\n\ts_branch Lpad" #first "\nLpad" #ret ":\n\ts_mov_b64 exec, -1")
#define LSET(off) do { asm volatile("s_waitcnt lgkmcnt(0)" ::: "memory"); *(volatile LAS u32*)(L + L_FL + (off)) = 1u; } while (0)
#define LWAIT1(off) do { _Pragma("nounroll") for (int it_ = 0; it_ < 200000; ++it_) { \
    if (__builtin_amdgcn_readfirstlane(*(volatile LAS u32*)(L + L_FL + (off))) != 0u) break; nap<LNAP>(); } asm volatile("" ::: "memory"); } while (0)
#define LWAIT4(off) do { _Pragma("nounroll") for (int it_ = 0; it_ < 200000; ++it_) { const v4u f_ = *(volatile LAS v4u*)(L + L_FL + (off)); \
    if (__builtin_amdgcn_readfirstlane(f_.x & f_.y & f_.z & f_.w) != 0u) break; nap<LNAP>(); } asm volatile("" ::: "memory"); } while (0)
#define LWAIT8(off) do { _Pragma("nounroll") for (int it_ = 0; it_ < 200000; ++it_) { \
    const v4u f_ = *(volatile LAS v4u*)(L + L_FL + (off)), g_ = *(volatile LAS v4u*)(L + L_FL + (off) + 16); \
    if (__builtin_amdgcn_readfirstlane((f_.x & f_.y & f_.z & f_.w) & (g_.x & g_.y & g_.z & g_.w)) != 0u) break; nap<LNAP>(); } asm volatile("" ::: "memory"); } while (0)
__device__ __forceinline__ u64 hint_peek(const char* p) {
  u64 v;
  asm volatile("s_load_dwordx2 %0, %1, 0x0 glc\n\ts_waitcnt lgkmcnt(0)" : "=s"(v) : "s"(p) : "memory");
  return v;
}
__device__ __forceinline__ void hint_wait(const char* p, u32 tag, u32 ticks, u64 v) {
#pragma nounroll
  for (int it = 0; it < 64; ++it) {
    if ((u32)v == tag) break;
    v = hint_peek(p);
  }
#pragma nounroll
  for (int it = 0; it < 64; ++it) { if ((u32)__builtin_amdgcn_s_memrealtime() - (u32)(v >> 32) >= ticks) break; }
}
typedef unsigned s8u __attribute__((ext_vector_type(8)));
__device__ __forceinline__ void hint_wait8(const char* p, u32 tag) {
#pragma nounroll
  for (int it = 0; it < 64; ++it) {
    s8u v;
    asm volatile("s_load_dwordx8 %0, %1, 0x0 glc\n\ts_waitcnt lgkmcnt(0)" : "=s"(v) : "s"(p) : "memory");
    if ((((v[0] ^ tag) | (v[1] ^ tag)) | ((v[2] ^ tag) | (v[3] ^ tag)) | ((v[4] ^ tag) | (v[5] ^ tag)) | ((v[6] ^ tag) | (v[7] ^ tag))) == 0u) break;
  }
}
__device__ __forceinline__ u32 max4(v4u v) { return max(max(v.x, v.y), max(v.z, v.w)); }
#define POLL_LOOP _Pragma("nounroll") for (int it = 0; it < SPIN_CAP; ++it)
#define RELOAD() asm volatile("" ::: "memory")
#define BARRIER() asm volatile("s_waitcnt lgkmcnt(0)\n\ts_barrier" ::: "memory")
#define VMWAIT(n) asm volatile("s_waitcnt vmcnt(" #n ")" ::: "memory")
#define FENCE() __builtin_amdgcn_sched_barrier(0)
#define PIN(x) asm volatile("" : "+v"(x))
template <int N> __device__ __forceinline__ void nap() {
  if constexpr (N > 0) { __builtin_amdgcn_s_sleep(N > 127 ? 127 : N); nap<(N > 127 ? N - 127 : 0)>(); }
}
__device__ __forceinline__ v4u ld16(rsrc_t rs, u32 off) { return __builtin_amdgcn_raw_buffer_load_b128(rs, off, 0, AUX_SC1); }
__device__ __forceinline__ u32 ld4(rsrc_t rs, u32 off) { return __builtin_amdgcn_raw_buffer_load_b32(rs, off, 0, AUX_SC1); }
__device__ __forceinline__ void st16(rsrc_t rs, u32 off, v4u v) { __builtin_amdgcn_raw_buffer_store_b128(v, rs, off, 0, AUX_SC1); }
__device__ __forceinline__ bool wave_clean(float v) { return __builtin_amdgcn_ballot_w64(v != v) == 0ull; }
__device__ __forceinline__ rsrc_t mkrs(const void* p, int bytes) { return __builtin_amdgcn_make_buffer_rsrc((void*)p, 0, bytes, 0x00020000); }

__device__ __forceinline__ float dot2(u32 a, u32 b, float c) {
  return __builtin_amdgcn_fdot2_f32_bf16(__builtin_bit_cast(bf16x2, a), __builtin_bit_cast(bf16x2, b), c, false);
}
__device__ __forceinline__ v4u ln_half(const v4u c, const v4u o) {
  float s = 0.f, q = 0.f;
#pragma unroll
  for (int i = 0; i < 4; ++i) { s = dot2(c[i], 0x3F803F80u, s); q = dot2(c[i], c[i], q); s = dot2(o[i], 0x3F803F80u, s); q = dot2(o[i], o[i], q); }
  const float mean = wave_sum(s) * (1.f / 1024.f);
  const float var = fmaxf(fmaf(-mean, mean, wave_sum(q) * (1.f / 1024.f)), 0.f);
  const u32 mv = pk2(mean, var);
  const float mb = bflo(mv);
  const float den = rbf(__builtin_amdgcn_sqrtf(rbf(bfhi(mv) + 1e-5f)));
  const float inv = __builtin_amdgcn_rcpf(den);
  v4u r;
#pragma unroll
  for (int i = 0; i < 4; ++i) {
    const u32 d = pk2(bflo(c[i]) - mb, bfhi(c[i]) - mb);
    const float d0 = bflo(d), d1 = bfhi(d);
    float q0 = d0 * inv, q1 = d1 * inv;
    q0 = fmaf(fmaf(-q0, den, d0), inv, q0);
    q1 = fmaf(fmaf(-q1, den, d1), inv, q1);
    r[i] = pk2(q0, q1);
  }
  return r;
}

#define STAMP(i) do {} while (0)
#define STAMP_DECL() do {} while (0)
#define STAMP_FLUSH() do {} while (0)

}

__global__ void __launch_bounds__(NTHR, 2)
fused_decode_layer(const char* x, const char* kc, const char* vc, const char* wq, const char* wo, const char* w1, const char* w2, char* out, char* ws) {
  extern __shared__ __attribute__((aligned(16))) char smem[];
  STAMP_DECL();
  STAMP(0);
  {
    u32 t0, t1, t2, t3, t4;
    asm volatile(
        "s_load_dwordx8 s[64:71], %[ka], 0x0\n\t"
        "s_getpc_b64 s[82:83]\n\t"
        "v_readfirstlane_b32 s72, %[tid]\n\t"
        "v_and_b32 %[t4], 63, %[tid]\n\t"
        "s_mov_b32 s79, 0x20000\n\t"
        "s_mov_b32 s78, 0x800000\n\t"
        "s_lshr_b32 s72, s72, 6\n\t"
        "s_and_b32 s73, s72, 3\n\t"
        "v_lshrrev_b32 %[t1], 4, %[t4]\n\t"
        "s_bfe_u32 s84, %[c], 0x20003\n\t"
        "s_bfe_u32 s74, %[c], 0x10006\n\t"
        "s_lshl1_add_u32 s84, s84, s74\n\t"
        "s_bfe_u32 s85, %[c], 0x10005\n\t"
        "s_lshl2_add_u32 s85, s85, s73\n\t"
        "s_and_b32 s74, %[c], 0x80\n\t"
        "s_cmp_gt_u32 s72, 3\n\t"
        "s_cbranch_scc1 Lpro_hi\n\t"
        "s_cmp_eq_u32 s74, 0\n\t"
        "s_cbranch_scc1 Lpro_bl\n\t"
        "s_cmp_lg_u32 s72, 3\n\t"
        "s_cbranch_scc1 Lpro_al\n\t"
        "s_bfe_u32 s74, %[c], 0x40003\n\t"
        "s_mul_i32 s74, s74, %[cl]*%[cs]\n\t"
        "s_and_b32 s82, s82, 0xffffff00\n\t"
        "s_add_u32 s82, s82, s74\n\t"
        "s_addc_u32 s83, s83, 0\n\t"
        ".irp o,0,1,2,3,4,5,6,7,8,9,10,11,12,13,14,15\n\t"
        ".if \\o < %[cl]\n\t"
        "s_load_dword s75, s[82:83], \\o*%[cs]\n\t"
        ".endif\n\t"
        ".endr\n\t"
        "s_branch Lpro_end\n\t"
        "Lpro_al:\n\t"
        "s_lshl1_add_u32 s74, s73, s84\n\t"
        "s_and_b32 s74, s74, 7\n\t"
        "s_add_u32 s75, s74, %[c]\n\t"
        "s_and_b32 s75, s75, 7\n\t"
        "s_lshl3_add_u32 s75, s74, s75\n\t"
        "s_lshl_b32 s75, s75, 7\n\t"
        "s_lshl1_add_u32 s74, s85, 1\n\t"
        "s_lshl_b32 s74, s74, 19\n\t"
        "s_or_b32 s74, s74, s75\n\t"
        "v_lshlrev_b32 %[t0], 13, %[t4]\n\t"
        "s_mov_b32 m0, %[junk]\n\t"
        "s_mov_b32 s78, %[wqb]\n\t"
        "s_waitcnt lgkmcnt(0)\n\t"
        "s_mov_b32 s76, s70\n\t"
        "s_and_b32 s77, s71, 0xffff\n\t"
        "buffer_load_dword %[t0], s[76:79], s74 offen lds\n\t"
        "s_add_u32 s85, s85, 1\n\t"
        "s_add_u32 s73, s73, 1\n\t"
        "s_cmp_eq_u32 s73, 3\n\t"
        "s_cbranch_scc1 Lpro_al\n\t"
        "s_branch Lpro_end\n\t"
        "Lpro_hi:\n\t"
        "s_lshl_b32 m0, s73, 14\n\t"
        "s_cmp_lg_u32 s74, 0\n\t"
        "s_cbranch_scc1 Lpro_ah\n\t"
        "v_and_b32 %[t1], 1, %[t1]\n\t"
        "v_lshlrev_b32 %[t1], 1, %[t1]\n\t"
        "v_and_b32 %[t0], 7, %[t4]\n\t"
        "v_xor_b32 %[t0], %[t0], %[t1]\n\t"
        "v_lshrrev_b32 %[t2], 3, %[t4]\n\t"
        "v_lshlrev_b32 %[t2], 13, %[t2]\n\t"
        "v_lshl_or_b32 %[t0], %[t0], 4, %[t2]\n\t"
        "v_xor_b32 %[t1], 64, %[t0]\n\t"
        "s_add_u32 s75, s84, %[c]\n\t"
        "s_and_b32 s75, s75, 7\n\t"
        "s_lshl3_add_u32 s75, s84, s75\n\t"
        "s_lshl_b32 s75, s75, 7\n\t"
        "s_lshl_b32 s74, s85, 20\n\t"
        "s_or_b32 s74, s74, s75\n\t"
        "s_mov_b32 s75, 0xfc00\n\t"
        "s_mov_b32 s73, 4\n\t"
        "s_waitcnt lgkmcnt(0)\n\t"
        "s_mov_b32 s76, s70\n\t"
        "s_and_b32 s77, s71, 0xffff\n\t"
        "Lpro_bh_loop:\n\t"
        "buffer_load_dwordx4 %[t0], s[76:79], s74 offen nt lds\n\t"
        "s_add_u32 s74, s74, s75\n\t"
        "buffer_load_dwordx4 %[t1], s[76:79], s74 offen offset:1024 nt lds\n\t"
        "s_add_u32 s74, s74, s75\n\t"
        "buffer_load_dwordx4 %[t0], s[76:79], s74 offen offset:2048 nt lds\n\t"
        "s_add_u32 s74, s74, s75\n\t"
        "buffer_load_dwordx4 %[t1], s[76:79], s74 offen offset:3072 nt lds\n\t"
        "s_add_u32 m0, m0, 0x1000\n\t"
        "s_add_u32 s74, s74, 0x10c00\n\t"
        "s_sub_u32 s73, s73, 1\n\t"
        "s_cmp_lg_u32 s73, 0\n\t"
        "s_cbranch_scc1 Lpro_bh_loop\n\t"
        "s_branch Lpro_end\n\t"
        "Lpro_bl:\n\t"
        "v_lshlrev_b32 %[t0], 4, %[t4]\n\t"
        "v_and_b32 %[t1], 31, %[t4]\n\t"
        "v_lshlrev_b32 %[t1], 13, %[t1]\n\t"
        "s_lshl_b32 s74, s73, 12\n\t"
        "s_add_u32 m0, s74, %[r1]\n\t"
        "s_waitcnt lgkmcnt(0)\n\t"
        "s_and_b32 s65, s65, 0xffff\n\t"
        "s_movk_i32 s66, 0x4000\n\t"
        "s_mov_b32 s67, s79\n\t"
        "buffer_load_dwordx4 %[t0], s[64:67], s74 offen lds\n\t"
        "buffer_load_dwordx4 %[t0], s[64:67], s74 offen offset:1024 lds\n\t"
        "buffer_load_dwordx4 %[t0], s[64:67], s74 offen offset:2048 lds\n\t"
        "buffer_load_dwordx4 %[t0], s[64:67], s74 offen offset:3072 lds\n\t"
        "s_cmp_lg_u32 s72, %[c3w]\n\t"
        "s_cbranch_scc1 Lpro_end\n\t"
        "s_lshr_b32 s74, s70, 7\n\t"
        "s_sub_u32 s74, 3, s74\n\t"
        "s_and_b32 s74, s74, 7\n\t"
        "s_sub_u32 s75, s74, %[c]\n\t"
        "s_and_b32 s75, s75, 7\n\t"
        "s_lshl3_add_u32 s75, s75, s74\n\t"
        "s_lshl_b32 s75, s75, 7\n\t"
        "s_bfe_u32 s82, %[c], 0x30003\n\t"
        "s_bfe_u32 s83, %[c], 0x10006\n\t"
        "s_lshl2_add_u32 s82, s82, s83\n\t"
        "s_lshl_b32 s82, s82, 18\n\t"
        "s_or_b32 s74, s82, s75\n\t"
        "s_mov_b32 m0, %[junk]\n\t"
        "s_mov_b32 s76, s70\n\t"
        "s_and_b32 s77, s71, 0xffff\n\t"
        "buffer_load_dword %[t1], s[76:79], s74 offen lds\n\t"
        "s_branch Lpro_end\n\t"
        "Lpro_ah:\n\t"
        "v_and_b32 %[t4], 15, %[t4]\n\t"
        "v_xor_b32 %[t0], %[t4], %[t1]\n\t"
        "v_lshlrev_b32 %[t1], 8, %[t1]\n\t"
        "v_lshl_or_b32 %[t0], %[t0], 4, %[t1]\n\t"
        "v_xor_b32 %[t1], 64, %[t0]\n\t"
        "v_xor_b32 %[t2], 0x80, %[t0]\n\t"
        "v_xor_b32 %[t3], 0x80, %[t1]\n\t"
        "s_bfe_u32 s74, %[c], 0x20005\n\t"
        "s_and_b32 s75, %[c], 7\n\t"
        "s_lshl3_add_u32 s74, s74, s75\n\t"
        "s_bfe_u32 s75, %[c], 0x20003\n\t"
        "s_lshl4_add_u32 s74, s74, s75\n\t"
        "s_or_b32 s74, s74, 12\n\t"
        "s_lshl2_add_u32 s74, s74, s73\n\t"
        "s_lshl_b32 s74, s74, 14\n\t"
        "s_brev_b32 s78, 64\n\t"
        "s_mov_b32 s73, 4\n\t"
        "s_waitcnt lgkmcnt(0)\n\t"
        "s_sleep %[dly]\n\t"
        "s_mov_b32 s76, s66\n\t"
        "s_and_b32 s77, s67, 0xffff\n\t"
        "Lpro_k_loop:\n\t"
        "buffer_load_dwordx4 %[t0], s[76:79], s74 offen nt lds\n\t"
        "buffer_load_dwordx4 %[t1], s[76:79], s74 offen offset:1024 nt lds\n\t"
        "buffer_load_dwordx4 %[t2], s[76:79], s74 offen offset:2048 nt lds\n\t"
        "buffer_load_dwordx4 %[t3], s[76:79], s74 offen offset:3072 nt lds\n\t"
        "s_add_u32 m0, m0, 0x1000\n\t"
        "s_addk_i32 s74, 0x1000\n\t"
        "s_sub_u32 s73, s73, 1\n\t"
        "s_cmp_lg_u32 s73, 0\n\t"
        "s_cbranch_scc1 Lpro_k_loop\n\t"
        "Lpro_end:\n\t"
        ".pushsection .text,1\n\t"
        ".fill 512, 4, 0xBF800000\n\t"
        ".popsection\n\t"
        "s_waitcnt lgkmcnt(0)"
        : [t0] "=&v"(t0), [t1] "=&v"(t1), [t2] "=&v"(t2), [t3] "=&v"(t3), [t4] "=&v"(t4)
        : [ka] "s"(__builtin_amdgcn_kernarg_segment_ptr()), [tid] "v"(threadIdx.x), [c] "s"(blockIdx.x), [cl] "i"(CODE_LINES), [cs] "i"(CODE_STRIDE),
          [junk] "i"(L_ST + 1024), [r1] "i"(L_R1), [dly] "i"(A_START_DELAY),
          [c3w] "i"(C3FIX ? 0 : 9), [wqb] "i"(WQ_TOUCH ? 0x800000 : 0)
        : "s64", "s65", "s66", "s67", "s68", "s69", "s70", "s71", "s72", "s73", "s74", "s75", "s76", "s77", "s78", "s79",
          "s82", "s83", "s84", "s85", "scc", "memory");
  }
  STAMP(1);
  LAS char* L = (LAS char*)smem;
  constexpr u32 lbase = 0;
  const int tid = threadIdx.x, l = tid & 63, w = __builtin_amdgcn_readfirstlane(tid >> 6), c = blockIdx.x;
  const int g = l >> 4, xcd = c & 7, sl = (c >> 3) & 15, u = w & 3;
  const bool hiw = w >= 4;
  const u32 vv = g * 256 + (((((l & 15) >> 1) ^ g) << 1) | (l & 1)) * 16;
  const int band = 2 * (sl & 3) + (sl >> 3), cg = 8 * band + ((xcd + band) & 7), kh = (sl >> 2) & 1;
  const int bk = (sl >> 2) * 8 + xcd;
  const u32 kvb = (bk * 4096 + 3072 + 256 * (sl & 3)) * 256;
  const int q4 = (l >> 2) & 3, p4 = l & 3, b8 = l & 7, d8 = (l >> 3) & 1;
  const int fsw = q4 | ((g & 1) << 2);
  const rsrc_t rsW = mkrs(ws, WS_END);
  const v4u sent4 = {SENT, SENT, SENT, SENT};
  const f32x4 zero4 = {0.f, 0.f, 0.f, 0.f};
  u32 tagLo = 0, tagHi = 0;

  if (c < 128) {
    if (!hiw) {
      LAS char* px = L + L_R1 + w * 4096 + l * 16;
      if (C3FIX && w == 0) VMWAIT(1); else VMWAIT(0);
      STAMP(13);
      const v4u xc0 = *(LAS v4u*)(px + kh * 1024), xo0 = *(LAS v4u*)(px + (kh ^ 1) * 1024);
      const v4u xc1 = *(LAS v4u*)(px + 2048 + kh * 1024), xo1 = *(LAS v4u*)(px + 2048 + (kh ^ 1) * 1024);
      *(LAS v4u*)(L + L_ACT + (2 * w) * 1056 + l * 16) = ln_half(xc0, xo0);
      *(LAS v4u*)(L + L_ACT + (2 * w + 1) * 1056 + l * 16) = ln_half(xc1, xo1);
      STAMP(12);
      if (w == 0) HOP(B0, BR);
    }
    const int j = c, kvo = sl >> 2, nq = sl & 3, ksl = kvo * 8 + xcd;
    const int jcol = 32 * (j >> 2) + 4 * (j & 3) + 16 * (g & 1);
    auto clear_mp = [&]() {
#pragma unroll
      for (int i = 0; i < 14; ++i) __builtin_amdgcn_raw_buffer_store_b128(sent4, rsW, WS_MP + j * MPJ + (i & 3) * 1024 + l * 16, (i >> 2) * 4096, AUX_SC1);
    };
    LOAD_TAG();
    PAD(B0, B1);
    BARRIER();
    if (w == 7) {
#pragma unroll
      for (int i = 0; i < 4; ++i) st16(rsW, WS_WP + j * 4096 + i * 1024 + l * 16, sent4);
      st16(rsW, l < 8 ? WS_H + j * 128 + l * 16 : WS_END, sent4);
    }
    if (MP_AT == 0 && w == 3) { nap<2>(); clear_mp(); }
    const rsrc_t rv = mkrs(vc, 8 * 4 * 4096 * 128 * 2);
    if (w < TOUCH_EARLY) { nap<2>(); touch<0>(rv, g * 16384 + (l & 15) * 128, kvb + (2 * V_OWN + w) * 2048, lbase + L_ST + 1024); }
    v2u xres = *(LAS v2u*)(L + L_R1 + b8 * 2048 + jcol * 2);
    PIN(xres);
    const int bq = 2 * u + (l >> 5), cp = l & 31;
    LAS char* pq = L + L_P + bq * 272 + cp * 8;
    u32 qo = WS_QG + (((bq * 4 + (cg >> 4)) * 2 + kh) * 1024 + 64 * (cg & 15) + 2 * cp) * 8;
    {
      const int kh2 = w >> 2;
      s16x8 bf[8];
      LAS char* pb = L + L_ACT + b8 * 1056 + kh2 * 512 + 16 * g;
#pragma unroll
      for (int ks = 0; ks < 8; ++ks) bf[ks] = *(LAS s16x8*)(pb + ks * 64);
      LAS char* pa = L + L_R0 + kh2 * 32768 + (8 * g + q4) * 128 + ((u ^ (((q4 >> 1) & 1) | ((g & 1) << 1))) << 5) + 8 * p4;
      LAS char* pw = L + L_P + (kh2 * 8 + b8) * 272 + (16 * u + 4 * g) * 4;
      PIN(pa); PIN(pw); PIN(pq); PIN(qo);
      if (w == 7) VMWAIT(5); else if (hiw) VMWAIT(0);
      STAMP(2);
      BARRIER();
      PAD(B1, B2);
      if (w == 6 && l < 8) __builtin_amdgcn_raw_buffer_store_b32(tagLo, rsW, WS_QH + (l * 16 + (cg >> 2)) * 128 + ((cg & 3) * 2 + kh) * 4, 0, AUX_SC1);
      s16x8 af[8];
#pragma unroll
      for (int ks = 0; ks < 8; ++ks) af[ks] = trfrag(pa + ks * 4096, 512);
      FENCE();
      f32x4 acc = zero4;
#pragma unroll
      for (int ks = 0; ks < 8; ++ks) acc = mfma(af[ks], bf[ks], acc);
      if (!d8) *(LAS f32x4*)pw = acc;
    }
    if (w == 7) VMWAIT(0);
    BARRIER();
    if (!hiw) {
      PAD(B2, BR);
      const f32x2 s = *(LAS f32x2*)pq + *(LAS f32x2*)(pq + 8 * 272);
      st16(rsW, qo, v4u{__float_as_uint(s[0]), tagLo, __float_as_uint(s[1]), tagHi});
      STAMP(3);
      nap<TOUCH_DELAY>();
      constexpr int T0 = 2 * V_OWN + TOUCH_EARLY;
      if (T0 + u < NTOUCH_V) touch<0>(rv, g * 16384 + (l & 15) * 128, kvb + (T0 + u) * 2048, lbase + L_ST + 1024);
      if (T0 + 4 + u < NTOUCH_V) touch<0>(rv, g * 16384 + (l & 15) * 128, kvb + (T0 + 4 + u) * 2048, lbase + L_ST + 1024);
      nap<WO_DELAY>();
      const rsrc_t ro_ = mkrs(wo, 4096 * 1024 * 2);
      const u32 vo = (l >> 5) * 2048 + (((((l & 31) >> 1) ^ (l >> 5)) << 1) | (l & 1)) * 16;
#pragma unroll
      for (int i = 0; i < 4; ++i) {
        const int h = 2 * u + (i >> 1), half = i & 1;
        const u32 vx = vo ^ (128 * half);
        dma4(ro_, vx, vx ^ 64, vx, vx ^ 64, ((kvo * 8 + h) * 128 + 16 * xcd + 8 * half) * 2048 + 512 * nq, 4096 - 1024, lbase + L_R0 + (8 * h + 4 * half) * 1024);
      }
      STAMP(4);
    } else if (w == 6) {
      if (MP_AT == 1) { nap<3>(); clear_mp(); }
    } else if (w == 5) {
      HOP(B6, BS);
    }
    const u32 one = (l & 8) ? 0u : ((l & 1) ? 0x3F800000u : 0x00003F80u);
    const int si = b8 >> 1;
    v4u sel = {si == 0 ? one : 0u, si == 1 ? one : 0u, si == 2 ? one : 0u, si == 3 ? one : 0u};
    u32 wb = WS_WP + j * 4096 + l * 16, hb = WS_H + j * 128 + l * 16, mb = WS_MP + j * MPJ + w * 2048 + l * 16, oo = b8 * 2048 + jcol * 2;
    {
      const int sh = l >> 5;
      u32 la = WS_LSE + w * 2048 + (l >> 2) * 128 + (l & 3) * 16;
      u32 bsrc = ((kvo * 4 + sh) * 4 + (b8 >> 1)) * 4;
      PIN(la); PIN(bsrc);
      u32 ob = WS_OP + (ksl * 8 + w) * 2048 + l * 16;
      LAS char* po = L + L_ACT + w * 288 + b8 * 32 + ((l >> 3) & 3) * 8 + sh * 4;
      u32 wpo = WS_WP + (4 * (8 * nq + w) + g) * 4096 + ksl * 128 + b8 * 16;
      LAS char* pb = L + L_ACT + b8 * 288 + 16 * g;
      LAS char* pa0 = L + L_R0 + (8 * g + q4) * 512 + (((2 * w) ^ fsw) << 5) + 8 * p4;
      PIN(ob); PIN(po); PIN(wpo); PIN(pb); PIN(pa0); PIN(sel); PIN(wb); PIN(hb); PIN(mb); PIN(oo);
      VMWAIT(0);
      STAMP(5);
      asm volatile("s_barrier" ::: "memory");
      PAD(B6, B7);
      v4u ea = ld16(rsW, la);
      v4u d0 = sent4, d1 = sent4;
      if (SPEC_O) { d0 = ld16(rsW, ob); d1 = ld16(rsW, ob + 1024); }
      s16x8 wf[2][4];
#pragma unroll
      for (int T = 0; T < 2; ++T)
#pragma unroll
        for (int ks = 0; ks < 4; ++ks) wf[T][ks] = trfrag((LAS char*)((u32)(size_t)pa0 ^ (32 * T)) + ks * 16384, 2048);
      FENCE();
      bool fresh = SPEC_O;
      POLL_LOOP {
        if (__builtin_amdgcn_ballot_w64(((ea.y ^ tagLo) | (ea.w ^ tagHi)) != 0u) == 0ull) break;
        fresh = false;
        nap<L_NAP>();
        RELOAD();
        ea = ld16(rsW, la);
      }
      STAMP(6);
      PAD(B7, B8);
      if (!fresh) { d0 = ld16(rsW, ob); d1 = ld16(rsW, ob + 1024); }
      FENCE();
      const int lx0 = __builtin_amdgcn_ds_bpermute(bsrc, ea.x), lz0 = __builtin_amdgcn_ds_bpermute(bsrc, ea.z);
      const int lx1 = __builtin_amdgcn_ds_bpermute(bsrc + 32, ea.x), lz1 = __builtin_amdgcn_ds_bpermute(bsrc + 32, ea.z);
      const float l0 = __int_as_float((l & 1) ? lz0 : lx0), l1 = __int_as_float((l & 1) ? lz1 : lx1);
      float mx = fmaxf(l0, l1);
      { auto t = __builtin_amdgcn_permlane32_swap(__float_as_uint(mx), __float_as_uint(mx), false, false); mx = fmaxf(__uint_as_float(t[0]), __uint_as_float(t[1])); }
      float w0 = __builtin_amdgcn_exp2f(l0 - mx), w1_ = __builtin_amdgcn_exp2f(l1 - mx);
      float tot = w0 + w1_;
      { auto t = __builtin_amdgcn_permlane32_swap(__float_as_uint(tot), __float_as_uint(tot), false, false); tot = __uint_as_float(t[0]) + __uint_as_float(t[1]); }
      const float it_ = __builtin_amdgcn_rcpf(tot);
      w0 *= it_; w1_ *= it_;
      PIN(w0); PIN(w1_);
      POLL_LOOP {
        if (__builtin_amdgcn_ballot_w64((((d0.y ^ tagLo) | (d0.w ^ tagHi)) | ((d1.y ^ tagLo) | (d1.w ^ tagHi))) != 0u) == 0ull) break;
        RELOAD();
        d0 = ld16(rsW, ob); d1 = ld16(rsW, ob + 1024);
      }
      PAD(B8, B9);
      const float v0 = fmaf(w0, bflo(d0.x), w1_ * bflo(d1.x)), v1 = fmaf(w0, bfhi(d0.x), w1_ * bfhi(d1.x));
      const float v2 = fmaf(w0, bflo(d0.z), w1_ * bflo(d1.z)), v3 = fmaf(w0, bfhi(d0.z), w1_ * bfhi(d1.z));
      auto t0 = __builtin_amdgcn_permlane32_swap(__float_as_uint(v0), __float_as_uint(v2), false, false);
      auto t1 = __builtin_amdgcn_permlane32_swap(__float_as_uint(v1), __float_as_uint(v3), false, false);
      *(LAS u32*)po = pk2(__uint_as_float(t0[0]) + __uint_as_float(t0[1]), __uint_as_float(t1[0]) + __uint_as_float(t1[1]));
      STAMP(8);
      BARRIER();
      s16x8 bf[4];
#pragma unroll
      for (int ks = 0; ks < 4; ++ks) bf[ks] = *(LAS s16x8*)(pb + ks * 64);
      FENCE();
      f32x4 a0 = zero4, a1 = zero4;
#pragma unroll
      for (int ks = 0; ks < 4; ++ks) { a0 = mfma(wf[0][ks], bf[ks], a0); a1 = mfma(wf[1][ks], bf[ks], a1); }
      if (!d8) st16(rsW, wpo, v4u{pk2(a0[0], a0[1]), pk2(a0[2], a0[3]), pk2(a1[0], a1[1]), pk2(a1[2], a1[3])});
      STAMP(9);
    }
    PAD(B9, B10);
    u32 h01 = 0, h23 = 0;
    if (w == 0) {
      if (MP_AT == 2) clear_mp(); else nap<R_DELAY>();
      f32x4 acc = zero4;
      POLL_LOOP {
        RELOAD();
        v4u d[4];
#pragma unroll
        for (int i = 0; i < 4; ++i) d[i] = ld16(rsW, wb + i * 1024);
        FENCE();
        acc = zero4;
#pragma unroll
        for (int i = 0; i < 4; ++i) acc = mfma(sel, d[i], acc);
        if (wave_clean(acc[0])) break;
      }
      PAD(B10, B11);
      const u32 a01 = pk2(acc[0] + dpp<DPP_R8>(acc[0]), acc[1] + dpp<DPP_R8>(acc[1]));
      const u32 a23 = pk2(acc[2] + dpp<DPP_R8>(acc[2]), acc[3] + dpp<DPP_R8>(acc[3]));
      h01 = pk2(bflo(xres.x) + bflo(a01), bfhi(xres.x) + bfhi(a01));
      h23 = pk2(bflo(xres.y) + bflo(a23), bfhi(xres.y) + bfhi(a23));
      auto s0 = __builtin_amdgcn_permlane16_swap(h01, h01, false, false);
      auto s1 = __builtin_amdgcn_permlane16_swap(h23, h23, false, false);
      if (MP_AT == 2) VMWAIT(0);
      if (l < 8) st16(rsW, hb, v4u{s0[0], s1[0], s0[1], s1[1]});
      STAMP(10);
    } else if (w == 7) {
      if (l == 0) __builtin_amdgcn_raw_buffer_store_b64(v2u{tagLo, (u32)__builtin_amdgcn_s_memrealtime()}, rsW, WS_FLAG + j * 128 + 64, 0, AUX_SC1);
    }
    asm volatile("s_barrier" ::: "memory");
    PAD(B11, B12);
    LAS char* pfs = L + L_P + (l & 31) * 16;
    if (w < 7) {
      nap<F_DELAY>();
      f32x4 acc = zero4;
      POLL_LOOP {
        RELOAD();
        const v4u d0 = ld16(rsW, mb), d1 = ld16(rsW, mb + 1024);
        FENCE();
        acc = mfma(sel, d1, mfma(sel, d0, zero4));
        if (wave_clean(acc[0])) break;
        nap<F_NAP>();
      }
      STAMP(13);
      if (l < 32) *(LAS f32x4*)(pfs + w * 512) = acc;
    }
    BARRIER();
    PAD(B12, BS);
    if (w == 0) {
      f32x4 p[7];
#pragma unroll
      for (int i = 0; i < 7; ++i) p[i] = *(LAS f32x4*)(pfs + i * 512);
      FENCE();
      const f32x4 acc = ((p[0] + p[1]) + (p[2] + p[3])) + ((p[4] + p[5]) + p[6]);
      const u32 y01 = pk2(acc[0] + dpp<DPP_R8>(acc[0]), acc[1] + dpp<DPP_R8>(acc[1]));
      const u32 y23 = pk2(acc[2] + dpp<DPP_R8>(acc[2]), acc[3] + dpp<DPP_R8>(acc[3]));
      const v2u r = {pk2(bflo(h01) + bflo(y01), bfhi(h01) + bfhi(y01)), pk2(bflo(h23) + bflo(y23), bfhi(h23) + bfhi(y23))};
      if ((l & 40) == 0) __builtin_amdgcn_raw_buffer_store_b64(r, mkrs(out, 8 * 1024 * 2), oo, 0, 0);
      if (j == 0 && l == 0) __builtin_amdgcn_raw_buffer_store_b64(v2u{tagLo, tagHi}, rsW, WS_EPOCH, 0, AUX_SC1);
      STAMP(11);
    }
  STAMP_FLUSH();
    return;
  }

  const int s = sl & 3, b = bk >> 2, kvh = bk & 3;
  const int m = xcd * 14 + sl;
  const rsrc_t rv = mkrs(vc, 8 * 4 * 4096 * 128 * 2);
  if (hiw) {
#pragma unroll
    for (int i = 0; i < V_OWN; ++i) dma4c(rv, vv, vv, vv ^ 128, vv ^ 128, kvb + u * 16384 + i * 4096, lbase + L_R1 + u * 16384 + i * 4096);
  } else {
    if (tid < 32) *(LAS u32*)(L + L_FL + tid * 4) = 0u;
    if (w == 3) HOP(A0, AR);
  }
  LOAD_TAG();
  BARRIER();
  if (hiw) {
    PAD(A0, A1);
    const rsrc_t r1 = mkrs(w1, 1024 * 3584 * 2), r2 = mkrs(w2, sl < 14 ? 3584 * 1024 * 2 : 0);
    const u32 v2o = vv + g * (2048 - 256);
    const u32 v1 = (l >> 2) * 7168 + ((((l >> 1) & 1) ^ ((l >> 4) & 1)) << 5) + (l & 1) * 16;
    auto load_w2 = [&](int i) {
      const int t = 2 * u + (i & 1), half = i >> 1;
      dma4(r2, v2o, v2o, v2o ^ 128, v2o ^ 128, (32 * m + 16 * half) * 2048 + 256 * t, 4 * 2048 - 1024, lbase + L_R0 + (8 * t + 4 * half) * 1024);
    };
    asm volatile("s_waitcnt vmcnt(%0)" :: "i"(4 * V_OWN) : "memory");
    LSET(F_KR + u * 4);
    STAMP(2);
    LWAIT4(V_GATE ? F_Q : F_KD);
#pragma unroll
    for (int i = V_OWN; i < 4; ++i) dma4c(rv, vv, vv, vv ^ 128, vv ^ 128, kvb + u * 16384 + i * 4096, lbase + L_R1 + u * 16384 + i * 4096);
    STAMP(3);
    if (!V_GATE) LWAIT4(F_Q);
#pragma unroll
    for (int i = 0; i < W2_PRE0; ++i) load_w2(i);
    asm volatile("s_waitcnt vmcnt(%0)" :: "i"(4 * W2_PRE0) : "memory");
    LSET(F_V + u * 4);
    STAMP(5);
#pragma unroll
    for (int i = W2_PRE0; i < W2_PRE; ++i) load_w2(i);
    LWAIT4(F_O);
    nap<AM_DELAY>();
    if (W1C3 && u == 0) {
      const int lc = (3 - (int)(((u32)(size_t)w1) >> 7) - 7 * xcd) & 7;
      if (lc < 7) touch<0>(r1, l * 7168, sl * 64 * 7168 + (xcd * 7 + lc) * 128, lbase + L_ST + 1024);
    }
    if (sl < 14) {
      const int h0 = (m & 1) * 512 + 128 * u, h1 = h0 ^ 512;
#pragma unroll
      for (int i = W2_PRE; i < 2; ++i) load_w2(i);
#pragma unroll
      for (int i = 0; i < 2; ++i) dma4<0>(r1, v1, v1, v1, v1, (h0 + 64 * i) * 7168 + 64 * m, 16 * 7168 - 1024, lbase + L_R1 + (h0 + 64 * i) * 64);
      STAMP(4);
#pragma unroll
      for (int i = (W2_PRE > 2 ? W2_PRE : 2); i < 4; ++i) load_w2(i);
      STAMP(6);
#pragma unroll
      for (int i = 0; i < 2; ++i) dma4<NT_W1B>(r1, v1, v1, v1, v1, (h1 + 64 * i) * 7168 + 64 * m, 16 * 7168 - 1024, lbase + L_R1 + (h1 + 64 * i) * 64);
      STAMP(7);
    }
  } else {
    PAD(A1, A2);
    LAS char* pk = L + L_R0 + (64 * u + (l & 15)) * 256;
    LAS char* pb = L + L_ACT + b8 * 288 + 16 * g;
    LAS char* pp = L + L_P + b8 * 544 + (64 * u + 16 * d8 + 4 * g) * 2;
    LAS char* ps = L + L_ST + b8 * 16;
    LAS char* pa = L + L_R1 + (8 * g + q4) * 256 + (((2 * u) ^ fsw) << 5) + 8 * p4;
    LAS char* pf_ = L + L_P + b8 * 544 + 16 * g;
    u32 oo = WS_OP + (((kvh * 8 + 2 * u + d8) * 8 + b) * 4 + s) * 512 + g * 128 + b8 * 16;
    const u32 qb = WS_QG + bk * 16384 + w * 2048 + l * 16;
    PIN(pb); PIN(pp); PIN(ps); PIN(pa); PIN(pf_); PIN(oo);
    hint_wait8(ws + WS_QH + (b * 16 + kvh * 4 + u) * 128, tagLo);
    nap<QH_DELAY>();
    STAMP(12);
    v4u d[4];
#pragma unroll
    for (int i = 0; i < 4; ++i) d[i] = ld16(rsW, qb + (i >> 1) * 8192 + (i & 1) * 1024);
    LSET(F_KD + u * 4);
    LWAIT1(F_KR + u * 4);
    s16x8 kf[4][4];
#pragma unroll
    for (int ks = 0; ks < 4; ++ks) {
      const int pos = ((4 * ks + g) ^ (l & 15)) * 16;
#pragma unroll
      for (int T = 0; T < 4; ++T) kf[T][ks] = *(LAS s16x8*)(pk + T * 4096 + pos);
    }
    FENCE();
    PAD(A2, A3);
    POLL_LOOP {
      u32 bad = 0;
#pragma unroll
      for (int i = 0; i < 4; ++i) bad |= (d[i].y ^ tagLo) | (d[i].w ^ tagHi);
      if (__builtin_amdgcn_ballot_w64(bad != 0u) == 0ull) break;
      nap<Q_NAP>();
      RELOAD();
#pragma unroll
      for (int i = 0; i < 4; ++i) d[i] = ld16(rsW, qb + (i >> 1) * 8192 + (i & 1) * 1024);
    }
#pragma unroll
    for (int i = 0; i < 2; ++i)
      *(LAS u32*)(L + L_ACT + (2 * w + i) * 288 + l * 4) = pk2(__uint_as_float(d[i].x) + __uint_as_float(d[i + 2].x), __uint_as_float(d[i].z) + __uint_as_float(d[i + 2].z));
    LSET(F_Q + u * 4);
    STAMP(2);
    LWAIT4(F_Q);
    STAMP(3);
    PAD(A3, A4);
    s16x8 bf[4];
#pragma unroll
    for (int ks = 0; ks < 4; ++ks) bf[ks] = *(LAS s16x8*)(pb + ks * 64);
    FENCE();
    f32x4 ac[4] = {zero4, zero4, zero4, zero4};
#pragma unroll
    for (int T = 0; T < 4; T += 2)
#pragma unroll
      for (int ks = 0; ks < 4; ++ks) { ac[T] = mfma(kf[T][ks], bf[ks], ac[T]); ac[T + 1] = mfma(kf[T + 1][ks], bf[ks], ac[T + 1]); }
    float sc[8];
#pragma unroll
    for (int pr = 0; pr < 2; ++pr)
#pragma unroll
      for (int r = 0; r < 4; r += 2) {
        const u32 t = pk2(d8 ? ac[2 * pr + 1][r] : ac[2 * pr][r], d8 ? ac[2 * pr + 1][r + 1] : ac[2 * pr][r + 1]);
        const u32 t2 = pk2(bflo(t) * 0.08838834764831845f, bfhi(t) * 0.08838834764831845f);
        sc[4 * pr + r] = bflo(t2); sc[4 * pr + r + 1] = bfhi(t2);
      }
    float mx = fmaxf(fmaxf(fmaxf(sc[0], sc[1]), fmaxf(sc[2], sc[3])), fmaxf(fmaxf(sc[4], sc[5]), fmaxf(sc[6], sc[7])));
    mx = fmaxf(mx, dpp<DPP_R8>(mx));
    mx = max16_32(mx) * LOG2E;
    PAD(A4, A5);
    float pr_[8];
#pragma unroll
    for (int r = 0; r < 8; ++r) pr_[r] = __builtin_amdgcn_exp2f(fmaf(sc[r], LOG2E, -mx));
    *(LAS v2u*)pp = v2u{pk2(pr_[0], pr_[1]), pk2(pr_[2], pr_[3])};
    *(LAS v2u*)(pp + 64) = v2u{pk2(pr_[4], pr_[5]), pk2(pr_[6], pr_[7])};
    float sm = ((pr_[0] + pr_[1]) + (pr_[2] + pr_[3])) + ((pr_[4] + pr_[5]) + (pr_[6] + pr_[7]));
    sm += dpp<DPP_R8>(sm);
    sm = sum16_32(sm);
    if (l < 8) { *(LAS float*)(ps + u * 4) = mx; *(LAS float*)(ps + 128 + u * 4) = sm; }
    LSET(F_P + u * 4);
    STAMP(4);
    LWAIT4(F_P);
    PAD(A5, A6);
    const f32x4 m4 = *(LAS f32x4*)ps, s4 = *(LAS f32x4*)(ps + 128);
    s16x8 pf[8], vf[2][8];
#pragma unroll
    for (int ks = 0; ks < 8; ++ks) pf[ks] = *(LAS s16x8*)(pf_ + ks * 64);
    v4u vfl = *(volatile LAS v4u*)(L + L_FL + F_V);
    FENCE();
    const float mm = fmaxf(fmaxf(m4[0], m4[1]), fmaxf(m4[2], m4[3]));
    f32x4 f;
#pragma unroll
    for (int i = 0; i < 4; ++i) f[i] = __builtin_amdgcn_exp2f(m4[i] - mm);
    const float tot = fmaf(f[0], s4[0], f[1] * s4[1]) + fmaf(f[2], s4[2], f[3] * s4[3]);
    if (w == 3 && l < 8)
      __builtin_amdgcn_raw_buffer_store_b64(v2u{__float_as_uint(mm + __builtin_amdgcn_logf(tot)), (l & 1) ? tagHi : tagLo}, rsW, WS_LSE + (bk * 4 + s) * 128 + l * 8, 0, AUX_SC1);
    if (__builtin_amdgcn_readfirstlane(vfl.x & vfl.y & vfl.z & vfl.w) == 0u) LWAIT4(F_V);
    asm volatile("" ::: "memory");
    STAMP(5);
    PAD(A6, A7);
#pragma unroll
    for (int ks = 0; ks < 8; ++ks) {
      vf[0][ks] = trfrag(pa + ks * 8192, 1024);
      vf[1][ks] = trfrag((LAS char*)((u32)(size_t)pa ^ 32) + ks * 8192, 1024);
    }
    FENCE();
    f *= __builtin_amdgcn_rcpf(tot);
    f32x4 o = zero4;
#pragma unroll
    for (int i = 0; i < 4; ++i) {
      const f32x4 a0 = mfma(vf[0][2 * i + 1], pf[2 * i + 1], mfma(vf[0][2 * i], pf[2 * i], zero4));
      const f32x4 a1 = mfma(vf[1][2 * i + 1], pf[2 * i + 1], mfma(vf[1][2 * i], pf[2 * i], zero4));
#pragma unroll
      for (int r = 0; r < 4; ++r) o[r] = fmaf(d8 ? a1[r] : a0[r], f[i], o[r]);
    }
    st16(rsW, oo, v4u{pk2(o[0], o[1]), tagLo, pk2(o[2], o[3]), tagHi});
    LSET(F_O + u * 4);
    STAMP(6);
  }
  if (sl >= 14) { STAMP_FLUSH(); return; }
  PAD(A7, A8);
  s16x8 f2[8];
  LAS char* pr = L + L_P + b8 * 144 + g * 32 + d8 * 16;
  LAS char* pc = L + L_P + 8 * 144 + g * 32 + d8 * 16;
  LAS char* psr = L + L_ST + d8 * 128 + b8 * 16;
  LAS char* pf2 = L + L_R0 + w * 8192 + (8 * g + q4) * 256 + 8 * p4;
  u32 mo = WS_MP + (4 * (4 * w + d8) + g) * MPJ + m * 128 + b8 * 16;
  PIN(mo); PIN(psr); PIN(pr); PIN(pc); PIN(pf2);
  if (hiw) {
    const int h0 = (m & 1) * 512 + 128 * u, h1 = h0 ^ 512;
    LAS char* pa1 = L + L_R1 + (4 * g + q4) * 64 + ((g & 1) << 5) + 8 * p4;
    LAS char* pz = L + L_P + (u * 9 + (d8 ? 8 : b8)) * 144 + 16 * g;
    LAS char* pst = L + L_ST + (g < 2 ? 128 : 0) + b8 * 16 + u * 4;
    u32 hb0 = WS_H + h0 * 16 + g * 128 + b8 * 16, hb1 = hb0 ^ 8192;
    PIN(pa1); PIN(pz); PIN(pst); PIN(hb0); PIN(hb1);
    v4u hf[8];
#pragma unroll
    for (int i = 0; i < 8; ++i) { hf[i] = v4u{0x3F803F80u, 0x3F803F80u, 0x3F803F80u, 0x3F803F80u}; PIN(hf[i]); }
    VMWAIT(8);
    LSET(F_W + u * 4);
    PAD(A8, A9);
    s16x8 f1[2][8];
#pragma unroll
    for (int nt = 0; nt < 2; ++nt)
#pragma unroll
      for (int ks = 0; ks < 4; ++ks) f1[nt][ks] = trfrag((LAS char*)((u32)(size_t)pa1 ^ (32 * nt)) + h0 * 64 + ks * 2048, 1024);
    LWAIT1(F_W + (w >> 1) * 4);
#pragma unroll
    for (int T = 0; T < 8; ++T) f2[T] = trfrag(pf2 + ((T ^ fsw) << 5), 1024);
    LWAIT1(F_H);
    STAMP(11);
    if (!d8) {
#pragma unroll
      for (int i = 0; i < 8; ++i) hf[i] = ld16(rsW, (i < 4 ? hb0 : hb1) + (i & 3) * 512);
    }
    VMWAIT(8);
    STAMP(8);
    PAD(A9, A10);
#pragma unroll
    for (int nt = 0; nt < 2; ++nt)
#pragma unroll
      for (int ks = 0; ks < 4; ++ks) f1[nt][4 + ks] = trfrag((LAS char*)((u32)(size_t)pa1 ^ (32 * nt)) + h1 * 64 + ks * 2048, 1024);
    FENCE();
    f32x4 z0 = zero4, z1 = zero4, aQ = zero4;
    PAD(A10, A11);
    POLL_LOOP {
      z0 = zero4; z1 = zero4; aQ = zero4;
#pragma unroll
      for (int i = 0; i < 8; ++i) aQ = mfma(hf[i], hf[i], aQ);
#pragma unroll
      for (int i = 0; i < 8; ++i) { z0 = mfma(f1[0][i], __builtin_bit_cast(s16x8, hf[i]), z0); z1 = mfma(f1[1][i], __builtin_bit_cast(s16x8, hf[i]), z1); }
      PIN(z0); PIN(z1);
      if (wave_clean(aQ[0])) break;
      POLL_LOOP {
        RELOAD();
        if (!d8) {
#pragma unroll
          for (int i = 0; i < 8; ++i) hf[i] = ld16(rsW, (i < 4 ? hb0 : hb1) + (i & 3) * 512);
        }
        u32 mxv = 0;
#pragma unroll
        for (int i = 0; i < 8; ++i) mxv = max(mxv, max4(hf[i]));
        if (__builtin_amdgcn_ballot_w64(mxv == SENT) == 0ull) break;
      }
      it += SPIN_CAP / 2;
    }
    STAMP(9);
    PAD(A11, A12);
    const float sv = (p4 & 2) ? ((p4 & 1) ? aQ[3] : aQ[2]) : ((p4 & 1) ? aQ[1] : aQ[0]);
    if (!d8 && (g == 2 || g == (b8 >> 2))) *(LAS float*)pst = sv;
    if (l < 9 + 16 * g && l >= 16 * g) { *(LAS f32x4*)pz = z0; *(LAS f32x4*)(pz + 64) = z1; }
  } else {
    if (w == 0) {
      POLL_LOOP {
        RELOAD();
        const v4u fl = ld16(rsW, WS_LSE + ((b * 4 + (l & 3)) * 4 + s) * 128);
        if (__builtin_amdgcn_ballot_w64(((fl.y ^ tagLo) | (fl.w ^ tagHi)) != 0u) == 0ull) break;
        nap<8>();
      }
      hint_wait(ws + WS_FLAG + (c - 128) * 128 + 64, tagLo, H_DELAY, 0);
      LSET(F_H);
    }
    LWAIT1(F_W + (w >> 1) * 4);
#pragma unroll
    for (int T = 0; T < 8; ++T) f2[T] = trfrag(pf2 + ((T ^ fsw) << 5), 1024);
  }
#pragma unroll
  for (int T = 0; T < 8; ++T) PIN(f2[T]);
  BARRIER();
  PAD(A12, A13);
  {
    const f32x4 t0 = *(LAS f32x4*)psr;
    const f32x4 z0 = *(LAS f32x4*)pr, z1 = *(LAS f32x4*)(pr + 9 * 144), z2 = *(LAS f32x4*)(pr + 18 * 144), z3 = *(LAS f32x4*)(pr + 27 * 144);
    const f32x4 c0 = *(LAS f32x4*)pc, c1 = *(LAS f32x4*)(pc + 9 * 144), c2 = *(LAS f32x4*)(pc + 18 * 144), c3 = *(LAS f32x4*)(pc + 27 * 144);
    FENCE();
    const float mine = ((t0[0] + t0[1]) + (t0[2] + t0[3])) * (1.f / 1024.f);
    const float other = dpp<DPP_R8>(mine);
    const float mean = d8 ? other : mine, ex2 = d8 ? mine : other;
    const float var = fmaxf(fmaf(-mean, mean, ex2), 0.f);
    const u32 mv = pk2(mean, var);
    const float den = rbf(__builtin_amdgcn_sqrtf(rbf(bfhi(mv) + 1e-5f)));
    const float inv = __builtin_amdgcn_rcpf(den);
    const float mb = bflo(mv);
    float gv[4];
#pragma unroll
    for (int i = 0; i < 4; ++i) gv[i] = fmaf(-mb, (c0[i] + c1[i]) + (c2[i] + c3[i]), (z0[i] + z1[i]) + (z2[i] + z3[i])) * inv;
    const s16x2 zz = {0, 0};
    const int g0 = __builtin_bit_cast(int, __builtin_elementwise_max(__builtin_bit_cast(s16x2, pk2(gv[0], gv[1])), zz));
    const int g1 = __builtin_bit_cast(int, __builtin_elementwise_max(__builtin_bit_cast(s16x2, pk2(gv[2], gv[3])), zz));
    PAD(A13, AR);
    v4u B0, B1;
    B0[0] = __builtin_amdgcn_update_dpp(0, g0, DPP_ID, 0xf, 0x3, false);
    B0[1] = __builtin_amdgcn_update_dpp(0, g1, DPP_ID, 0xf, 0x3, false);
    B0[2] = __builtin_amdgcn_update_dpp(0, g0, DPP_R8, 0xf, 0x3, false);
    B0[3] = __builtin_amdgcn_update_dpp(0, g1, DPP_R8, 0xf, 0x3, false);
    B1[0] = __builtin_amdgcn_update_dpp(0, g0, DPP_R8, 0xf, 0xc, false);
    B1[1] = __builtin_amdgcn_update_dpp(0, g1, DPP_R8, 0xf, 0xc, false);
    B1[2] = __builtin_amdgcn_update_dpp(0, g0, DPP_ID, 0xf, 0xc, false);
    B1[3] = __builtin_amdgcn_update_dpp(0, g1, DPP_ID, 0xf, 0xc, false);
#pragma unroll
    for (int i = 0; i < 2; ++i) {
      f32x4 u0 = mfma(f2[4 * i], __builtin_bit_cast(s16x8, B0), zero4), u1 = mfma(f2[4 * i + 1], __builtin_bit_cast(s16x8, B0), zero4);
      u0 = mfma(f2[4 * i + 2], __builtin_bit_cast(s16x8, B1), u0);
      u1 = mfma(f2[4 * i + 3], __builtin_bit_cast(s16x8, B1), u1);
      __builtin_amdgcn_raw_buffer_store_b128(v4u{pk2(u0[0], u0[1]), pk2(u0[2], u0[3]), pk2(u1[0], u1[1]), pk2(u1[2], u1[3])}, rsW, mo, i * 8 * MPJ, AUX_SC1);
    }
  }
  STAMP(10);
  STAMP_FLUSH();
}

extern "C" void kernel_launch(void* const* d_in, const int* in_sizes, int n_in, void* d_out, int out_size, void* d_ws, size_t ws_size,
                              hipStream_t stream) {
  (void)hipFuncSetAttribute((const void*)fused_decode_layer, hipFuncAttributeMaxDynamicSharedMemorySize, L_BYTES);
  fused_decode_layer<<<NWG, NTHR, L_BYTES, stream>>>((const char*)d_in[0], (const char*)d_in[1], (const char*)d_in[2], (const char*)d_in[3],
                                                     (const char*)d_in[4], (const char*)d_in[6], (const char*)d_in[7], (char*)d_out, (char*)d_ws);
}
```

```cpp
#include <hip/hip_runtime.h>
#include <hip/hip_bf16.h>
#include <stdint.h>

#define DIAG 0
#define TOUCH_DELAY 8
#define QH_DELAY 4
#define O_DELAY 62
#define OQ_LEAD 42
#define OV_LEAD 16
#define RG_DELAY 95
#define H_DELAY 225
#define HR_DELAY 100
#define F_DELAY 92
#define OP_NAP 50
#define CODE_LINES 15
#define V_OWN 2
#define W1E 2
#define W1_PRE 0
#define W2_MID 1
#define W2_FIRST 0
#define SPARE_N 2
#define MP_AT 1
#define PRE_AT 0
#define C3_PRE 1
#define VTNT 0
#define W2NT 0
#define WQPOL ""
#define ZM 0
#define SZ_W1 ((ZM & 1) ? 0 : 1024 * 3584 * 2)
#define SZ_W2 ((ZM & 2) ? 0 : 3584 * 1024 * 2)
#define SZ_WO ((ZM & 4) ? 0 : 4096 * 1024 * 2)
#define SZ_V ((ZM & 8) ? 0 : 8 * 4 * 4096 * 128 * 2)
#define SZ_K ((ZM & 16) ? 0 : 8 * 4 * 4096 * 128 * 2)
#define SZ_WQ ((ZM & 32) ? 0 : 1024 * 4096 * 2)

#define LAS __attribute__((address_space(3)))
typedef unsigned u32;
typedef unsigned long long u64;
typedef unsigned v4u __attribute__((ext_vector_type(4)));
typedef unsigned v2u __attribute__((ext_vector_type(2)));
typedef float f32x4 __attribute__((ext_vector_type(4)));
typedef float f32x2 __attribute__((ext_vector_type(2)));
typedef short s16x2 __attribute__((ext_vector_type(2)));
typedef short s16x4 __attribute__((ext_vector_type(4)));
typedef short s16x8 __attribute__((ext_vector_type(8)));
typedef __bf16 bf16x2 __attribute__((ext_vector_type(2)));
typedef __bf16 bf16x8 __attribute__((ext_vector_type(8)));
typedef __amdgpu_buffer_rsrc_t rsrc_t;

typedef unsigned s8u __attribute__((ext_vector_type(8)));

namespace {

constexpr int NWG = 256, NTHR = 512;
constexpr u32 SENT = 0xFFFFFFFFu;
constexpr int AUX_SC1 = 16;
constexpr int SPIN_CAP = 1 << 18;
constexpr int LDS_CAP = 1 << 25;
constexpr float LOG2E = 1.4426950408889634f;

constexpr int WS_EPOCH = 0;
constexpr int WS_H = 32768;
constexpr int WS_QG = 65536;
constexpr int WS_QH = 0xA0000;
constexpr int WS_OH = 0xB0000;
constexpr int WS_GH = 0xC0000;
constexpr int WS_FH = 0xD0000;
constexpr int WS_OP = 1 << 20;
constexpr int WS_WP = WS_OP + 524288;
constexpr int WS_MP = WS_WP + 524288;
constexpr int MPJ = 112 * 128;
constexpr int WS_END = WS_MP + 128 * MPJ;
constexpr int WS_STAMP = 8 << 20;

constexpr int L_R0 = 0, L_R1 = 65536, L_MISC = 131072;
constexpr int L_ACT = L_MISC;
constexpr int L_P = L_ACT + 8 * 1056;
constexpr int L_ST = L_P + 5376;
constexpr int L_FL = L_ST + 512;
constexpr int F_KR = 0, F_KD = 16, F_V = 32, F_H = 48, F_W = 64;
constexpr int L_JUNK = L_ST + 1024;
constexpr int L_BYTES = L_ST + 2048;

__device__ __forceinline__ float bflo(u32 v) { return __uint_as_float(v << 16); }
__device__ __forceinline__ float bfhi(u32 v) { return __uint_as_float(v & 0xFFFF0000u); }
__device__ __forceinline__ u32 pk2(float lo, float hi) {
  bf16x2 r = {(__bf16)lo, (__bf16)hi};
  return __builtin_bit_cast(u32, r);
}
__device__ __forceinline__ float rbf(float x) { return (float)(__bf16)x; }

template <int CTRL> __device__ __forceinline__ float dpp(float x) {
  return __builtin_bit_cast(float, __builtin_amdgcn_mov_dpp(__builtin_bit_cast(int, x), CTRL, 0xf, 0xf, true));
}
constexpr int DPP_X1 = 0xB1, DPP_X2 = 0x4E, DPP_HM = 0x141, DPP_M = 0x140, DPP_R8 = 0x128, DPP_ID = 0xE4;
__device__ __forceinline__ float sum16_32(float x) {
  auto s = __builtin_amdgcn_permlane16_swap(__float_as_uint(x), __float_as_uint(x), false, false);
  x = __uint_as_float(s[0]) + __uint_as_float(s[1]);
  auto t = __builtin_amdgcn_permlane32_swap(__float_as_uint(x), __float_as_uint(x), false, false);
  return __uint_as_float(t[0]) + __uint_as_float(t[1]);
}
__device__ __forceinline__ float wave_sum(float x) {
  x += dpp<DPP_X1>(x); x += dpp<DPP_X2>(x); x += dpp<DPP_HM>(x); x += dpp<DPP_M>(x);
  return sum16_32(x);
}
__device__ __forceinline__ f32x4 mfma(s16x8 a, s16x8 b, f32x4 c) {
  return __builtin_amdgcn_mfma_f32_16x16x32_bf16(__builtin_bit_cast(bf16x8, a), __builtin_bit_cast(bf16x8, b), c, 0, 0, 0);
}
__device__ __forceinline__ f32x4 mfma(v4u a, v4u b, f32x4 c) {
  return __builtin_amdgcn_mfma_f32_16x16x32_bf16(__builtin_bit_cast(bf16x8, a), __builtin_bit_cast(bf16x8, b), c, 0, 0, 0);
}
__device__ __forceinline__ s16x8 trfrag(LAS char* p, int half_off) {
  typedef short v4i16_t __attribute__((ext_vector_type(4)));
  s16x4 lo = __builtin_amdgcn_ds_read_tr16_b64_v4i16((LAS v4i16_t*)p);
  s16x4 hi = __builtin_amdgcn_ds_read_tr16_b64_v4i16((LAS v4i16_t*)(p + half_off));
  return __builtin_shufflevector(lo, hi, 0, 1, 2, 3, 4, 5, 6, 7);
}
#define DMA4_ASM(POL) \
  asm volatile("s_mov_b32 m0, %6\n\ts_nop 0\n\t" \
               "buffer_load_dwordx4 %1, %5, %0 offen " POL " lds\n\ts_add_u32 %0, %0, %7\n\t" \
               "buffer_load_dwordx4 %2, %5, %0 offen offset:1024 " POL " lds\n\ts_add_u32 %0, %0, %7\n\t" \
               "buffer_load_dwordx4 %3, %5, %0 offen offset:2048 " POL " lds\n\ts_add_u32 %0, %0, %7\n\t" \
               "buffer_load_dwordx4 %4, %5, %0 offen offset:3072 " POL " lds" \
               : "+s"(soff) : "v"(v0), "v"(v1), "v"(v2), "v"(v3), "s"(rs), "s"(lds), "s"(sdelta) : "memory", "scc")
template <int NT = 1>
__device__ __forceinline__ u32 dma4(rsrc_t rs, u32 v0, u32 v1, u32 v2, u32 v3, u32 soff, u32 sdelta, u32 lds) {
  if constexpr (NT) DMA4_ASM("nt"); else DMA4_ASM("");
  return soff;
}
__device__ __forceinline__ void dma4c(rsrc_t rs, u32 v0, u32 v1, u32 v2, u32 v3, u32 soff, u32 lds) {
  asm volatile("s_mov_b32 m0, %6\n\ts_nop 0\n\t"
               "buffer_load_dwordx4 %1, %5, %0 offen nt lds\n\t"
               "buffer_load_dwordx4 %2, %5, %0 offen offset:1024 nt lds\n\t"
               "buffer_load_dwordx4 %3, %5, %0 offen offset:2048 nt lds\n\t"
               "buffer_load_dwordx4 %4, %5, %0 offen offset:3072 nt lds"
               :: "s"(soff), "v"(v0), "v"(v1), "v"(v2), "v"(v3), "s"(rs), "s"(lds) : "memory");
}
template <int NT = 1>
__device__ __forceinline__ void touch(rsrc_t rs, u32 voff, u32 soff, u32 lds) {
  if constexpr (NT) asm volatile("s_mov_b32 m0, %0\n\ts_nop 0\n\tbuffer_load_dword %1, %2, %3 offen nt lds" ::"s"(lds), "v"(voff), "s"(rs), "s"(soff) : "memory");
  else asm volatile("s_mov_b32 m0, %0\n\ts_nop 0\n\tbuffer_load_dword %1, %2, %3 offen lds" ::"s"(lds), "v"(voff), "s"(rs), "s"(soff) : "memory");
}
#define LOAD_TAG() do { u64 e_; asm volatile("s_load_dwordx2 %0, %1, 0x0\n\ts_waitcnt lgkmcnt(0)" : "=s"(e_) : "s"(ws) : "memory"); tagLo = (u32)e_ + 1u; tagHi = (u32)(e_ >> 32) + 1u; } while (0)
#define POLL_LOOP _Pragma("nounroll") for (int it = 0; it < SPIN_CAP; ++it)
#define RELOAD() asm volatile("" ::: "memory")
#define BARRIER() asm volatile("s_waitcnt lgkmcnt(0)\n\ts_barrier" ::: "memory")
#define RAWBAR() asm volatile("s_barrier" ::: "memory")
#define VMWAIT(n) asm volatile("s_waitcnt vmcnt(%0)" :: "i"(n) : "memory")
#define FENCE() __builtin_amdgcn_sched_barrier(0)
#define PIN(x) asm volatile("" : "+v"(x))
template <int N> __device__ __forceinline__ void nap() {
  if constexpr (N > 0) { __builtin_amdgcn_s_sleep(N > 127 ? 127 : N); nap<(N > 127 ? N - 127 : 0)>(); }
}
#define LSET(off) do { RELOAD(); *(volatile LAS u32*)(L + L_FL + (off)) = 1u; } while (0)
#define LWAIT1(off) do { _Pragma("nounroll") for (int it_ = 0; it_ < LDS_CAP; ++it_) { \
    if (__builtin_amdgcn_readfirstlane(*(volatile LAS u32*)(L + L_FL + (off))) != 0u) break; } RELOAD(); } while (0)
#define LWAIT4(off) do { _Pragma("nounroll") for (int it_ = 0; it_ < LDS_CAP; ++it_) { const v4u f_ = *(volatile LAS v4u*)(L + L_FL + (off)); \
    if (__builtin_amdgcn_readfirstlane(f_.x & f_.y & f_.z & f_.w) != 0u) break; } RELOAD(); } while (0)
__device__ __forceinline__ u32 clk() { return (u32)__builtin_amdgcn_s_memrealtime(); }
__device__ __forceinline__ void until(u32 t) {
#pragma nounroll
  for (int it = 0; it < 160; ++it) { if ((int)(clk() - t) >= 0) break; }
}
__device__ __forceinline__ u32 later(u32 a, u32 b) { return (int)(a - b) > 0 ? a : b; }
__device__ __forceinline__ void hint_wait(const char* p, u32 tag, u32 ticks) {
  u64 v;
#pragma nounroll
  for (int it = 0; it < 64; ++it) {
    asm volatile("s_load_dwordx2 %0, %1, 0x0 glc\n\ts_waitcnt lgkmcnt(0)" : "=s"(v) : "s"(p) : "memory");
    if ((u32)v == tag) break;
  }
  until((u32)(v >> 32) + ticks);
}
__device__ __forceinline__ void hint_wait4(const char* p, u32 tag, u32 ticks) {
  s8u v;
#pragma nounroll
  for (int it = 0; it < 64; ++it) {
    asm volatile("s_load_dwordx8 %0, %1, 0x0 glc\n\ts_waitcnt lgkmcnt(0)" : "=s"(v) : "s"(p) : "memory");
    if ((((v[0] ^ tag) | (v[2] ^ tag)) | ((v[4] ^ tag) | (v[6] ^ tag))) == 0u) break;
  }
  until(later(later(v[1], v[3]), later(v[5], v[7])) + ticks);
}
__device__ __forceinline__ void hint_wait8(const char* p, u32 tag) {
#pragma nounroll
  for (int it = 0; it < 64; ++it) {
    s8u v;
    asm volatile("s_load_dwordx8 %0, %1, 0x0 glc\n\ts_waitcnt lgkmcnt(0)" : "=s"(v) : "s"(p) : "memory");
    if ((((v[0] ^ tag) | (v[1] ^ tag)) | ((v[2] ^ tag) | (v[3] ^ tag)) | ((v[4] ^ tag) | (v[5] ^ tag)) | ((v[6] ^ tag) | (v[7] ^ tag))) == 0u) break;
  }
}
__device__ __forceinline__ u32 max4(v4u v) { return max(max(v.x, v.y), max(v.z, v.w)); }
__device__ __forceinline__ float max2(float a, float b) { float r; asm("v_max_f32 %0, %1, %2" : "=v"(r) : "v"(a), "v"(b)); return r; }
__device__ __forceinline__ float max3(float a, float b, float c) { float r; asm("v_max3_f32 %0, %1, %2, %3" : "=v"(r) : "v"(a), "v"(b), "v"(c)); return r; }
__device__ __forceinline__ float max8_16_32(float x) {
  float y;
  asm("s_nop 1\n\tv_max_f32_dpp %0, %0, %0 row_ror:8 row_mask:0xf bank_mask:0xf\n\tv_mov_b32 %1, %0\n\ts_nop 1\n\t"
      "v_permlane16_swap_b32 %0, %1\n\tv_max_f32 %0, %0, %1\n\tv_mov_b32 %1, %0\n\ts_nop 1\n\t"
      "v_permlane32_swap_b32 %0, %1\n\tv_max_f32 %0, %0, %1" : "+v"(x), "=&v"(y));
  return x;
}
__device__ __forceinline__ v4u ld16(rsrc_t rs, u32 off) { return __builtin_amdgcn_raw_buffer_load_b128(rs, off, 0, AUX_SC1); }
__device__ __forceinline__ void st16(rsrc_t rs, u32 off, v4u v) { __builtin_amdgcn_raw_buffer_store_b128(v, rs, off, 0, AUX_SC1); }
__device__ __forceinline__ void st8(rsrc_t rs, u32 off, u32 a, u32 b) { __builtin_amdgcn_raw_buffer_store_b64(v2u{a, b}, rs, off, 0, AUX_SC1); }
__device__ __forceinline__ bool none(bool p) { return __builtin_amdgcn_ballot_w64(p) == 0ull; }
__device__ __forceinline__ rsrc_t mkrs(const void* p, int bytes) { return __builtin_amdgcn_make_buffer_rsrc((void*)p, 0, bytes, 0x00020000); }

__device__ __forceinline__ float dot2(u32 a, u32 b, float c) {
  return __builtin_amdgcn_fdot2_f32_bf16(__builtin_bit_cast(bf16x2, a), __builtin_bit_cast(bf16x2, b), c, false);
}
__device__ __forceinline__ v4u ln_half(const v4u c, const v4u o) {
  float s = 0.f, q = 0.f;
#pragma unroll
  for (int i = 0; i < 4; ++i) { s = dot2(c[i], 0x3F803F80u, s); q = dot2(c[i], c[i], q); s = dot2(o[i], 0x3F803F80u, s); q = dot2(o[i], o[i], q); }
  const float mean = wave_sum(s) * (1.f / 1024.f);
  const float var = fmaxf(fmaf(-mean, mean, wave_sum(q) * (1.f / 1024.f)), 0.f);
  const u32 mv = pk2(mean, var);
  const float mb = bflo(mv);
  const float den = rbf(__builtin_amdgcn_sqrtf(rbf(bfhi(mv) + 1e-5f)));
  const float inv = __builtin_amdgcn_rcpf(den);
  v4u r;
#pragma unroll
  for (int i = 0; i < 4; ++i) {
    const u32 d = pk2(bflo(c[i]) - mb, bfhi(c[i]) - mb);
    const float d0 = bflo(d), d1 = bfhi(d);
    float q0 = d0 * inv, q1 = d1 * inv;
    q0 = fmaf(fmaf(-q0, den, d0), inv, q0);
    q1 = fmaf(fmaf(-q1, den, d1), inv, q1);
    r[i] = pk2(q0, q1);
  }
  return r;
}

#define STAMP(i) do {} while (0)
#define STAMP_DECL() do {} while (0)
#define STAMP_FLUSH() do {} while (0)

}

__global__ void __launch_bounds__(NTHR, 2)
fused_decode_layer(const char* x, const char* kc, const char* vc, const char* wq, const char* wo, const char* w1, const char* w2, char* out, char* ws) {
  extern __shared__ __attribute__((aligned(16))) char smem[];
  STAMP_DECL();
  STAMP(0);
  {
    u32 t0, t1, t2, t3, t4;
    asm volatile(
        "s_load_dwordx8 s[64:71], %[ka], 0x0\n\t"
        "s_getpc_b64 s[82:83]\n\t"
        "v_readfirstlane_b32 s72, %[tid]\n\t"
        "v_and_b32 %[t4], 63, %[tid]\n\t"
        "s_mov_b32 s79, 0x20000\n\t"
        "s_mov_b32 s78, %[szq]\n\t"
        "s_lshr_b32 s72, s72, 6\n\t"
        "s_and_b32 s73, s72, 3\n\t"
        "v_lshrrev_b32 %[t1], 4, %[t4]\n\t"
        "s_bfe_u32 s84, %[c], 0x20003\n\t"
        "s_bfe_u32 s74, %[c], 0x10006\n\t"
        "s_lshl1_add_u32 s84, s84, s74\n\t"
        "s_bfe_u32 s85, %[c], 0x10005\n\t"
        "s_lshl2_add_u32 s85, s85, s73\n\t"
        "s_and_b32 s74, %[c], 0x80\n\t"
        "s_cmp_gt_u32 s72, 3\n\t"
        "s_cbranch_scc1 Lpro_hi\n\t"
        "s_cmp_eq_u32 s74, 0\n\t"
        "s_cbranch_scc1 Lpro_bl\n\t"
        "s_cmp_lg_u32 s72, 3\n\t"
        "s_cbranch_scc1 Lpro_al\n\t"
        "s_bfe_u32 s74, %[c], 0x40003\n\t"
        "s_mul_i32 s74, s74, %[cl]*64\n\t"
        "s_and_b32 s82, s82, 0xffffff00\n\t"
        "s_add_u32 s82, s82, s74\n\t"
        "s_addc_u32 s83, s83, 0\n\t"
        ".irp o,0,1,2,3,4,5,6,7,8,9,10,11,12,13,14,15,16,17\n\t"
        ".if \\o < %[cl]\n\t"
        "s_load_dword s75, s[82:83], \\o*64+1024\n\t"
        ".endif\n\t"
        ".endr\n\t"
        "s_barrier\n\t"
        "s_branch Lpro_end\n\t"
        "Lpro_al:\n\t"
        "v_lshlrev_b32 %[t2], 2, %[t4]\n\t"
        "v_add_u32 %[t2], %[lfl], %[t2]\n\t"
        "v_mov_b32 %[t1], 0\n\t"
        "ds_write_b32 %[t2], %[t1]\n\t"
        "s_lshl1_add_u32 s74, s73, s84\n\t"
        "s_and_b32 s74, s74, 7\n\t"
        "s_add_u32 s75, s74, %[c]\n\t"
        "s_and_b32 s75, s75, 7\n\t"
        "s_lshl3_add_u32 s75, s74, s75\n\t"
        "s_lshl_b32 s75, s75, 7\n\t"
        "s_lshl1_add_u32 s74, s85, 1\n\t"
        "s_lshl_b32 s74, s74, 19\n\t"
        "s_or_b32 s74, s74, s75\n\t"
        "v_lshlrev_b32 %[t0], 13, %[t4]\n\t"
        "s_mov_b32 m0, %[junk]\n\t"
        "s_waitcnt lgkmcnt(0)\n\t"
        "s_mov_b32 s76, s70\n\t"
        "s_and_b32 s77, s71, 0xffff\n\t"
        "buffer_load_dword %[t0], s[76:79], s74 offen " WQPOL " lds\n\t"
        "s_add_u32 s85, s85, 1\n\t"
        "s_add_u32 s73, s73, 1\n\t"
        "s_cmp_eq_u32 s73, 3\n\t"
        "s_cbranch_scc1 Lpro_al\n\t"
        "s_barrier\n\t"
        "s_branch Lpro_end\n\t"
        "Lpro_hi:\n\t"
        "s_lshl_b32 m0, s73, 14\n\t"
        "s_cmp_lg_u32 s74, 0\n\t"
        "s_cbranch_scc1 Lpro_ah\n\t"
        "v_and_b32 %[t1], 1, %[t1]\n\t"
        "v_lshlrev_b32 %[t1], 1, %[t1]\n\t"
        "v_and_b32 %[t0], 7, %[t4]\n\t"
        "v_xor_b32 %[t0], %[t0], %[t1]\n\t"
        "v_lshrrev_b32 %[t2], 3, %[t4]\n\t"
        "v_lshlrev_b32 %[t2], 13, %[t2]\n\t"
        "v_lshl_or_b32 %[t0], %[t0], 4, %[t2]\n\t"
        "v_xor_b32 %[t1], 64, %[t0]\n\t"
        "s_add_u32 s75, s84, %[c]\n\t"
        "s_and_b32 s75, s75, 7\n\t"
        "s_lshl3_add_u32 s75, s84, s75\n\t"
        "s_lshl_b32 s75, s75, 7\n\t"
        "s_lshl_b32 s74, s85, 20\n\t"
        "s_or_b32 s74, s74, s75\n\t"
        "s_mov_b32 s75, 0xfc00\n\t"
        "s_mov_b32 s73, 4\n\t"
        "s_waitcnt lgkmcnt(0)\n\t"
        "s_mov_b32 s76, s70\n\t"
        "s_and_b32 s77, s71, 0xffff\n\t"
        "Lpro_bh_loop:\n\t"
        "buffer_load_dwordx4 %[t0], s[76:79], s74 offen nt lds\n\t"
        "s_add_u32 s74, s74, s75\n\t"
        "buffer_load_dwordx4 %[t1], s[76:79], s74 offen offset:1024 nt lds\n\t"
        "s_add_u32 s74, s74, s75\n\t"
        "buffer_load_dwordx4 %[t0], s[76:79], s74 offen offset:2048 nt lds\n\t"
        "s_add_u32 s74, s74, s75\n\t"
        "buffer_load_dwordx4 %[t1], s[76:79], s74 offen offset:3072 nt lds\n\t"
        "s_add_u32 m0, m0, 0x1000\n\t"
        "s_add_u32 s74, s74, 0x10c00\n\t"
        "s_sub_u32 s73, s73, 1\n\t"
        "s_cmp_lg_u32 s73, 0\n\t"
        "s_cbranch_scc1 Lpro_bh_loop\n\t"
        "s_branch Lpro_end\n\t"
        "Lpro_bl:\n\t"
        "v_lshlrev_b32 %[t0], 4, %[t4]\n\t"
        "v_and_b32 %[t1], 31, %[t4]\n\t"
        "v_lshlrev_b32 %[t1], 13, %[t1]\n\t"
        "s_lshl_b32 s74, s73, 12\n\t"
        "s_add_u32 m0, s74, %[r1]\n\t"
        "s_waitcnt lgkmcnt(0)\n\t"
        "s_and_b32 s65, s65, 0xffff\n\t"
        "s_movk_i32 s66, 0x4000\n\t"
        "s_mov_b32 s67, s79\n\t"
        "buffer_load_dwordx4 %[t0], s[64:67], s74 offen lds\n\t"
        "buffer_load_dwordx4 %[t0], s[64:67], s74 offen offset:1024 lds\n\t"
        "buffer_load_dwordx4 %[t0], s[64:67], s74 offen offset:2048 lds\n\t"
        "buffer_load_dwordx4 %[t0], s[64:67], s74 offen offset:3072 lds\n\t"
        "s_cmp_lg_u32 s72, 0\n\t"
        "s_cbranch_scc1 Lpro_end\n\t"
        "s_lshr_b32 s74, s70, 7\n\t"
        "s_sub_u32 s74, 3, s74\n\t"
        "s_and_b32 s74, s74, 7\n\t"
        "s_sub_u32 s75, s74, %[c]\n\t"
        "s_and_b32 s75, s75, 7\n\t"
        "s_lshl3_add_u32 s75, s75, s74\n\t"
        "s_lshl_b32 s75, s75, 7\n\t"
        "s_bfe_u32 s82, %[c], 0x30003\n\t"
        "s_bfe_u32 s83, %[c], 0x10006\n\t"
        "s_lshl2_add_u32 s82, s82, s83\n\t"
        "s_lshl_b32 s82, s82, 18\n\t"
        "s_or_b32 s74, s82, s75\n\t"
        "s_mov_b32 m0, %[junk]\n\t"
        "s_mov_b32 s76, s70\n\t"
        "s_and_b32 s77, s71, 0xffff\n\t"
        "buffer_load_dword %[t1], s[76:79], s74 offen lds\n\t"
        "s_branch Lpro_end\n\t"
        "Lpro_ah:\n\t"
        "v_and_b32 %[t4], 15, %[t4]\n\t"
        "v_xor_b32 %[t0], %[t4], %[t1]\n\t"
        "v_lshlrev_b32 %[t1], 8, %[t1]\n\t"
        "v_lshl_or_b32 %[t0], %[t0], 4, %[t1]\n\t"
        "v_xor_b32 %[t1], 64, %[t0]\n\t"
        "v_xor_b32 %[t2], 0x80, %[t0]\n\t"
        "v_xor_b32 %[t3], 0x80, %[t1]\n\t"
        "s_bfe_u32 s74, %[c], 0x20005\n\t"
        "s_and_b32 s75, %[c], 7\n\t"
        "s_lshl3_add_u32 s74, s74, s75\n\t"
        "s_bfe_u32 s75, %[c], 0x20003\n\t"
        "s_lshl4_add_u32 s74, s74, s75\n\t"
        "s_or_b32 s74, s74, 12\n\t"
        "s_lshl2_add_u32 s74, s74, s73\n\t"
        "s_lshl_b32 s74, s74, 14\n\t"
        "s_mov_b32 s78, %[szk]\n\t"
        "s_mov_b32 s73, 4\n\t"
        "s_waitcnt lgkmcnt(0)\n\t"
        "s_barrier\n\t"
        "s_mov_b32 s76, s66\n\t"
        "s_and_b32 s77, s67, 0xffff\n\t"
        "Lpro_k_loop:\n\t"
        "buffer_load_dwordx4 %[t0], s[76:79], s74 offen nt lds\n\t"
        "buffer_load_dwordx4 %[t1], s[76:79], s74 offen offset:1024 nt lds\n\t"
        "buffer_load_dwordx4 %[t2], s[76:79], s74 offen offset:2048 nt lds\n\t"
        "buffer_load_dwordx4 %[t3], s[76:79], s74 offen offset:3072 nt lds\n\t"
        "s_add_u32 m0, m0, 0x1000\n\t"
        "s_addk_i32 s74, 0x1000\n\t"
        "s_sub_u32 s73, s73, 1\n\t"
        "s_cmp_lg_u32 s73, 0\n\t"
        "s_cbranch_scc1 Lpro_k_loop\n\t"
        "Lpro_end:\n\t"
        ".pushsection .text,1\n\t"
        ".fill 512, 4, 0xBF800000\n\t"
        ".popsection\n\t"
        "s_waitcnt lgkmcnt(0)"
        : [t0] "=&v"(t0), [t1] "=&v"(t1), [t2] "=&v"(t2), [t3] "=&v"(t3), [t4] "=&v"(t4)
        : [ka] "s"(__builtin_amdgcn_kernarg_segment_ptr()), [tid] "v"(threadIdx.x), [c] "s"(blockIdx.x), [cl] "i"(CODE_LINES),
          [junk] "i"(L_JUNK), [r1] "i"(L_R1),
          [szk] "i"(SZ_K), [szq] "i"(SZ_WQ), [lfl] "i"(L_FL)
        : "s64", "s65", "s66", "s67", "s68", "s69", "s70", "s71", "s72", "s73", "s74", "s75", "s76", "s77", "s78", "s79",
          "s82", "s83", "s84", "s85", "scc", "memory");
  }
  STAMP(1);
  LAS char* L = (LAS char*)smem;
  const int tid = threadIdx.x, l = tid & 63, w = __builtin_amdgcn_readfirstlane(tid >> 6), c = blockIdx.x;
  const int g = l >> 4, xcd = c & 7, sl = (c >> 3) & 15, u = w & 3;
  const bool hiw = w >= 4;
  const int bk = (sl >> 2) * 8 + xcd;
  const u32 kvb = (bk * 4096 + 3072 + 256 * (sl & 3)) * 256;
  const int q4 = (l >> 2) & 3, p4 = l & 3, b8 = l & 7, d8 = (l >> 3) & 1;
  const int fsw = q4 | ((g & 1) << 2);
  const rsrc_t rsW = mkrs(ws, WS_END);
  const f32x4 zero4 = {0.f, 0.f, 0.f, 0.f};
  u32 tagLo = 0, tagHi = 0;
  auto q_published = [&](int cb) {
    const int s_ = cb >> 3, bd = 2 * (s_ & 3) + (s_ >> 3);
    const u32 off = WS_QG + xcd * 65536 + (((s_ & 3) * 2 + ((s_ >> 2) & 1)) * 16 + (bd & 1) * 8 + ((cb + bd) & 7)) * 512;
    POLL_LOOP {
      RELOAD();
      const v4u f0 = ld16(rsW, off);
      if (none(((f0.y ^ tagLo) | (f0.w ^ tagHi)) != 0u)) break;
      nap<4>();
    }
  };

  if (c < 128) {
    const int band = 2 * (sl & 3) + (sl >> 3), cg = 8 * band + ((xcd + band) & 7), kh = (sl >> 2) & 1;
    if (!hiw) {
      LAS char* px = L + L_R1 + w * 4096 + l * 16;
      if (w == 0) VMWAIT(1); else VMWAIT(0);
      STAMP(13);
      const v4u xc0 = *(LAS v4u*)(px + kh * 1024), xo0 = *(LAS v4u*)(px + (kh ^ 1) * 1024);
      const v4u xc1 = *(LAS v4u*)(px + 2048 + kh * 1024), xo1 = *(LAS v4u*)(px + 2048 + (kh ^ 1) * 1024);
      *(LAS v4u*)(L + L_ACT + (2 * w) * 1056 + l * 16) = ln_half(xc0, xo0);
      *(LAS v4u*)(L + L_ACT + (2 * w + 1) * 1056 + l * 16) = ln_half(xc1, xo1);
      STAMP(12);
    }
    const v4u sent4 = {SENT, SENT, SENT, SENT};
    const int j = c, kvo = sl >> 2, nq = sl & 3, ksl = kvo * 8 + xcd;
    BARRIER();
    if (w == 7) {
#pragma unroll
      for (int i = 0; i < 4; ++i) __builtin_amdgcn_raw_buffer_store_b128(sent4, rsW, WS_WP + (32 * nq + (l >> 3)) * 4096 + ksl * 128 + b8 * 16, i * 8 * 4096, AUX_SC1);
      st16(rsW, (nq == 3 && l < 32) ? WS_H + ksl * 512 + l * 16 : WS_END, sent4);
    }
    if (MP_AT == 0 && w == 3) {
      nap<2>();
#pragma unroll
      for (int i = 0; i < 14; ++i) __builtin_amdgcn_raw_buffer_store_b128(sent4, rsW, WS_MP + j * MPJ + (i & 3) * 1024 + l * 16, (i >> 2) * 4096, AUX_SC1);
    }
    const int bq = 2 * u + (l >> 5), cp = l & 31;
    LAS char* pq = L + L_P + bq * 272 + cp * 8;
    u32 qo = WS_QG + (((bq * 4 + (cg >> 4)) * 2 + kh) * 1024 + 64 * (cg & 15) + 2 * cp) * 8;
    {
      const int kh2 = w >> 2;
      s16x8 bf[8];
      LAS char* pb = L + L_ACT + b8 * 1056 + kh2 * 512 + 16 * g;
#pragma unroll
      for (int ks = 0; ks < 8; ++ks) bf[ks] = *(LAS s16x8*)(pb + ks * 64);
      LAS char* pa = L + L_R0 + kh2 * 32768 + (8 * g + q4) * 128 + ((u ^ (((q4 >> 1) & 1) | ((g & 1) << 1))) << 5) + 8 * p4;
      LAS char* pw = L + L_P + (kh2 * 8 + b8) * 272 + (16 * u + 4 * g) * 4;
      u32 ho = (w == 6 && l < 8) ? WS_QH + (l * 16 + (cg >> 2)) * 128 + ((cg & 3) * 2 + kh) * 4 : WS_END;
      PIN(pa); PIN(pw); PIN(pq); PIN(qo); PIN(ho);
      LOAD_TAG();
      if (w == 7) VMWAIT(5); else if (hiw) VMWAIT(0);
      STAMP(2);
      RAWBAR();
      if (w == 6) __builtin_amdgcn_raw_buffer_store_b32(tagLo, rsW, ho, 0, AUX_SC1);
      s16x8 af[8];
#pragma unroll
      for (int ks = 0; ks < 8; ++ks) af[ks] = trfrag(pa + ks * 4096, 512);
      FENCE();
      f32x4 acc = zero4;
#pragma unroll
      for (int ks = 0; ks < 8; ++ks) acc = mfma(af[ks], bf[ks], acc);
      if (!d8) *(LAS f32x4*)pw = acc;
    }
    if (w == 7) VMWAIT(0);
    BARRIER();
    if (!hiw) {
      const f32x2 s = *(LAS f32x2*)pq + *(LAS f32x2*)(pq + 8 * 272);
      st16(rsW, qo, v4u{__float_as_uint(s[0]), tagLo, __float_as_uint(s[1]), tagHi});
      STAMP(3);
      nap<TOUCH_DELAY>();
      const rsrc_t rv = mkrs(vc, SZ_V), ro_ = mkrs(wo, SZ_WO);
#pragma unroll
      for (int i = 2 * V_OWN; i < 8; i += 4) if (i + u < 8) touch<VTNT>(rv, g * 16384 + (l & 15) * 128, kvb + (i + u) * 2048, L_JUNK);
      const u32 vo = (l >> 5) * 2048 + (((((l & 31) >> 1) ^ (l >> 5)) << 1) | (l & 1)) * 16;
#pragma unroll
      for (int i = 0; i < 4; ++i) {
        const int h = 2 * u + (i >> 1), half = i & 1;
        const u32 vx = vo ^ (128 * half);
        dma4(ro_, vx, vx ^ 64, vx, vx ^ 64, ((kvo * 8 + h) * 128 + 16 * xcd + 8 * half) * 2048 + 512 * nq, 4096 - 1024, L_R0 + (8 * h + 4 * half) * 1024);
      }
      const int id = u * 64 + l;
      if (u < W1E) touch<0>(mkrs(w1, SZ_W1), (id / 7) * 7168 + (id % 7) * 128, sl * 64 * 7168 + xcd * 7 * 128, L_JUNK);
      STAMP(4);
    } else {
      if (MP_AT == 1 && w == 6) {
        nap<3>();
#pragma unroll
        for (int i = 0; i < 14; ++i) __builtin_amdgcn_raw_buffer_store_b128(sent4, rsW, WS_MP + j * MPJ + (i & 3) * 1024 + l * 16, (i >> 2) * 4096, AUX_SC1);
      }
      nap<OP_NAP>();
      VMWAIT(0);
    }
    const u32 one = (l & 8) ? 0u : ((l & 1) ? 0x3F800000u : 0x00003F80u);
    const int si = b8 >> 1;
    v4u sel = {si == 0 ? one : 0u, si == 1 ? one : 0u, si == 2 ? one : 0u, si == 3 ? one : 0u};
    const int rj = ksl * 4 + u;
    v2u xres = *(LAS v2u*)(L + L_R1 + b8 * 2048 + (32 * (rj >> 2) + 4 * (rj & 3) + 16 * (g & 1)) * 2);
    u32 wb = WS_WP + rj * 4096 + l * 16, hb = l < 8 ? WS_H + rj * 128 + l * 16 : WS_END;
    {
      u32 ob = WS_OP + (ksl * 8 + w) * 2048 + l * 16;
      LAS char* po = L + L_ACT + w * 288 + b8 * 32 + ((l >> 3) & 3) * 8 + (l >> 5) * 4;
      u32 wpo = d8 ? WS_END : WS_WP + (4 * (8 * nq + w) + g) * 4096 + ksl * 128 + b8 * 16;
      LAS char* pb = L + L_ACT + b8 * 288 + 16 * g;
      LAS char* pa0 = L + L_R0 + (8 * g + q4) * 512 + (((2 * w) ^ fsw) << 5) + 8 * p4;
      u32 gho = (w == 7 && l < 2) ? WS_GH + (l * 32 + xcd * 4 + nq) * 128 + kvo * 8 : WS_END;
      PIN(gho); PIN(ob); PIN(po); PIN(wpo); PIN(pb); PIN(pa0); PIN(sel); PIN(wb); PIN(hb); PIN(xres);
      hint_wait4(ws + WS_OH + ((w * 4 + kvo) * 8 + xcd) * 128, tagLo, O_DELAY);
      STAMP(6);
      v4u d0 = ld16(rsW, ob), d1 = ld16(rsW, ob + 1024);
      VMWAIT(2);
      STAMP(5);
      RAWBAR();
      s16x8 wf[2][4];
#pragma unroll
      for (int T = 0; T < 2; ++T)
#pragma unroll
        for (int ks = 0; ks < 4; ++ks) wf[T][ks] = trfrag((LAS char*)((u32)(size_t)pa0 ^ (32 * T)) + ks * 16384, 2048);
      st8(rsW, gho, tagLo, clk());
      FENCE();
      const u32 okey = 2u * tagLo + 1u;
      if (!none(((d0.y ^ d0.w ^ okey) | (d1.y ^ d1.w ^ okey)) != 0u)) {
        POLL_LOOP {
          RELOAD();
          d0 = ld16(rsW, ob); d1 = ld16(rsW, ob + 1024);
          if (none(((d0.y ^ d0.w ^ okey) | (d1.y ^ d1.w ^ okey)) != 0u)) break;
        }
      }
      const float l0 = __uint_as_float(d0.w ^ tagHi), l1 = __uint_as_float(d1.w ^ tagHi);
      float mx = max2(l0, l1);
      { auto t = __builtin_amdgcn_permlane32_swap(__float_as_uint(mx), __float_as_uint(mx), false, false); mx = max2(__uint_as_float(t[0]), __uint_as_float(t[1])); }
      const float w0 = __builtin_amdgcn_exp2f(l0 - mx), w1_ = __builtin_amdgcn_exp2f(l1 - mx);
      float tot = w0 + w1_;
      { auto t = __builtin_amdgcn_permlane32_swap(__float_as_uint(tot), __float_as_uint(tot), false, false); tot = __uint_as_float(t[0]) + __uint_as_float(t[1]); }
      const float it_ = __builtin_amdgcn_rcpf(tot);
      const float v0 = fmaf(w0, bflo(d0.x), w1_ * bflo(d1.x)), v1 = fmaf(w0, bfhi(d0.x), w1_ * bfhi(d1.x));
      const float v2 = fmaf(w0, bflo(d0.z), w1_ * bflo(d1.z)), v3 = fmaf(w0, bfhi(d0.z), w1_ * bfhi(d1.z));
      auto t0 = __builtin_amdgcn_permlane32_swap(__float_as_uint(v0), __float_as_uint(v2), false, false);
      auto t1 = __builtin_amdgcn_permlane32_swap(__float_as_uint(v1), __float_as_uint(v3), false, false);
      *(LAS u32*)po = pk2((__uint_as_float(t0[0]) + __uint_as_float(t0[1])) * it_, (__uint_as_float(t1[0]) + __uint_as_float(t1[1])) * it_);
      STAMP(8);
      BARRIER();
      s16x8 bf[4];
#pragma unroll
      for (int ks = 0; ks < 4; ++ks) bf[ks] = *(LAS s16x8*)(pb + ks * 64);
      FENCE();
      f32x4 a0 = zero4, a1 = zero4;
#pragma unroll
      for (int ks = 0; ks < 4; ++ks) { a0 = mfma(wf[0][ks], bf[ks], a0); a1 = mfma(wf[1][ks], bf[ks], a1); }
      st16(rsW, wpo, v4u{pk2(a0[0], a0[1]), pk2(a0[2], a0[3]), pk2(a1[0], a1[1]), pk2(a1[2], a1[3])});
      STAMP(9);
    }
    if (nq != 3) {
      RAWBAR();
      const int id = 28 * W2_MID + 8 * SPARE_N + w * 12 + kvo * 3 + nq;
      if (id < 112) touch<W2NT>(mkrs(w2, SZ_W2), l * 128, (xcd * 14 + id % 14) * 65536 + (id / 14) * 8192, L_JUNK);
    } else if (!hiw) {
      hint_wait4(ws + WS_GH + (xcd * 4 + u) * 128, tagLo, RG_DELAY);
      if (HR_DELAY) st8(rsW, l == 0 ? WS_GH + (64 + xcd * 4 + u) * 128 + kvo * 8 : WS_END, tagLo, clk());
      STAMP(12);
      f32x4 acc = zero4;
      POLL_LOOP {
        RELOAD();
        v4u d[4];
#pragma unroll
        for (int i = 0; i < 4; ++i) d[i] = ld16(rsW, wb + i * 1024);
        FENCE();
        acc = zero4;
#pragma unroll
        for (int i = 0; i < 4; ++i) acc = mfma(sel, d[i], acc);
        if (none(acc[0] != acc[0])) break;
        if (none(max(max(max4(d[0]), max4(d[1])), max(max4(d[2]), max4(d[3]))) == SENT)) break;
      }
      u32 a01 = pk2(acc[0] + dpp<DPP_R8>(acc[0]), acc[1] + dpp<DPP_R8>(acc[1]));
      u32 a23 = pk2(acc[2] + dpp<DPP_R8>(acc[2]), acc[3] + dpp<DPP_R8>(acc[3]));
      PIN(a01); PIN(a23);
      const u32 h01 = pk2(bflo(xres.x) + bflo(a01), bfhi(xres.x) + bfhi(a01));
      const u32 h23 = pk2(bflo(xres.y) + bflo(a23), bfhi(xres.y) + bfhi(a23));
      auto s0 = __builtin_amdgcn_permlane16_swap(h01, h01, false, false);
      auto s1 = __builtin_amdgcn_permlane16_swap(h23, h23, false, false);
      st16(rsW, hb, v4u{s0[0], s1[0], s0[1], s1[1]});
      STAMP(10);
    }
    LAS char* pfs = L + L_P + (l & 31) * 16;
    u32 mb = WS_MP + j * MPJ + w * 2048 + l * 16;
    u32 oo = (l & 40) == 0 ? b8 * 2048 + (32 * (j >> 2) + 4 * (j & 3) + 16 * (g & 1)) * 2 : 16384;
    PIN(pfs); PIN(mb); PIN(oo);
    if (w == 7) {
      nap<24>();
      q_published(((j >> 5) * 4 + 3) * 8 + ((j >> 2) & 7));
      hint_wait(ws + WS_FH + (sl < 14 ? j : j - 16) * 128, tagLo, F_DELAY);
    }
    RAWBAR();
    STAMP(7);
    v2u hres = {0u, 0u};
    if (w < 7) {
      f32x4 acc = zero4;
      POLL_LOOP {
        RELOAD();
        const v4u d0 = ld16(rsW, mb), d1 = ld16(rsW, mb + 1024);
        FENCE();
        acc = mfma(sel, d1, mfma(sel, d0, zero4));
        if (none(acc[0] != acc[0])) break;
        if (none(max(max4(d0), max4(d1)) == SENT)) break;
      }
      STAMP(13);
      if (l < 32) *(LAS f32x4*)(pfs + w * 512) = acc;
    } else {
      POLL_LOOP {
        RELOAD();
        hres = __builtin_amdgcn_raw_buffer_load_b64(rsW, WS_H + j * 128 + b8 * 16 + (g & 1) * 8, 0, AUX_SC1);
        if (none(max(hres.x, hres.y) == SENT)) break;
      }
    }
    BARRIER();
    if (w == 7) {
      f32x4 p[7];
#pragma unroll
      for (int i = 0; i < 7; ++i) p[i] = *(LAS f32x4*)(pfs + i * 512);
      FENCE();
      const f32x4 acc = ((p[0] + p[1]) + (p[2] + p[3])) + ((p[4] + p[5]) + p[6]);
      u32 y01 = pk2(acc[0] + dpp<DPP_R8>(acc[0]), acc[1] + dpp<DPP_R8>(acc[1]));
      u32 y23 = pk2(acc[2] + dpp<DPP_R8>(acc[2]), acc[3] + dpp<DPP_R8>(acc[3]));
      PIN(y01); PIN(y23);
      const v2u r = {pk2(bflo(hres.x) + bflo(y01), bfhi(hres.x) + bfhi(y01)), pk2(bflo(hres.y) + bflo(y23), bfhi(hres.y) + bfhi(y23))};
      __builtin_amdgcn_raw_buffer_store_b64(r, mkrs(out, 8 * 1024 * 2), oo, 0, 0);
      if (j == 0 && l == 0) st8(rsW, WS_EPOCH, tagLo, tagHi);
      STAMP(11);
    }
    STAMP_FLUSH();
    return;
  }

  const int s = sl & 3, b = bk >> 2, kvh = bk & 3;
  const int m = xcd * 14 + sl;
  const int h0 = (m & 1) * 512 + 128 * u, h1 = h0 ^ 512;
  if (hiw) {
    const rsrc_t rv = mkrs(vc, SZ_V), r1 = mkrs(w1, SZ_W1), r2 = mkrs(w2, SZ_W2);
    const u32 vv = g * 256 + (((((l & 15) >> 1) ^ g) << 1) | (l & 1)) * 16;
#pragma unroll
    for (int i = 0; i < V_OWN; ++i) dma4c(rv, vv, vv, vv ^ 128, vv ^ 128, kvb + u * 16384 + i * 4096, L_R1 + u * 16384 + i * 4096);
    LOAD_TAG();
    LAS char* pa = L + L_R1 + (8 * g + q4) * 256 + (((2 * u) ^ fsw) << 5) + 8 * p4;
    LAS char* pf_ = L + L_P + b8 * 544 + 16 * g;
    LAS char* ps = L + L_ST + b8 * 16;
    u32 oo = WS_OP + (((kvh * 8 + 2 * u + d8) * 8 + b) * 4 + s) * 512 + g * 128 + b8 * 16;
    u32 v1 = (l >> 2) * 7168 + ((((l >> 1) & 1) ^ ((l >> 4) & 1)) << 5) + (l & 1) * 16;
    PIN(pa); PIN(pf_); PIN(ps); PIN(oo); PIN(v1);
    VMWAIT(4 * V_OWN);
    LSET(F_KR + u * 4);
    STAMP(2);
    LWAIT4(F_KD);
#pragma unroll
    for (int i = V_OWN; i < 4; ++i) dma4c(rv, vv, vv, vv ^ 128, vv ^ 128, kvb + u * 16384 + i * 4096, L_R1 + u * 16384 + i * 4096);
    RAWBAR();
    const u32 tq = clk();
    const rsrc_t r1m = mkrs(w1, sl < 14 ? SZ_W1 : 0);
    auto c3_touch = [&]() {
      const int lc = (3 - (int)(((u32)(size_t)w1) >> 7) - 7 * xcd) & 7;
      touch<0>(r1, (lc < 7 && l < 16) ? l * 7168 : 0x7fffffff, (sl * 64 + u * 16) * 7168 + (xcd * 7 + lc) * 128, L_JUNK);
    };
    auto w1_pre = [&]() {
      if (C3_PRE) c3_touch();
#pragma unroll
      for (int i = 0; i < W1_PRE; ++i) dma4<0>(r1m, v1, v1, v1, v1, (h0 + 64 * i) * 7168 + 64 * m, 16 * 7168 - 1024, L_R0 + (h0 + 64 * i) * 64);
    };
    if (PRE_AT == 0) { w1_pre(); VMWAIT(4 * W1_PRE + C3_PRE); } else { VMWAIT(0); }
    LSET(F_V + u * 4);
    LWAIT4(F_V);
    STAMP(3);
    if (w == 7) st8(rsW, l < 8 ? WS_OH + (bk * 8 + l) * 128 + s * 8 : WS_END, tagLo, later(tq + OQ_LEAD, clk() + OV_LEAD));
    s16x8 pf[8], vf[2][8];
#pragma unroll
    for (int ks = 0; ks < 8; ++ks) {
      vf[0][ks] = trfrag(pa + ks * 8192, 1024);
      vf[1][ks] = trfrag((LAS char*)((u32)(size_t)pa ^ 32) + ks * 8192, 1024);
    }
    if (PRE_AT == 1) w1_pre();
    BARRIER();
    STAMP(5);
    const f32x4 m4 = *(LAS f32x4*)ps, s4 = *(LAS f32x4*)(ps + 128);
#pragma unroll
    for (int ks = 0; ks < 8; ++ks) pf[ks] = *(LAS s16x8*)(pf_ + ks * 64);
    FENCE();
    f32x4 ac[2][4];
#pragma unroll
    for (int i = 0; i < 4; ++i)
#pragma unroll
      for (int T = 0; T < 2; ++T) ac[T][i] = mfma(vf[T][2 * i + 1], pf[2 * i + 1], mfma(vf[T][2 * i], pf[2 * i], zero4));
    const float mm = max2(max3(m4[0], m4[1], m4[2]), m4[3]);
    f32x4 f;
#pragma unroll
    for (int i = 0; i < 4; ++i) f[i] = __builtin_amdgcn_exp2f(m4[i] - mm);
    const float tot = fmaf(f[0], s4[0], f[1] * s4[1]) + fmaf(f[2], s4[2], f[3] * s4[3]);
    const u32 lk = __float_as_uint(mm + __builtin_amdgcn_logf(tot)) ^ tagHi;
    f *= __builtin_amdgcn_rcpf(tot);
    const f32x4 o0 = ac[0][0] * f[0] + ac[0][1] * f[1] + ac[0][2] * f[2] + ac[0][3] * f[3];
    const f32x4 o1 = ac[1][0] * f[0] + ac[1][1] * f[1] + ac[1][2] * f[2] + ac[1][3] * f[3];
    u32 x0 = pk2(o0[0], o0[1]), x1 = pk2(o0[2], o0[3]), y0 = pk2(o1[0], o1[1]), y1 = pk2(o1[2], o1[3]);
    PIN(x0); PIN(x1); PIN(y0); PIN(y1);
    st16(rsW, oo, v4u{d8 ? y0 : x0, lk ^ (2u * tagLo + 1u), d8 ? y1 : x1, lk});
    STAMP(6);
    RAWBAR();
    if (!C3_PRE) c3_touch();
    if (sl >= 14) {
#pragma unroll
      for (int i = 0; i < SPARE_N; ++i) { const int id = 28 * W2_MID + i * 8 + u * 2 + (sl & 1); touch<0>(r2, l * 128, (xcd * 14 + id % 14) * 65536 + (id / 14) * 8192, L_JUNK); }
    } else {
      const u32 v2o = vv + g * (2048 - 256);
      auto load_w2 = [&](int i) {
        const u32 vx = v2o ^ (128 * (i & 1)), so = (32 * m + 8 * i) * 2048 + 512 * u, ld = L_R1 + 16384 * u + 2048 * i;
        asm volatile("s_mov_b32 m0, %2\n\ts_nop 0\n\tbuffer_load_dwordx4 %0, %1, %3 offen nt lds\n\tbuffer_load_dwordx4 %0, %1, %4 offen offset:1024 nt lds\n\t"
                     "s_add_u32 m0, m0, 0x1f00\n\ts_nop 0\n\tbuffer_load_dwordx4 %0, %1, %3 offen offset:256 nt lds\n\tbuffer_load_dwordx4 %0, %1, %4 offen offset:1280 nt lds"
                     :: "v"(vx), "s"(r2), "s"(ld), "s"(so), "s"(so + 4 * 2048 - 1024) : "memory", "scc");
      };
#pragma unroll
      for (int i = 0; i < W2_FIRST; ++i) load_w2(i);
#pragma unroll
      for (int i = W1_PRE; i < 2; ++i) dma4<0>(r1, v1, v1, v1, v1, (h0 + 64 * i) * 7168 + 64 * m, 16 * 7168 - 1024, L_R0 + (h0 + 64 * i) * 64);
#pragma unroll
      for (int i = W2_FIRST; i < W2_MID; ++i) load_w2(i);
      STAMP(4);
#pragma unroll
      for (int i = 0; i < 2; ++i) dma4(r1, v1, v1, v1, v1, (h1 + 64 * i) * 7168 + 64 * m, 16 * 7168 - 1024, L_R0 + (h1 + 64 * i) * 64);
      STAMP(12);
#pragma unroll
      for (int i = W2_MID; i < 4; ++i) load_w2(i);
      STAMP(7);
    }
  } else {
    LOAD_TAG();
    LAS char* pk = L + L_R0 + (64 * u + (l & 15)) * 256;
    LAS char* pb = L + L_ACT + b8 * 288 + 16 * g;
    LAS char* pqw = L + L_ACT + 2 * u * 288 + l * 4;
    LAS char* pp = L + L_P + b8 * 544 + (64 * u + 16 * d8 + 4 * g) * 2;
    LAS char* ps = l < 8 ? L + L_ST + b8 * 16 + u * 4 : L + L_JUNK + 256;
    u32 qb = WS_QG + bk * 16384 + u * 2048 + l * 16;
    PIN(pb); PIN(pp); PIN(ps); PIN(pqw); PIN(qb);
    hint_wait8(ws + WS_QH + (b * 16 + kvh * 4 + u) * 128, tagLo);
    nap<QH_DELAY>();
    STAMP(12);
    v4u d[4];
#pragma unroll
    for (int i = 0; i < 4; ++i) d[i] = __builtin_amdgcn_raw_buffer_load_b128(rsW, qb + (i & 1) * 1024, (i >> 1) * 8192, AUX_SC1);
    LSET(F_KD + u * 4);
    LWAIT1(F_KR + u * 4);
    s16x8 kf[4][4];
#pragma unroll
    for (int ks = 0; ks < 4; ++ks) {
      const int pos = ((4 * ks + g) ^ (l & 15)) * 16;
#pragma unroll
      for (int T = 0; T < 4; ++T) kf[T][ks] = *(LAS s16x8*)(pk + T * 4096 + pos);
    }
    FENCE();
    auto q_bad = [&]() { return (((d[0].y ^ tagLo) | (d[1].y ^ tagLo)) | ((d[2].y ^ tagLo) | (d[3].y ^ tagLo))) | (((d[0].w ^ tagHi) | (d[1].w ^ tagHi)) | ((d[2].w ^ tagHi) | (d[3].w ^ tagHi))); };
    if (!none(q_bad() != 0u)) {
      POLL_LOOP {
        RELOAD();
#pragma unroll
        for (int i = 0; i < 4; ++i) d[i] = __builtin_amdgcn_raw_buffer_load_b128(rsW, qb + (i & 1) * 1024, (i >> 1) * 8192, AUX_SC1);
        if (none(q_bad() != 0u)) break;
      }
    }
#pragma unroll
    for (int i = 0; i < 2; ++i)
      *(LAS u32*)(pqw + i * 288) = pk2(__uint_as_float(d[i].x) + __uint_as_float(d[i + 2].x), __uint_as_float(d[i].z) + __uint_as_float(d[i + 2].z));
    STAMP(2);
    BARRIER();
    STAMP(3);
    s16x8 bf[4];
#pragma unroll
    for (int ks = 0; ks < 4; ++ks) bf[ks] = *(LAS s16x8*)(pb + ks * 64);
    FENCE();
    f32x4 ac[4] = {zero4, zero4, zero4, zero4};
#pragma unroll
    for (int ks = 0; ks < 4; ++ks) { ac[0] = mfma(kf[0][ks], bf[ks], ac[0]); ac[1] = mfma(kf[1][ks], bf[ks], ac[1]); }
    FENCE();
#pragma unroll
    for (int ks = 0; ks < 4; ++ks) { ac[2] = mfma(kf[2][ks], bf[ks], ac[2]); ac[3] = mfma(kf[3][ks], bf[ks], ac[3]); }
    float sc[8];
#pragma unroll
    for (int pr = 0; pr < 2; ++pr)
#pragma unroll
      for (int r = 0; r < 4; r += 2) {
        u32 t = pk2(d8 ? ac[2 * pr + 1][r] : ac[2 * pr][r], d8 ? ac[2 * pr + 1][r + 1] : ac[2 * pr][r + 1]);
        asm("" : "+v"(t));
        u32 t2 = pk2(bflo(t) * 0.08838834764831845f, bfhi(t) * 0.08838834764831845f);
        asm("" : "+v"(t2));
        sc[4 * pr + r] = bflo(t2); sc[4 * pr + r + 1] = bfhi(t2);
      }
    const float mx = max8_16_32(max3(max3(sc[0], sc[1], sc[2]), max3(sc[3], sc[4], sc[5]), max2(sc[6], sc[7]))) * LOG2E;
    float pr_[8];
#pragma unroll
    for (int r = 0; r < 8; ++r) pr_[r] = __builtin_amdgcn_exp2f(fmaf(sc[r], LOG2E, -mx));
    *(LAS v2u*)pp = v2u{pk2(pr_[0], pr_[1]), pk2(pr_[2], pr_[3])};
    *(LAS v2u*)(pp + 64) = v2u{pk2(pr_[4], pr_[5]), pk2(pr_[6], pr_[7])};
    float sm = ((pr_[0] + pr_[1]) + (pr_[2] + pr_[3])) + ((pr_[4] + pr_[5]) + (pr_[6] + pr_[7]));
    sm += dpp<DPP_R8>(sm);
    sm = sum16_32(sm);
    *(LAS float*)ps = mx; *(LAS float*)(ps + 128) = sm;
    STAMP(4);
    BARRIER();
    RAWBAR();
  }
  if (sl >= 14) { STAMP_FLUSH(); return; }
  s16x8 f2[8];
  LAS char* pr = L + L_P + b8 * 144 + g * 32 + d8 * 16;
  LAS char* pc = L + L_P + 8 * 144 + g * 32 + d8 * 16;
  LAS char* psr = L + L_ST + d8 * 128 + b8 * 16;
  LAS char* pf2 = L + L_R1 + w * 8192 + (8 * g + q4) * 256 + 8 * p4;
  u32 mo = WS_MP + (4 * (4 * w + d8) + g) * MPJ + m * 128 + b8 * 16;
  PIN(mo); PIN(psr); PIN(pr); PIN(pc); PIN(pf2);
  if (hiw) {
    LAS char* pa1 = L + L_R0 + (4 * g + q4) * 64 + ((g & 1) << 5) + 8 * p4;
    LAS char* pz = (l & 15) < 9 ? L + L_P + (u * 9 + (l & 15)) * 144 + 16 * g : L + L_JUNK + 256;
    LAS char* pst = (!d8 && (g == 2 || g == (b8 >> 2))) ? L + L_ST + (g < 2 ? 128 : 0) + b8 * 16 + u * 4 : L + L_JUNK + 512;
    u32 hb0 = WS_H + h0 * 16 + g * 128 + b8 * 16, hb1 = hb0 ^ 8192;
    u32 fh = tid == 448 ? WS_FH + (c - 128) * 128 : WS_END;
    PIN(pa1); PIN(pz); PIN(pst); PIN(hb0); PIN(hb1); PIN(fh);
    v4u hf[8];
#pragma unroll
    for (int i = 0; i < 8; ++i) { hf[i] = v4u{0x3F803F80u, 0x3F803F80u, 0x3F803F80u, 0x3F803F80u}; PIN(hf[i]); }
    VMWAIT(16 - 4 * W2_MID);
    s16x8 f1[2][8];
#pragma unroll
    for (int nt = 0; nt < 2; ++nt)
#pragma unroll
      for (int ks = 0; ks < 8; ++ks) f1[nt][ks] = trfrag((LAS char*)((u32)(size_t)pa1 ^ (32 * nt)) + (ks < 4 ? h0 : h1) * 64 + (ks & 3) * 2048, 1024);
    LWAIT1(F_H);
    STAMP(11);
    if (!d8) {
#pragma unroll
      for (int i = 0; i < 8; ++i) hf[i] = ld16(rsW, (i < 4 ? hb0 : hb1) + (i & 3) * 512);
    }
    VMWAIT(8);
    LSET(F_W + u * 4);
    STAMP(8);
    LWAIT1(F_W + (w >> 1) * 4);
#pragma unroll
    for (int T = 0; T < 8; ++T) f2[T] = trfrag(pf2 + ((T ^ fsw) << 5), 1024);
    FENCE();
    f32x4 z0 = zero4, z1 = zero4, aQ = zero4;
#pragma nounroll
    for (int round = 0; round < 2; ++round) {
      z0 = zero4; z1 = zero4; aQ = zero4;
#pragma unroll
      for (int i = 0; i < 8; ++i) {
        aQ = mfma(hf[i], hf[i], aQ);
        z0 = mfma(f1[0][i], __builtin_bit_cast(s16x8, hf[i]), z0);
        z1 = mfma(f1[1][i], __builtin_bit_cast(s16x8, hf[i]), z1);
      }
      PIN(z0); PIN(z1);
      if (round || none(aQ[0] != aQ[0])) break;
      POLL_LOOP {
        RELOAD();
        if (!d8) {
#pragma unroll
          for (int i = 0; i < 8; ++i) hf[i] = ld16(rsW, (i < 4 ? hb0 : hb1) + (i & 3) * 512);
        }
        u32 mxv = 0;
#pragma unroll
        for (int i = 0; i < 8; ++i) mxv = max(mxv, max4(hf[i]));
        if (none(mxv == SENT)) break;
      }
    }
    STAMP(9);
    *(LAS float*)pst = (p4 & 2) ? ((p4 & 1) ? aQ[3] : aQ[2]) : ((p4 & 1) ? aQ[1] : aQ[0]);
    *(LAS f32x4*)pz = z0; *(LAS f32x4*)(pz + 64) = z1;
    if (w == 7) st8(rsW, fh, tagLo, clk());
  } else {
    if (w == 0) {
      q_published((((l >> 3) & 3) * 4 + 3) * 8 + b8);
      hint_wait4(ws + WS_GH + ((HR_DELAY ? 64 : 32) + xcd * 4 + (sl & 3)) * 128, tagLo, HR_DELAY ? HR_DELAY : H_DELAY);
      LSET(F_H);
    }
    LWAIT1(F_W + (w >> 1) * 4);
#pragma unroll
    for (int T = 0; T < 8; ++T) f2[T] = trfrag(pf2 + ((T ^ fsw) << 5), 1024);
  }
  BARRIER();
  {
    const f32x4 t0 = *(LAS f32x4*)psr;
    const f32x4 z0 = *(LAS f32x4*)pr, z1 = *(LAS f32x4*)(pr + 9 * 144), z2 = *(LAS f32x4*)(pr + 18 * 144), z3 = *(LAS f32x4*)(pr + 27 * 144);
    const f32x4 c0 = *(LAS f32x4*)pc, c1 = *(LAS f32x4*)(pc + 9 * 144), c2 = *(LAS f32x4*)(pc + 18 * 144), c3 = *(LAS f32x4*)(pc + 27 * 144);
    FENCE();
    const float mine = ((t0[0] + t0[1]) + (t0[2] + t0[3])) * (1.f / 1024.f);
    const float other = dpp<DPP_R8>(mine);
    const float mean = d8 ? other : mine, ex2 = d8 ? mine : other;
    u32 mv = pk2(mean, fmaxf(fmaf(-mean, mean, ex2), 0.f));
    asm("" : "+v"(mv));
    const float den = rbf(__builtin_amdgcn_sqrtf(rbf(bfhi(mv) + 1e-5f)));
    const float inv = __builtin_amdgcn_rcpf(den);
    const float mb = bflo(mv);
    float gv[4];
#pragma unroll
    for (int i = 0; i < 4; ++i) gv[i] = fmaf(-mb, (c0[i] + c1[i]) + (c2[i] + c3[i]), (z0[i] + z1[i]) + (z2[i] + z3[i])) * inv;
    const s16x2 zz = {0, 0};
    const int g0 = __builtin_bit_cast(int, __builtin_elementwise_max(__builtin_bit_cast(s16x2, pk2(gv[0], gv[1])), zz));
    const int g1 = __builtin_bit_cast(int, __builtin_elementwise_max(__builtin_bit_cast(s16x2, pk2(gv[2], gv[3])), zz));
    v4u B0, B1;
    B0[0] = __builtin_amdgcn_update_dpp(0, g0, DPP_ID, 0xf, 0x3, false);
    B0[1] = __builtin_amdgcn_update_dpp(0, g1, DPP_ID, 0xf, 0x3, false);
    B0[2] = __builtin_amdgcn_update_dpp(0, g0, DPP_R8, 0xf, 0x3, false);
    B0[3] = __builtin_amdgcn_update_dpp(0, g1, DPP_R8, 0xf, 0x3, false);
    B1[0] = __builtin_amdgcn_update_dpp(0, g0, DPP_R8, 0xf, 0xc, false);
    B1[1] = __builtin_amdgcn_update_dpp(0, g1, DPP_R8, 0xf, 0xc, false);
    B1[2] = __builtin_amdgcn_update_dpp(0, g0, DPP_ID, 0xf, 0xc, false);
    B1[3] = __builtin_amdgcn_update_dpp(0, g1, DPP_ID, 0xf, 0xc, false);
#pragma unroll
    for (int i = 0; i < 2; ++i) {
      f32x4 u0 = mfma(f2[4 * i], __builtin_bit_cast(s16x8, B0), zero4), u1 = mfma(f2[4 * i + 1], __builtin_bit_cast(s16x8, B0), zero4);
      u0 = mfma(f2[4 * i + 2], __builtin_bit_cast(s16x8, B1), u0);
      u1 = mfma(f2[4 * i + 3], __builtin_bit_cast(s16x8, B1), u1);
      __builtin_amdgcn_raw_buffer_store_b128(v4u{pk2(u0[0], u0[1]), pk2(u0[2], u0[3]), pk2(u1[0], u1[1]), pk2(u1[2], u1[3])}, rsW, mo, i * 8 * MPJ, AUX_SC1);
      FENCE();
    }
  }
  STAMP(10);
  STAMP_FLUSH();
}

extern "C" void kernel_launch(void* const* d_in, const int* in_sizes, int n_in, void* d_out, int out_size, void* d_ws, size_t ws_size,
                              hipStream_t stream) {
  (void)hipFuncSetAttribute((const void*)fused_decode_layer, hipFuncAttributeMaxDynamicSharedMemorySize, L_BYTES);
  fused_decode_layer<<<NWG, NTHR, L_BYTES, stream>>>((const char*)d_in[0], (const char*)d_in[1], (const char*)d_in[2], (const char*)d_in[3],
                                                     (const char*)d_in[4], (const char*)d_in[6], (const char*)d_in[7], (char*)d_out, (char*)d_ws);
}
```

```cpp
#define EXP_ROLE 0
#define EXP_HI 0
#define EXP_IDX 0
#include <hip/hip_runtime.h>
#include <hip/hip_bf16.h>
#include <stdint.h>

#define DIAG 1
#define TOUCH_DELAY 2
#define WO_DELAY 6
#define QH_DELAY 4
#define O_DELAY 62
#define OQ_LEAD 42
#define OV_LEAD 12
#define RG_DELAY 100
#define HR_DELAY 96
#define F_DELAY 96
#define OP_NAP 50
#define CODE_LINES 15
#define W2_MID 1
#define ZM 0
#define SZ_W1 ((ZM & 1) ? 0 : 1024 * 3584 * 2)
#define SZ_W2 ((ZM & 2) ? 0 : 3584 * 1024 * 2)
#define SZ_WO ((ZM & 4) ? 0 : 4096 * 1024 * 2)
#define SZ_V ((ZM & 8) ? 0 : 8 * 4 * 4096 * 128 * 2)
#define SZ_K ((ZM & 16) ? 0 : 8 * 4 * 4096 * 128 * 2)
#define SZ_WQ ((ZM & 32) ? 0 : 1024 * 4096 * 2)

#define LAS __attribute__((address_space(3)))
typedef unsigned u32;
typedef unsigned long long u64;
typedef unsigned v4u __attribute__((ext_vector_type(4)));
typedef unsigned v2u __attribute__((ext_vector_type(2)));
typedef float f32x4 __attribute__((ext_vector_type(4)));
typedef float f32x2 __attribute__((ext_vector_type(2)));
typedef short s16x2 __attribute__((ext_vector_type(2)));
typedef short s16x4 __attribute__((ext_vector_type(4)));
typedef short s16x8 __attribute__((ext_vector_type(8)));
typedef __bf16 bf16x2 __attribute__((ext_vector_type(2)));
typedef __bf16 bf16x8 __attribute__((ext_vector_type(8)));
typedef __amdgpu_buffer_rsrc_t rsrc_t;

typedef unsigned s8u __attribute__((ext_vector_type(8)));

namespace {

constexpr int NWG = 256, NTHR = 512;
constexpr u32 SENT = 0xFFFFFFFFu;
constexpr int AUX_SC1 = 16;
constexpr int SPIN_CAP = 1 << 18;
constexpr int LDS_CAP = 1 << 25;
constexpr float LOG2E = 1.4426950408889634f;

constexpr int WS_EPOCH = 0;
constexpr int WS_H = 32768;
constexpr int WS_QG = 65536;
constexpr int WS_QH = 0xA0000;
constexpr int WS_OH = 0xB0000;
constexpr int WS_GH = 0xC0000;
constexpr int WS_G2 = 0xE0000;
constexpr int WS_FH = 0xD0000;
constexpr int WS_OP = 1 << 20;
constexpr int WS_WP = WS_OP + 524288;
constexpr int WS_MP = WS_WP + 524288;
constexpr int MPJ = 112 * 128;
constexpr int WS_END = WS_MP + 128 * MPJ;
constexpr int WS_STAMP = 8 << 20;

constexpr int L_R0 = 0, L_R1 = 65536, L_MISC = 131072;
constexpr int L_ACT = L_MISC;
constexpr int L_P = L_ACT + 8 * 1056;
constexpr int L_ST = L_P + 5376;
constexpr int L_FL = L_ST + 512;
constexpr int F_KR = 0, F_KD = 16, F_V = 32, F_H = 48, F_W = 64;
constexpr int L_JUNK = L_ST + 1024;
constexpr int L_BYTES = L_ST + 2048;

__device__ __forceinline__ float bflo(u32 v) { return __uint_as_float(v << 16); }
__device__ __forceinline__ float bfhi(u32 v) { return __uint_as_float(v & 0xFFFF0000u); }
__device__ __forceinline__ u32 pk2(float lo, float hi) {
  bf16x2 r = {(__bf16)lo, (__bf16)hi};
  return __builtin_bit_cast(u32, r);
}
__device__ __forceinline__ float rbf(float x) { return (float)(__bf16)x; }

template <int CTRL> __device__ __forceinline__ float dpp(float x) {
  return __builtin_bit_cast(float, __builtin_amdgcn_mov_dpp(__builtin_bit_cast(int, x), CTRL, 0xf, 0xf, true));
}
constexpr int DPP_X1 = 0xB1, DPP_X2 = 0x4E, DPP_HM = 0x141, DPP_M = 0x140, DPP_R8 = 0x128, DPP_R4 = 0x124, DPP_ID = 0xE4;
__device__ __forceinline__ float sum16_32(float x) {
  auto s = __builtin_amdgcn_permlane16_swap(__float_as_uint(x), __float_as_uint(x), false, false);
  x = __uint_as_float(s[0]) + __uint_as_float(s[1]);
  auto t = __builtin_amdgcn_permlane32_swap(__float_as_uint(x), __float_as_uint(x), false, false);
  return __uint_as_float(t[0]) + __uint_as_float(t[1]);
}
__device__ __forceinline__ float wave_sum(float x) {
  x += dpp<DPP_X1>(x); x += dpp<DPP_X2>(x); x += dpp<DPP_HM>(x); x += dpp<DPP_M>(x);
  return sum16_32(x);
}
__device__ __forceinline__ f32x4 mfma(s16x8 a, s16x8 b, f32x4 c) {
  return __builtin_amdgcn_mfma_f32_16x16x32_bf16(__builtin_bit_cast(bf16x8, a), __builtin_bit_cast(bf16x8, b), c, 0, 0, 0);
}
__device__ __forceinline__ f32x4 mfma(v4u a, v4u b, f32x4 c) {
  return __builtin_amdgcn_mfma_f32_16x16x32_bf16(__builtin_bit_cast(bf16x8, a), __builtin_bit_cast(bf16x8, b), c, 0, 0, 0);
}
__device__ __forceinline__ s16x8 trfrag(LAS char* p, int half_off) {
  typedef short v4i16_t __attribute__((ext_vector_type(4)));
  s16x4 lo = __builtin_amdgcn_ds_read_tr16_b64_v4i16((LAS v4i16_t*)p);
  s16x4 hi = __builtin_amdgcn_ds_read_tr16_b64_v4i16((LAS v4i16_t*)(p + half_off));
  return __builtin_shufflevector(lo, hi, 0, 1, 2, 3, 4, 5, 6, 7);
}
#define DMA4_ASM(POL) \
  asm volatile("s_mov_b32 m0, %6\n\ts_nop 0\n\t" \
               "buffer_load_dwordx4 %1, %5, %0 offen " POL " lds\n\ts_add_u32 %0, %0, %7\n\t" \
               "buffer_load_dwordx4 %2, %5, %0 offen offset:1024 " POL " lds\n\ts_add_u32 %0, %0, %7\n\t" \
               "buffer_load_dwordx4 %3, %5, %0 offen offset:2048 " POL " lds\n\ts_add_u32 %0, %0, %7\n\t" \
               "buffer_load_dwordx4 %4, %5, %0 offen offset:3072 " POL " lds" \
               : "+s"(soff) : "v"(v0), "v"(v1), "v"(v2), "v"(v3), "s"(rs), "s"(lds), "s"(sdelta) : "memory", "scc")
template <int NT = 1>
__device__ __forceinline__ u32 dma4(rsrc_t rs, u32 v0, u32 v1, u32 v2, u32 v3, u32 soff, u32 sdelta, u32 lds) {
  if constexpr (NT) DMA4_ASM("nt"); else DMA4_ASM("");
  return soff;
}
__device__ __forceinline__ void dma4c(rsrc_t rs, u32 v0, u32 v1, u32 v2, u32 v3, u32 soff, u32 lds) {
  asm volatile("s_mov_b32 m0, %6\n\ts_nop 0\n\t"
               "buffer_load_dwordx4 %1, %5, %0 offen nt lds\n\t"
               "buffer_load_dwordx4 %2, %5, %0 offen offset:1024 nt lds\n\t"
               "buffer_load_dwordx4 %3, %5, %0 offen offset:2048 nt lds\n\t"
               "buffer_load_dwordx4 %4, %5, %0 offen offset:3072 nt lds"
               :: "s"(soff), "v"(v0), "v"(v1), "v"(v2), "v"(v3), "s"(rs), "s"(lds) : "memory");
}
__device__ __forceinline__ void touch(rsrc_t rs, u32 voff, u32 soff, u32 lds) {
  asm volatile("s_mov_b32 m0, %0\n\ts_nop 0\n\tbuffer_load_dword %1, %2, %3 offen lds" ::"s"(lds), "v"(voff), "s"(rs), "s"(soff) : "memory");
}
#define LOAD_TAG() do { u64 e_; asm volatile("s_load_dwordx2 %0, %1, 0x0\n\ts_waitcnt lgkmcnt(0)" : "=s"(e_) : "s"(ws) : "memory"); tagLo = (u32)e_ + 1u; tagHi = (u32)(e_ >> 32) + 1u; } while (0)
#define POLL_LOOP _Pragma("nounroll") for (int it = 0; it < SPIN_CAP; ++it)
#define RELOAD() asm volatile("" ::: "memory")
#define BARRIER() asm volatile("s_waitcnt lgkmcnt(0)\n\ts_barrier" ::: "memory")
#define RAWBAR() asm volatile("s_barrier" ::: "memory")
#define VMWAIT(n) asm volatile("s_waitcnt vmcnt(%0)" :: "i"(n) : "memory")
#define FENCE() __builtin_amdgcn_sched_barrier(0)
#define PIN(x) asm volatile("" : "+v"(x))
template <int N> __device__ __forceinline__ void nap() {
  if constexpr (N > 0) { __builtin_amdgcn_s_sleep(N > 127 ? 127 : N); nap<(N > 127 ? N - 127 : 0)>(); }
}
#define LSET(off) do { RELOAD(); *(volatile LAS u32*)(L + L_FL + (off)) = 1u; } while (0)
#define LWAIT1(off) do { _Pragma("nounroll") for (int it_ = 0; it_ < LDS_CAP; ++it_) { \
    if (__builtin_amdgcn_readfirstlane(*(volatile LAS u32*)(L + L_FL + (off))) != 0u) break; } RELOAD(); } while (0)
#define LWAIT4(off) do { _Pragma("nounroll") for (int it_ = 0; it_ < LDS_CAP; ++it_) { const v4u f_ = *(volatile LAS v4u*)(L + L_FL + (off)); \
    if (__builtin_amdgcn_readfirstlane(f_.x & f_.y & f_.z & f_.w) != 0u) break; } RELOAD(); } while (0)
__device__ __forceinline__ u32 clk() { return (u32)__builtin_amdgcn_s_memrealtime(); }
__device__ __forceinline__ void until(u32 t) {
#pragma nounroll
  for (int it = 0; it < 160; ++it) { if ((int)(clk() - t) >= 0) break; }
}
__device__ __forceinline__ u32 later(u32 a, u32 b) { return (int)(a - b) > 0 ? a : b; }
__device__ __forceinline__ void hint_wait(const char* p, u32 tag, u32 ticks) {
  u64 v;
#pragma nounroll
  for (int it = 0; it < 64; ++it) {
    asm volatile("s_load_dwordx2 %0, %1, 0x0 glc\n\ts_waitcnt lgkmcnt(0)" : "=s"(v) : "s"(p) : "memory");
    if ((u32)v == tag) break;
  }
  until((u32)(v >> 32) + ticks);
}
__device__ __forceinline__ void hint_wait4(const char* p, u32 tag, u32 ticks) {
  s8u v;
#pragma nounroll
  for (int it = 0; it < 64; ++it) {
    asm volatile("s_load_dwordx8 %0, %1, 0x0 glc\n\ts_waitcnt lgkmcnt(0)" : "=s"(v) : "s"(p) : "memory");
    if ((((v[0] ^ tag) | (v[2] ^ tag)) | ((v[4] ^ tag) | (v[6] ^ tag))) == 0u) break;
  }
  until(later(later(v[1], v[3]), later(v[5], v[7])) + ticks);
}
typedef unsigned s16u __attribute__((ext_vector_type(16)));
__device__ __forceinline__ void hint_wait8t(const char* p, u32 tag, u32 ticks) {
  s16u v;
#pragma nounroll
  for (int it = 0; it < 64; ++it) {
    asm volatile("s_load_dwordx16 %0, %1, 0x0 glc\n\ts_waitcnt lgkmcnt(0)" : "=s"(v) : "s"(p) : "memory");
    if (((((v[0] ^ tag) | (v[2] ^ tag)) | ((v[4] ^ tag) | (v[6] ^ tag))) | (((v[8] ^ tag) | (v[10] ^ tag)) | ((v[12] ^ tag) | (v[14] ^ tag)))) == 0u) break;
  }
  until(later(later(later(v[1], v[3]), later(v[5], v[7])), later(later(v[9], v[11]), later(v[13], v[15]))) + ticks);
}
__device__ __forceinline__ void hint_wait8(const char* p, u32 tag) {
#pragma nounroll
  for (int it = 0; it < 64; ++it) {
    s8u v;
    asm volatile("s_load_dwordx8 %0, %1, 0x0 glc\n\ts_waitcnt lgkmcnt(0)" : "=s"(v) : "s"(p) : "memory");
    if ((((v[0] ^ tag) | (v[1] ^ tag)) | ((v[2] ^ tag) | (v[3] ^ tag)) | ((v[4] ^ tag) | (v[5] ^ tag)) | ((v[6] ^ tag) | (v[7] ^ tag))) == 0u) break;
  }
}
__device__ __forceinline__ u32 max4(v4u v) { return max(max(v.x, v.y), max(v.z, v.w)); }
__device__ __forceinline__ float max2(float a, float b) { float r; asm("v_max_f32 %0, %1, %2" : "=v"(r) : "v"(a), "v"(b)); return r; }
__device__ __forceinline__ float max3(float a, float b, float c) { float r; asm("v_max3_f32 %0, %1, %2, %3" : "=v"(r) : "v"(a), "v"(b), "v"(c)); return r; }
__device__ __forceinline__ float max8_16_32(float x) {
  float y;
  asm("s_nop 1\n\tv_max_f32_dpp %0, %0, %0 row_ror:8 row_mask:0xf bank_mask:0xf\n\tv_mov_b32 %1, %0\n\ts_nop 1\n\t"
      "v_permlane16_swap_b32 %0, %1\n\tv_max_f32 %0, %0, %1\n\tv_mov_b32 %1, %0\n\ts_nop 1\n\t"
      "v_permlane32_swap_b32 %0, %1\n\tv_max_f32 %0, %0, %1" : "+v"(x), "=&v"(y));
  return x;
}
__device__ __forceinline__ v4u ld16(rsrc_t rs, u32 off) { return __builtin_amdgcn_raw_buffer_load_b128(rs, off, 0, AUX_SC1); }
__device__ __forceinline__ void st16(rsrc_t rs, u32 off, v4u v) { __builtin_amdgcn_raw_buffer_store_b128(v, rs, off, 0, AUX_SC1); }
__device__ __forceinline__ void st8(rsrc_t rs, u32 off, u32 a, u32 b) { __builtin_amdgcn_raw_buffer_store_b64(v2u{a, b}, rs, off, 0, AUX_SC1); }
__device__ __forceinline__ bool none(bool p) { return __builtin_amdgcn_ballot_w64(p) == 0ull; }
__device__ __forceinline__ rsrc_t mkrs(const void* p, int bytes) { return __builtin_amdgcn_make_buffer_rsrc((void*)p, 0, bytes, 0x00020000); }

__device__ __forceinline__ float dot2(u32 a, u32 b, float c) {
  return __builtin_amdgcn_fdot2_f32_bf16(__builtin_bit_cast(bf16x2, a), __builtin_bit_cast(bf16x2, b), c, false);
}
__device__ __forceinline__ v4u ln_half(const v4u c, const v4u o) {
  float s = 0.f, q = 0.f;
#pragma unroll
  for (int i = 0; i < 4; ++i) { s = dot2(c[i], 0x3F803F80u, s); q = dot2(c[i], c[i], q); s = dot2(o[i], 0x3F803F80u, s); q = dot2(o[i], o[i], q); }
  const float mean = wave_sum(s) * (1.f / 1024.f);
  const float var = fmaxf(fmaf(-mean, mean, wave_sum(q) * (1.f / 1024.f)), 0.f);
  const u32 mv = pk2(mean, var);
  const float mb = bflo(mv);
  const float den = rbf(__builtin_amdgcn_sqrtf(rbf(bfhi(mv) + 1e-5f)));
  const float inv = __builtin_amdgcn_rcpf(den);
  v4u r;
#pragma unroll
  for (int i = 0; i < 4; ++i) {
    const u32 d = pk2(bflo(c[i]) - mb, bfhi(c[i]) - mb);
    const float d0 = bflo(d), d1 = bfhi(d);
    float q0 = d0 * inv, q1 = d1 * inv;
    q0 = fmaf(fmaf(-q0, den, d0), inv, q0);
    q1 = fmaf(fmaf(-q1, den, d1), inv, q1);
    r[i] = pk2(q0, q1);
  }
  return r;
}

#define STAMP(i) do { if ((i) != 0 && (i) != EXP_IDX) break; asm volatile("s_memrealtime s[90:91]\n\ts_waitcnt lgkmcnt(0)\n\tv_writelane_b32 %0, s90, " #i : "+v"(st_) :: "s90", "s91", "memory"); } while (0)
#define STAMP_DECL() u32 st_ = 0
#define STAMP_FLUSH() do { const u32 s0_ = __builtin_amdgcn_readlane(st_, 0), si_ = __builtin_amdgcn_readlane(st_, EXP_IDX); u32 tgt_ = s0_ + 5000; \
  if (((c < 128) == (EXP_ROLE == 0)) && (hiw == (EXP_HI != 0)) && si_ != 0) tgt_ += 20 * (si_ - s0_); \
  for (int i_ = 0; i_ < 200000; ++i_) { if ((int)(clk() - tgt_) >= 0) break; __builtin_amdgcn_s_sleep(2); } } while (0)

}

__global__ void __launch_bounds__(NTHR, 2)
fused_decode_layer(const char* x, const char* kc, const char* vc, const char* wq, const char* wo, const char* w1, const char* w2, char* out, char* ws) {
  extern __shared__ __attribute__((aligned(16))) char smem[];
  STAMP_DECL();
  STAMP(0);
  {
    u32 t0, t1, t2, t3, t4;
    asm volatile(
        "s_load_dwordx8 s[64:71], %[ka], 0x0\n\t"
        "s_getpc_b64 s[82:83]\n\t"
        "v_readfirstlane_b32 s72, %[tid]\n\t"
        "v_and_b32 %[t4], 63, %[tid]\n\t"
        "s_mov_b32 s79, 0x20000\n\t"
        "s_mov_b32 s78, %[szq]\n\t"
        "s_lshr_b32 s72, s72, 6\n\t"
        "s_and_b32 s73, s72, 3\n\t"
        "v_lshrrev_b32 %[t1], 4, %[t4]\n\t"
        "s_bfe_u32 s84, %[c], 0x20003\n\t"
        "s_bfe_u32 s74, %[c], 0x10006\n\t"
        "s_lshl1_add_u32 s84, s84, s74\n\t"
        "s_bfe_u32 s85, %[c], 0x10005\n\t"
        "s_lshl2_add_u32 s85, s85, s73\n\t"
        "s_and_b32 s74, %[c], 0x80\n\t"
        "s_cmp_gt_u32 s72, 3\n\t"
        "s_cbranch_scc1 Lpro_hi\n\t"
        "s_cmp_eq_u32 s74, 0\n\t"
        "s_cbranch_scc1 Lpro_bl\n\t"
        "s_cmp_lg_u32 s72, 3\n\t"
        "s_cbranch_scc1 Lpro_al\n\t"
        "s_bfe_u32 s74, %[c], 0x40003\n\t"
        "s_mul_i32 s74, s74, %[cl]*64\n\t"
        "s_and_b32 s82, s82, 0xffffff00\n\t"
        "s_add_u32 s82, s82, s74\n\t"
        "s_addc_u32 s83, s83, 0\n\t"
        ".irp o,0,1,2,3,4,5,6,7,8,9,10,11,12,13,14,15,16,17\n\t"
        ".if \\o < %[cl]\n\t"
        "s_load_dword s75, s[82:83], \\o*64+1024\n\t"
        ".endif\n\t"
        ".endr\n\t"
        "s_barrier\n\t"
        "s_branch Lpro_end\n\t"
        "Lpro_al:\n\t"
        "v_lshlrev_b32 %[t2], 2, %[t4]\n\t"
        "v_add_u32 %[t2], %[lfl], %[t2]\n\t"
        "v_mov_b32 %[t1], 0\n\t"
        "ds_write_b32 %[t2], %[t1]\n\t"
        "s_lshl1_add_u32 s74, s73, s84\n\t"
        "s_and_b32 s74, s74, 7\n\t"
        "s_add_u32 s75, s74, %[c]\n\t"
        "s_and_b32 s75, s75, 7\n\t"
        "s_lshl3_add_u32 s75, s74, s75\n\t"
        "s_lshl_b32 s75, s75, 7\n\t"
        "s_lshl1_add_u32 s74, s85, 1\n\t"
        "s_lshl_b32 s74, s74, 19\n\t"
        "s_or_b32 s74, s74, s75\n\t"
        "v_lshlrev_b32 %[t0], 13, %[t4]\n\t"
        "s_mov_b32 m0, %[junk]\n\t"
        "s_waitcnt lgkmcnt(0)\n\t"
        "s_mov_b32 s76, s70\n\t"
        "s_and_b32 s77, s71, 0xffff\n\t"
        "buffer_load_dword %[t0], s[76:79], s74 offen  lds\n\t"
        "s_add_u32 s85, s85, 1\n\t"
        "s_add_u32 s73, s73, 1\n\t"
        "s_cmp_eq_u32 s73, 3\n\t"
        "s_cbranch_scc1 Lpro_al\n\t"
        "s_barrier\n\t"
        "s_branch Lpro_end\n\t"
        "Lpro_hi:\n\t"
        "s_lshl_b32 m0, s73, 14\n\t"
        "s_cmp_lg_u32 s74, 0\n\t"
        "s_cbranch_scc1 Lpro_ah\n\t"
        "v_and_b32 %[t1], 1, %[t1]\n\t"
        "v_lshlrev_b32 %[t1], 1, %[t1]\n\t"
        "v_and_b32 %[t0], 7, %[t4]\n\t"
        "v_xor_b32 %[t0], %[t0], %[t1]\n\t"
        "v_lshrrev_b32 %[t2], 3, %[t4]\n\t"
        "v_lshlrev_b32 %[t2], 13, %[t2]\n\t"
        "v_lshl_or_b32 %[t0], %[t0], 4, %[t2]\n\t"
        "v_xor_b32 %[t1], 64, %[t0]\n\t"
        "s_add_u32 s75, s84, %[c]\n\t"
        "s_and_b32 s75, s75, 7\n\t"
        "s_lshl3_add_u32 s75, s84, s75\n\t"
        "s_lshl_b32 s75, s75, 7\n\t"
        "s_lshl_b32 s74, s85, 20\n\t"
        "s_or_b32 s74, s74, s75\n\t"
        "s_mov_b32 s75, 0xfc00\n\t"
        "s_mov_b32 s73, 4\n\t"
        "s_waitcnt lgkmcnt(0)\n\t"
        "s_mov_b32 s76, s70\n\t"
        "s_and_b32 s77, s71, 0xffff\n\t"
        "Lpro_bh_loop:\n\t"
        "buffer_load_dwordx4 %[t0], s[76:79], s74 offen nt lds\n\t"
        "s_add_u32 s74, s74, s75\n\t"
        "buffer_load_dwordx4 %[t1], s[76:79], s74 offen offset:1024 nt lds\n\t"
        "s_add_u32 s74, s74, s75\n\t"
        "buffer_load_dwordx4 %[t0], s[76:79], s74 offen offset:2048 nt lds\n\t"
        "s_add_u32 s74, s74, s75\n\t"
        "buffer_load_dwordx4 %[t1], s[76:79], s74 offen offset:3072 nt lds\n\t"
        "s_add_u32 m0, m0, 0x1000\n\t"
        "s_add_u32 s74, s74, 0x10c00\n\t"
        "s_sub_u32 s73, s73, 1\n\t"
        "s_cmp_lg_u32 s73, 0\n\t"
        "s_cbranch_scc1 Lpro_bh_loop\n\t"
        "s_branch Lpro_end\n\t"
        "Lpro_bl:\n\t"
        "v_lshlrev_b32 %[t0], 4, %[t4]\n\t"
        "v_and_b32 %[t1], 31, %[t4]\n\t"
        "v_lshlrev_b32 %[t1], 13, %[t1]\n\t"
        "s_lshl_b32 s74, s73, 12\n\t"
        "s_add_u32 m0, s74, %[r1]\n\t"
        "s_waitcnt lgkmcnt(0)\n\t"
        "s_and_b32 s65, s65, 0xffff\n\t"
        "s_movk_i32 s66, 0x4000\n\t"
        "s_mov_b32 s67, s79\n\t"
        "buffer_load_dwordx4 %[t0], s[64:67], s74 offen lds\n\t"
        "buffer_load_dwordx4 %[t0], s[64:67], s74 offen offset:1024 lds\n\t"
        "buffer_load_dwordx4 %[t0], s[64:67], s74 offen offset:2048 lds\n\t"
        "buffer_load_dwordx4 %[t0], s[64:67], s74 offen offset:3072 lds\n\t"
        "s_cmp_lg_u32 s72, 0\n\t"
        "s_cbranch_scc1 Lpro_end\n\t"
        "s_lshr_b32 s74, s70, 7\n\t"
        "s_sub_u32 s74, 3, s74\n\t"
        "s_and_b32 s74, s74, 7\n\t"
        "s_sub_u32 s75, s74, %[c]\n\t"
        "s_and_b32 s75, s75, 7\n\t"
        "s_lshl3_add_u32 s75, s75, s74\n\t"
        "s_lshl_b32 s75, s75, 7\n\t"
        "s_bfe_u32 s82, %[c], 0x30003\n\t"
        "s_bfe_u32 s83, %[c], 0x10006\n\t"
        "s_lshl2_add_u32 s82, s82, s83\n\t"
        "s_lshl_b32 s82, s82, 18\n\t"
        "s_or_b32 s74, s82, s75\n\t"
        "s_mov_b32 m0, %[junk]\n\t"
        "s_mov_b32 s76, s70\n\t"
        "s_and_b32 s77, s71, 0xffff\n\t"
        "buffer_load_dword %[t1], s[76:79], s74 offen lds\n\t"
        "s_branch Lpro_end\n\t"
        "Lpro_ah:\n\t"
        "v_and_b32 %[t4], 15, %[t4]\n\t"
        "v_xor_b32 %[t0], %[t4], %[t1]\n\t"
        "v_lshlrev_b32 %[t1], 8, %[t1]\n\t"
        "v_lshl_or_b32 %[t0], %[t0], 4, %[t1]\n\t"
        "v_xor_b32 %[t1], 64, %[t0]\n\t"
        "v_xor_b32 %[t2], 0x80, %[t0]\n\t"
        "v_xor_b32 %[t3], 0x80, %[t1]\n\t"
        "s_bfe_u32 s74, %[c], 0x20005\n\t"
        "s_and_b32 s75, %[c], 7\n\t"
        "s_lshl3_add_u32 s74, s74, s75\n\t"
        "s_bfe_u32 s75, %[c], 0x20003\n\t"
        "s_lshl4_add_u32 s74, s74, s75\n\t"
        "s_or_b32 s74, s74, 12\n\t"
        "s_lshl2_add_u32 s74, s74, s73\n\t"
        "s_lshl_b32 s74, s74, 14\n\t"
        "s_mov_b32 s78, %[szk]\n\t"
        "s_mov_b32 s73, 4\n\t"
        "s_waitcnt lgkmcnt(0)\n\t"
        "s_barrier\n\t"
        "s_mov_b32 s76, s66\n\t"
        "s_and_b32 s77, s67, 0xffff\n\t"
        "Lpro_k_loop:\n\t"
        "buffer_load_dwordx4 %[t0], s[76:79], s74 offen nt lds\n\t"
        "buffer_load_dwordx4 %[t1], s[76:79], s74 offen offset:1024 nt lds\n\t"
        "buffer_load_dwordx4 %[t2], s[76:79], s74 offen offset:2048 nt lds\n\t"
        "buffer_load_dwordx4 %[t3], s[76:79], s74 offen offset:3072 nt lds\n\t"
        "s_add_u32 m0, m0, 0x1000\n\t"
        "s_addk_i32 s74, 0x1000\n\t"
        "s_sub_u32 s73, s73, 1\n\t"
        "s_cmp_lg_u32 s73, 0\n\t"
        "s_cbranch_scc1 Lpro_k_loop\n\t"
        "Lpro_end:\n\t"
        ".pushsection .text,1\n\t"
        ".fill 512, 4, 0xBF800000\n\t"
        ".popsection\n\t"
        "s_waitcnt lgkmcnt(0)"
        : [t0] "=&v"(t0), [t1] "=&v"(t1), [t2] "=&v"(t2), [t3] "=&v"(t3), [t4] "=&v"(t4)
        : [ka] "s"(__builtin_amdgcn_kernarg_segment_ptr()), [tid] "v"(threadIdx.x), [c] "s"(blockIdx.x), [cl] "i"(CODE_LINES),
          [junk] "i"(L_JUNK), [r1] "i"(L_R1),
          [szk] "i"(SZ_K), [szq] "i"(SZ_WQ), [lfl] "i"(L_FL)
        : "s64", "s65", "s66", "s67", "s68", "s69", "s70", "s71", "s72", "s73", "s74", "s75", "s76", "s77", "s78", "s79",
          "s82", "s83", "s84", "s85", "scc", "memory");
  }
  STAMP(1);
  LAS char* L = (LAS char*)smem;
  const int tid = threadIdx.x, l = tid & 63, w = __builtin_amdgcn_readfirstlane(tid >> 6), c = blockIdx.x;
  const int g = l >> 4, xcd = c & 7, sl = (c >> 3) & 15, u = w & 3;
  const bool hiw = w >= 4;
  const int bk = (sl >> 2) * 8 + xcd;
  const u32 kvb = (bk * 4096 + 3072 + 256 * (sl & 3)) * 256;
  const int q4 = (l >> 2) & 3, p4 = l & 3, b8 = l & 7, d8 = (l >> 3) & 1;
  const int fsw = q4 | ((g & 1) << 2);
  const rsrc_t rsW = mkrs(ws, WS_END);
  const f32x4 zero4 = {0.f, 0.f, 0.f, 0.f};
  u32 tagLo = 0, tagHi = 0;
  auto q_published = [&](int cb) {
    const int s_ = cb >> 3, bd = 2 * (s_ & 3) + (s_ >> 3);
    const u32 off = WS_QG + xcd * 65536 + (((s_ & 3) * 2 + ((s_ >> 2) & 1)) * 16 + (bd & 1) * 8 + ((cb + bd) & 7)) * 512;
    POLL_LOOP {
      RELOAD();
      const v4u f0 = ld16(rsW, off);
      if (none(((f0.y ^ tagLo) | (f0.w ^ tagHi)) != 0u)) break;
      nap<4>();
    }
  };

  auto selector = [&]() {
    const u32 one = (l & 8) ? 0u : ((l & 1) ? 0x3F800000u : 0x00003F80u);
    const int si = b8 >> 1;
    return v4u{si == 0 ? one : 0u, si == 1 ? one : 0u, si == 2 ? one : 0u, si == 3 ? one : 0u};
  };

  if (c < 128) {
    const int band = 2 * (sl & 3) + (sl >> 3), cg = 8 * band + ((xcd + band) & 7), kh = (sl >> 2) & 1;
    if (!hiw) {
      LAS char* px = L + L_R1 + w * 4096 + l * 16;
      if (w == 0) VMWAIT(1); else VMWAIT(0);
      STAMP(13);
      const v4u xc0 = *(LAS v4u*)(px + kh * 1024), xo0 = *(LAS v4u*)(px + (kh ^ 1) * 1024);
      const v4u xc1 = *(LAS v4u*)(px + 2048 + kh * 1024), xo1 = *(LAS v4u*)(px + 2048 + (kh ^ 1) * 1024);
      *(LAS v4u*)(L + L_ACT + (2 * w) * 1056 + l * 16) = ln_half(xc0, xo0);
      *(LAS v4u*)(L + L_ACT + (2 * w + 1) * 1056 + l * 16) = ln_half(xc1, xo1);
      STAMP(12);
    }
    const v4u sent4 = {SENT, SENT, SENT, SENT};
    const int j = c, kvo = sl >> 2, nq = sl & 3, ksl = kvo * 8 + xcd;
    BARRIER();
    if (w == 7) {
#pragma unroll
      for (int i = 0; i < 4; ++i) __builtin_amdgcn_raw_buffer_store_b128(sent4, rsW, WS_WP + (32 * nq + (l >> 3)) * 4096 + ksl * 128 + b8 * 16, i * 8 * 4096, AUX_SC1);
      st16(rsW, (nq == 3 && l < 32) ? WS_H + ksl * 512 + l * 16 : WS_END, sent4);
    }
    const int bq = 2 * u + (l >> 5), cp = l & 31;
    LAS char* pq = L + L_P + bq * 272 + cp * 8;
    u32 qo = WS_QG + (((bq * 4 + (cg >> 4)) * 2 + kh) * 1024 + 64 * (cg & 15) + 2 * cp) * 8;
    {
      const int kh2 = w >> 2;
      s16x8 bf[8];
      LAS char* pb = L + L_ACT + b8 * 1056 + kh2 * 512 + 16 * g;
#pragma unroll
      for (int ks = 0; ks < 8; ++ks) bf[ks] = *(LAS s16x8*)(pb + ks * 64);
      LAS char* pa = L + L_R0 + kh2 * 32768 + (8 * g + q4) * 128 + ((u ^ (((q4 >> 1) & 1) | ((g & 1) << 1))) << 5) + 8 * p4;
      LAS char* pw = L + L_P + (kh2 * 8 + b8) * 272 + (16 * u + 4 * g) * 4;
      u32 ho = (w == 6 && l < 8) ? WS_QH + (l * 16 + (cg >> 2)) * 128 + ((cg & 3) * 2 + kh) * 4 : WS_END;
      PIN(pa); PIN(pw); PIN(pq); PIN(qo); PIN(ho);
      LOAD_TAG();
      if (w == 7) VMWAIT(5); else if (hiw) VMWAIT(0);
      STAMP(2);
      RAWBAR();
      if (w == 6) __builtin_amdgcn_raw_buffer_store_b32(tagLo, rsW, ho, 0, AUX_SC1);
      s16x8 af[8];
#pragma unroll
      for (int ks = 0; ks < 8; ++ks) af[ks] = trfrag(pa + ks * 4096, 512);
      FENCE();
      f32x4 acc = zero4;
#pragma unroll
      for (int ks = 0; ks < 8; ++ks) acc = mfma(af[ks], bf[ks], acc);
      if (!d8) *(LAS f32x4*)pw = acc;
    }
    if (w == 7) VMWAIT(0);
    BARRIER();
    if (!hiw) {
      const f32x2 s = *(LAS f32x2*)pq + *(LAS f32x2*)(pq + 8 * 272);
      st16(rsW, qo, v4u{__float_as_uint(s[0]), tagLo, __float_as_uint(s[1]), tagHi});
      STAMP(3);
      {
        nap<1>();
#pragma unroll
        for (int i = 0; i < 4; ++i) st16(rsW, u * 4 + i < 14 ? WS_MP + j * MPJ + (u * 4 + i) * 1024 + l * 16 : WS_END, sent4);
      }
      nap<TOUCH_DELAY>();
      const rsrc_t rv = mkrs(vc, SZ_V), ro_ = mkrs(wo, SZ_WO);
      touch(rv, g * 16384 + (l & 15) * 128, kvb + (4 + u) * 2048, L_JUNK);
      nap<WO_DELAY>();
      const u32 vo = (l >> 5) * 2048 + (((((l & 31) >> 1) ^ (l >> 5)) << 1) | (l & 1)) * 16;
#pragma unroll
      for (int i = 0; i < 4; ++i) {
        const int h = 2 * u + (i >> 1), half = i & 1;
        const u32 vx = vo ^ (128 * half);
        dma4(ro_, vx, vx ^ 64, vx, vx ^ 64, ((kvo * 8 + h) * 128 + 16 * xcd + 8 * half) * 2048 + 512 * nq, 4096 - 1024, L_R0 + (8 * h + 4 * half) * 1024);
      }
      STAMP(4);
    } else {
      nap<OP_NAP>();
    }
    {
      u32 ob = WS_OP + (ksl * 8 + w) * 2048 + l * 16;
      LAS char* po = L + L_ACT + w * 288 + b8 * 32 + ((l >> 3) & 3) * 8 + (l >> 5) * 4;
      u32 wpo = d8 ? WS_END : WS_WP + (4 * (8 * nq + w) + g) * 4096 + ksl * 128 + b8 * 16;
      LAS char* pb = L + L_ACT + b8 * 288 + 16 * g;
      LAS char* pa0 = L + L_R0 + (8 * g + q4) * 512 + (((2 * w) ^ fsw) << 5) + 8 * p4;
      u32 gho = l < 4 ? WS_G2 + (nq * 4 + l) * 256 + ksl * 8 : WS_END;
      PIN(gho); PIN(ob); PIN(po); PIN(wpo); PIN(pb); PIN(pa0);
      hint_wait4(ws + WS_OH + ((w * 4 + kvo) * 8 + xcd) * 128, tagLo, O_DELAY);
      STAMP(6);
      v4u d0, d1;
      asm volatile("buffer_load_dwordx4 %0, %2, %3, 0 offen sc1\n\tbuffer_load_dwordx4 %1, %2, %3, 0 offen offset:1024 sc1" : "=&v"(d0), "=&v"(d1) : "v"(ob), "s"(rsW) : "memory");
      if (!hiw) VMWAIT(2);
      STAMP(5);
      RAWBAR();
      if (w == 7) {
        const v2u hv = {tagLo, clk()};
        asm volatile("buffer_store_dwordx2 %0, %1, %2, 0 offen sc1" :: "v"(hv), "v"(gho), "s"(rsW) : "memory");
      }
      s16x8 wf[2][4];
#pragma unroll
      for (int T = 0; T < 2; ++T)
#pragma unroll
        for (int ks = 0; ks < 4; ++ks) wf[T][ks] = trfrag((LAS char*)(size_t)((u32)(size_t)pa0 ^ (32 * T)) + ks * 16384, 2048);
      asm volatile("s_cmp_eq_u32 %2, 7\n\ts_cbranch_scc1 Lo_done\n\ts_waitcnt vmcnt(0)\nLo_done:\n\ts_waitcnt vmcnt(1)" : "+v"(d0), "+v"(d1) : "s"(w) : "memory", "scc");
      FENCE();
      const u32 okey = 2u * tagLo + 1u;
      if (!none(((d0.y ^ d0.w ^ okey) | (d1.y ^ d1.w ^ okey)) != 0u)) {
        POLL_LOOP {
          RELOAD();
          d0 = ld16(rsW, ob); d1 = ld16(rsW, ob + 1024);
          if (none(((d0.y ^ d0.w ^ okey) | (d1.y ^ d1.w ^ okey)) != 0u)) break;
        }
      }
      const float l0 = __uint_as_float(d0.w ^ tagHi), l1 = __uint_as_float(d1.w ^ tagHi);
      float mx = max2(l0, l1);
      { auto t = __builtin_amdgcn_permlane32_swap(__float_as_uint(mx), __float_as_uint(mx), false, false); mx = max2(__uint_as_float(t[0]), __uint_as_float(t[1])); }
      const float w0 = __builtin_amdgcn_exp2f(l0 - mx), w1_ = __builtin_amdgcn_exp2f(l1 - mx);
      float tot = w0 + w1_;
      { auto t = __builtin_amdgcn_permlane32_swap(__float_as_uint(tot), __float_as_uint(tot), false, false); tot = __uint_as_float(t[0]) + __uint_as_float(t[1]); }
      const float it_ = __builtin_amdgcn_rcpf(tot);
      const float v0 = fmaf(w0, bflo(d0.x), w1_ * bflo(d1.x)), v1 = fmaf(w0, bfhi(d0.x), w1_ * bfhi(d1.x));
      const float v2 = fmaf(w0, bflo(d0.z), w1_ * bflo(d1.z)), v3 = fmaf(w0, bfhi(d0.z), w1_ * bfhi(d1.z));
      auto t0 = __builtin_amdgcn_permlane32_swap(__float_as_uint(v0), __float_as_uint(v2), false, false);
      auto t1 = __builtin_amdgcn_permlane32_swap(__float_as_uint(v1), __float_as_uint(v3), false, false);
      *(LAS u32*)po = pk2((__uint_as_float(t0[0]) + __uint_as_float(t0[1])) * it_, (__uint_as_float(t1[0]) + __uint_as_float(t1[1])) * it_);
      STAMP(8);
      BARRIER();
      s16x8 bf[4];
#pragma unroll
      for (int ks = 0; ks < 4; ++ks) bf[ks] = *(LAS s16x8*)(pb + ks * 64);
      FENCE();
      f32x4 a0 = zero4, a1 = zero4;
#pragma unroll
      for (int ks = 0; ks < 4; ++ks) { a0 = mfma(wf[0][ks], bf[ks], a0); a1 = mfma(wf[1][ks], bf[ks], a1); }
      st16(rsW, wpo, v4u{pk2(a0[0], a0[1]), pk2(a0[2], a0[3]), pk2(a1[0], a1[1]), pk2(a1[2], a1[3])});
      STAMP(9);
    }
    {
      RAWBAR();
      const int id = 28 * W2_MID + w * 16 + sl;
      if (id < 112) touch(mkrs(w2, SZ_W2), l * 128, (xcd * 14 + id % 14) * 65536 + (id / 14) * 8192, L_JUNK);
    }
    if (w >= 6) {
      const int fb = 4 * (w & 1) + p4;
      u32 mb = WS_MP + j * MPJ + (l >> 2) * 128 + fb * 16;
      u32 oo = (l & 44) == 0 ? fb * 2048 + (32 * (j >> 2) + 4 * (j & 3) + 16 * (g & 1)) * 2 : 16384;
      u32 hro = WS_H + j * 128 + fb * 16 + (g & 1) * 8;
      v4u sel = selector();
      PIN(mb); PIN(oo); PIN(hro); PIN(sel);
      nap<24>();
      q_published(((j >> 5) * 4 + 3) * 8 + ((j >> 2) & 7));
      hint_wait(ws + WS_FH + (sl < 14 ? j : j - 16) * 128, tagLo, F_DELAY);
      STAMP(7);
      v2u hres;
      f32x4 acc;
      POLL_LOOP {
        RELOAD();
        hres = __builtin_amdgcn_raw_buffer_load_b64(rsW, hro, 0, AUX_SC1);
        v4u d[7];
#pragma unroll
        for (int i = 0; i < 7; ++i) d[i] = __builtin_amdgcn_raw_buffer_load_b128(rsW, mb + (i & 1) * 2048, (i >> 1) * 4096, AUX_SC1);
        FENCE();
        acc = zero4;
#pragma unroll
        for (int i = 0; i < 7; ++i) acc = mfma(sel, d[i], acc);
        const bool hbad = max(hres.x, hres.y) == SENT;
        if (none(hbad || acc[0] != acc[0])) break;
        u32 mxv = 0;
#pragma unroll
        for (int i = 0; i < 7; ++i) mxv = max(mxv, max4(d[i]));
        if (none(hbad || mxv == SENT)) break;
      }
      STAMP(13);
#pragma unroll
      for (int i = 0; i < 4; ++i) { acc[i] += dpp<DPP_R8>(acc[i]); acc[i] += dpp<DPP_R4>(acc[i]); }
      u32 y01 = pk2(acc[0], acc[1]), y23 = pk2(acc[2], acc[3]);
      PIN(y01); PIN(y23);
      const v2u r = {pk2(bflo(hres.x) + bflo(y01), bfhi(hres.x) + bfhi(y01)), pk2(bflo(hres.y) + bflo(y23), bfhi(hres.y) + bfhi(y23))};
      __builtin_amdgcn_raw_buffer_store_b64(r, mkrs(out, 8 * 1024 * 2), oo, 0, 0);
      if (j == 0 && tid == 448) st8(rsW, WS_EPOCH, tagLo, tagHi);
      STAMP(11);
    }
    STAMP_FLUSH();
    return;
  }

  const int s = sl & 3, b = bk >> 2, kvh = bk & 3;
  const int m = xcd * 14 + sl;
  const int h0 = (m & 1) * 512 + 128 * u, h1 = h0 ^ 512;
  if (hiw) {
    const rsrc_t rv = mkrs(vc, SZ_V), r1 = mkrs(w1, SZ_W1), r2 = mkrs(w2, SZ_W2);
    const u32 vv = g * 256 + (((((l & 15) >> 1) ^ g) << 1) | (l & 1)) * 16;
#pragma unroll
    for (int i = 0; i < 2; ++i) dma4c(rv, vv, vv, vv ^ 128, vv ^ 128, kvb + u * 16384 + i * 4096, L_R1 + u * 16384 + i * 4096);
    LOAD_TAG();
    LAS char* pa = L + L_R1 + (8 * g + q4) * 256 + (((2 * u) ^ fsw) << 5) + 8 * p4;
    LAS char* pf_ = L + L_P + b8 * 544 + 16 * g;
    LAS char* ps = L + L_ST + b8 * 16;
    u32 oo = WS_OP + (((kvh * 8 + 2 * u + d8) * 8 + b) * 4 + s) * 512 + g * 128 + b8 * 16;
    u32 v1 = (l >> 2) * 7168 + ((((l >> 1) & 1) ^ ((l >> 4) & 1)) << 5) + (l & 1) * 16;
    PIN(pa); PIN(pf_); PIN(ps); PIN(oo); PIN(v1);
    VMWAIT(8);
    LSET(F_KR + u * 4);
    STAMP(2);
    LWAIT4(F_KD);
#pragma unroll
    for (int i = 2; i < 4; ++i) dma4c(rv, vv, vv, vv ^ 128, vv ^ 128, kvb + u * 16384 + i * 4096, L_R1 + u * 16384 + i * 4096);
    VMWAIT(8);
    RAWBAR();
    const u32 tq = clk();
    s16x8 pf[8], vf[2][8];
    auto read_v = [&](int first) {
#pragma unroll
      for (int ks = first; ks < 8; ks += 2) {
        vf[0][ks] = trfrag(pa + ks * 8192, 1024);
        vf[1][ks] = trfrag((LAS char*)(size_t)((u32)(size_t)pa ^ 32) + ks * 8192, 1024);
      }
    };
    read_v(0);
    {
      const int lc = (3 - (int)(((u32)(size_t)w1) >> 7) - 7 * xcd) & 7;
      touch(r1, (lc < 7 && l < 16) ? l * 7168 : 0x7fffffff, (sl * 64 + u * 16) * 7168 + (xcd * 7 + lc) * 128, L_JUNK);
    }
    VMWAIT(1);
    LSET(F_V + u * 4);
    LWAIT4(F_V);
    STAMP(3);
    if (w == 7) st8(rsW, l < 8 ? WS_OH + (bk * 8 + l) * 128 + s * 8 : WS_END, tagLo, later(tq + OQ_LEAD, clk() + OV_LEAD));
    read_v(1);
    BARRIER();
    STAMP(5);
    const f32x4 m4 = *(LAS f32x4*)ps, s4 = *(LAS f32x4*)(ps + 128);
#pragma unroll
    for (int ks = 0; ks < 8; ++ks) pf[ks] = *(LAS s16x8*)(pf_ + ks * 64);
    FENCE();
    f32x4 ac[2][4];
#pragma unroll
    for (int i = 0; i < 4; ++i)
#pragma unroll
      for (int T = 0; T < 2; ++T) ac[T][i] = mfma(vf[T][2 * i + 1], pf[2 * i + 1], mfma(vf[T][2 * i], pf[2 * i], zero4));
    const float mm = max2(max3(m4[0], m4[1], m4[2]), m4[3]);
    f32x4 f;
#pragma unroll
    for (int i = 0; i < 4; ++i) f[i] = __builtin_amdgcn_exp2f(m4[i] - mm);
    const float tot = fmaf(f[0], s4[0], f[1] * s4[1]) + fmaf(f[2], s4[2], f[3] * s4[3]);
    const u32 lk = __float_as_uint(mm + __builtin_amdgcn_logf(tot)) ^ tagHi;
    f *= __builtin_amdgcn_rcpf(tot);
    const f32x4 o0 = ac[0][0] * f[0] + ac[0][1] * f[1] + ac[0][2] * f[2] + ac[0][3] * f[3];
    const f32x4 o1 = ac[1][0] * f[0] + ac[1][1] * f[1] + ac[1][2] * f[2] + ac[1][3] * f[3];
    u32 x0 = pk2(o0[0], o0[1]), x1 = pk2(o0[2], o0[3]), y0 = pk2(o1[0], o1[1]), y1 = pk2(o1[2], o1[3]);
    PIN(x0); PIN(x1); PIN(y0); PIN(y1);
    st16(rsW, oo, v4u{d8 ? y0 : x0, lk ^ (2u * tagLo + 1u), d8 ? y1 : x1, lk});
    STAMP(6);
    RAWBAR();
    if (sl < 14) {
      const u32 v2o = vv + g * (2048 - 256);
      auto load_w2 = [&](int i) {
        const u32 vx = v2o ^ (128 * (i & 1)), so = (32 * m + 8 * i) * 2048 + 512 * u, ld = L_R1 + 16384 * u + 2048 * i;
        asm volatile("s_mov_b32 m0, %2\n\ts_nop 0\n\tbuffer_load_dwordx4 %0, %1, %3 offen nt lds\n\tbuffer_load_dwordx4 %0, %1, %4 offen offset:1024 nt lds\n\t"
                     "s_add_u32 m0, m0, 0x1f00\n\ts_nop 0\n\tbuffer_load_dwordx4 %0, %1, %3 offen offset:256 nt lds\n\tbuffer_load_dwordx4 %0, %1, %4 offen offset:1280 nt lds"
                     :: "v"(vx), "s"(r2), "s"(ld), "s"(so), "s"(so + 4 * 2048 - 1024) : "memory", "scc");
      };
#pragma unroll
      for (int i = 0; i < 2; ++i) dma4<0>(r1, v1, v1, v1, v1, (h0 + 64 * i) * 7168 + 64 * m, 16 * 7168 - 1024, L_R0 + (h0 + 64 * i) * 64);
#pragma unroll
      for (int i = 0; i < W2_MID; ++i) load_w2(i);
      STAMP(4);
#pragma unroll
      for (int i = 0; i < 2; ++i) dma4(r1, v1, v1, v1, v1, (h1 + 64 * i) * 7168 + 64 * m, 16 * 7168 - 1024, L_R0 + (h1 + 64 * i) * 64);
      STAMP(12);
#pragma unroll
      for (int i = W2_MID; i < 4; ++i) load_w2(i);
      STAMP(7);
    }
  } else {
    LOAD_TAG();
    LAS char* pk = L + L_R0 + (64 * u + (l & 15)) * 256;
    LAS char* pb = L + L_ACT + b8 * 288 + 16 * g;
    LAS char* pqw = L + L_ACT + 2 * u * 288 + l * 4;
    LAS char* pp = L + L_P + b8 * 544 + (64 * u + 16 * d8 + 4 * g) * 2;
    LAS char* ps = l < 8 ? L + L_ST + b8 * 16 + u * 4 : L + L_JUNK + 256;
    u32 qb = WS_QG + bk * 16384 + u * 2048 + l * 16;
    PIN(pb); PIN(pp); PIN(ps); PIN(pqw); PIN(qb);
    hint_wait8(ws + WS_QH + (b * 16 + kvh * 4 + u) * 128, tagLo);
    nap<QH_DELAY>();
    STAMP(12);
    v4u d[4];
#pragma unroll
    for (int i = 0; i < 4; ++i) d[i] = __builtin_amdgcn_raw_buffer_load_b128(rsW, qb + (i & 1) * 1024, (i >> 1) * 8192, AUX_SC1);
    LSET(F_KD + u * 4);
    LWAIT1(F_KR + u * 4);
    s16x8 kf[4][4];
#pragma unroll
    for (int ks = 0; ks < 4; ++ks) {
      const int pos = ((4 * ks + g) ^ (l & 15)) * 16;
#pragma unroll
      for (int T = 0; T < 4; ++T) kf[T][ks] = *(LAS s16x8*)(pk + T * 4096 + pos);
    }
    FENCE();
    auto q_bad = [&]() { return (((d[0].y ^ tagLo) | (d[1].y ^ tagLo)) | ((d[2].y ^ tagLo) | (d[3].y ^ tagLo))) | (((d[0].w ^ tagHi) | (d[1].w ^ tagHi)) | ((d[2].w ^ tagHi) | (d[3].w ^ tagHi))); };
    if (!none(q_bad() != 0u)) {
      POLL_LOOP {
        RELOAD();
#pragma unroll
        for (int i = 0; i < 4; ++i) d[i] = __builtin_amdgcn_raw_buffer_load_b128(rsW, qb + (i & 1) * 1024, (i >> 1) * 8192, AUX_SC1);
        if (none(q_bad() != 0u)) break;
      }
    }
#pragma unroll
    for (int i = 0; i < 2; ++i)
      *(LAS u32*)(pqw + i * 288) = pk2(__uint_as_float(d[i].x) + __uint_as_float(d[i + 2].x), __uint_as_float(d[i].z) + __uint_as_float(d[i + 2].z));
    STAMP(2);
    BARRIER();
    STAMP(3);
    s16x8 bf[4];
#pragma unroll
    for (int ks = 0; ks < 4; ++ks) bf[ks] = *(LAS s16x8*)(pb + ks * 64);
    FENCE();
    f32x4 ac[4] = {zero4, zero4, zero4, zero4};
#pragma unroll
    for (int ks = 0; ks < 4; ++ks) { ac[0] = mfma(kf[0][ks], bf[ks], ac[0]); ac[1] = mfma(kf[1][ks], bf[ks], ac[1]); }
    FENCE();
#pragma unroll
    for (int ks = 0; ks < 4; ++ks) { ac[2] = mfma(kf[2][ks], bf[ks], ac[2]); ac[3] = mfma(kf[3][ks], bf[ks], ac[3]); }
    float sc[8];
#pragma unroll
    for (int pr = 0; pr < 2; ++pr)
#pragma unroll
      for (int r = 0; r < 4; r += 2) {
        u32 t = pk2(d8 ? ac[2 * pr + 1][r] : ac[2 * pr][r], d8 ? ac[2 * pr + 1][r + 1] : ac[2 * pr][r + 1]);
        asm("" : "+v"(t));
        u32 t2 = pk2(bflo(t) * 0.08838834764831845f, bfhi(t) * 0.08838834764831845f);
        asm("" : "+v"(t2));
        sc[4 * pr + r] = bflo(t2); sc[4 * pr + r + 1] = bfhi(t2);
      }
    const float mx = max8_16_32(max3(max3(sc[0], sc[1], sc[2]), max3(sc[3], sc[4], sc[5]), max2(sc[6], sc[7]))) * LOG2E;
    float pr_[8];
#pragma unroll
    for (int r = 0; r < 8; ++r) pr_[r] = __builtin_amdgcn_exp2f(fmaf(sc[r], LOG2E, -mx));
    *(LAS v2u*)pp = v2u{pk2(pr_[0], pr_[1]), pk2(pr_[2], pr_[3])};
    *(LAS v2u*)(pp + 64) = v2u{pk2(pr_[4], pr_[5]), pk2(pr_[6], pr_[7])};
    float sm = ((pr_[0] + pr_[1]) + (pr_[2] + pr_[3])) + ((pr_[4] + pr_[5]) + (pr_[6] + pr_[7]));
    sm += dpp<DPP_R8>(sm);
    sm = sum16_32(sm);
    *(LAS float*)ps = mx; *(LAS float*)(ps + 128) = sm;
    STAMP(4);
    BARRIER();
    RAWBAR();
  }
  if (sl >= 14) {
    const int f = (sl - 14) * 8 + xcd, rj = f * 8 + w, grp = f >> 2;
    v4u sel = selector();
    u32 wb = WS_WP + rj * 4096 + l * 16, hb = l < 8 ? WS_H + rj * 128 + l * 16 : WS_END;
    u32 rho = l < 8 ? WS_GH + ((f & 3) * 8 + l) * 128 + grp * 8 : WS_END;
    v2u xres = __builtin_amdgcn_raw_buffer_load_b64(mkrs(x, 8 * 1024 * 2), b8 * 2048 + (32 * (rj >> 2) + 4 * (rj & 3) + 16 * (g & 1)) * 2, 0, 0);
    PIN(sel); PIN(wb); PIN(hb); PIN(rho); PIN(xres);
    if (w == 4) q_published(l < 32 ? ((l >> 3) * 4 + grp) * 8 + b8 : (grp * 4 + 3) * 8 + (f & 3) * 2 + (l & 1));
    if (!hiw) hint_wait8t(ws + WS_G2 + f * 256 + u * 64, tagLo, RG_DELAY);
    RAWBAR();
    if (w == 7) st8(rsW, rho, tagLo, clk());
    STAMP(11);
    f32x4 acc = zero4;
    POLL_LOOP {
      RELOAD();
      v4u d[4];
#pragma unroll
      for (int i = 0; i < 4; ++i) d[i] = ld16(rsW, wb + i * 1024);
      FENCE();
      acc = zero4;
#pragma unroll
      for (int i = 0; i < 4; ++i) acc = mfma(sel, d[i], acc);
      if (none(acc[0] != acc[0])) break;
      if (none(max(max(max4(d[0]), max4(d[1])), max(max4(d[2]), max4(d[3]))) == SENT)) break;
    }
    u32 a01 = pk2(acc[0] + dpp<DPP_R8>(acc[0]), acc[1] + dpp<DPP_R8>(acc[1]));
    u32 a23 = pk2(acc[2] + dpp<DPP_R8>(acc[2]), acc[3] + dpp<DPP_R8>(acc[3]));
    PIN(a01); PIN(a23);
    const u32 h01 = pk2(bflo(xres.x) + bflo(a01), bfhi(xres.x) + bfhi(a01));
    const u32 h23 = pk2(bflo(xres.y) + bflo(a23), bfhi(xres.y) + bfhi(a23));
    auto s0 = __builtin_amdgcn_permlane16_swap(h01, h01, false, false);
    auto s1 = __builtin_amdgcn_permlane16_swap(h23, h23, false, false);
    st16(rsW, hb, v4u{s0[0], s1[0], s0[1], s1[1]});
    STAMP(13);
    STAMP_FLUSH();
    return;
  }
  s16x8 f2[8];
  LAS char* pr = L + L_P + b8 * 144 + g * 32 + d8 * 16;
  LAS char* pc = L + L_P + 8 * 144 + g * 32 + d8 * 16;
  LAS char* psr = L + L_ST + d8 * 128 + b8 * 16;
  LAS char* pf2 = L + L_R1 + w * 8192 + (8 * g + q4) * 256 + 8 * p4;
  u32 mo = WS_MP + (4 * (4 * w + d8) + g) * MPJ + m * 128 + b8 * 16;
  PIN(mo); PIN(psr); PIN(pr); PIN(pc); PIN(pf2);
  if (hiw) {
    LAS char* pa1 = L + L_R0 + (4 * g + q4) * 64 + ((g & 1) << 5) + 8 * p4;
    LAS char* pz = (l & 15) < 9 ? L + L_P + (u * 9 + (l & 15)) * 144 + 16 * g : L + L_JUNK + 256;
    LAS char* pst = (!d8 && (g == 2 || g == (b8 >> 2))) ? L + L_ST + (g < 2 ? 128 : 0) + b8 * 16 + u * 4 : L + L_JUNK + 512;
    u32 hb0 = WS_H + h0 * 16 + g * 128 + b8 * 16, hb1 = hb0 ^ 8192;
    u32 fh = tid == 448 ? WS_FH + (c - 128) * 128 : WS_END;
    PIN(pa1); PIN(pz); PIN(pst); PIN(hb0); PIN(hb1); PIN(fh);
    v4u hf[8];
#pragma unroll
    for (int i = 0; i < 8; ++i) { hf[i] = v4u{0x3F803F80u, 0x3F803F80u, 0x3F803F80u, 0x3F803F80u}; PIN(hf[i]); }
    VMWAIT(16 - 4 * W2_MID);
    s16x8 f1[2][8];
#pragma unroll
    for (int nt = 0; nt < 2; ++nt)
#pragma unroll
      for (int ks = 0; ks < 8; ++ks) f1[nt][ks] = trfrag((LAS char*)(size_t)((u32)(size_t)pa1 ^ (32 * nt)) + (ks < 4 ? h0 : h1) * 64 + (ks & 3) * 2048, 1024);
    LWAIT1(F_H);
    STAMP(11);
    if (!d8) {
#pragma unroll
      for (int i = 0; i < 8; ++i) hf[i] = ld16(rsW, (i < 4 ? hb0 : hb1) + (i & 3) * 512);
    }
    VMWAIT(8);
    LSET(F_W + u * 4);
    STAMP(8);
    LWAIT1(F_W + (w >> 1) * 4);
#pragma unroll
    for (int T = 0; T < 8; ++T) f2[T] = trfrag(pf2 + ((T ^ fsw) << 5), 1024);
    FENCE();
    f32x4 z0 = zero4, z1 = zero4, aQ = zero4;
#pragma nounroll
    for (int round = 0; round < 2; ++round) {
      z0 = zero4; z1 = zero4; aQ = zero4;
#pragma unroll
      for (int i = 0; i < 8; ++i) {
        aQ = mfma(hf[i], hf[i], aQ);
        z0 = mfma(f1[0][i], __builtin_bit_cast(s16x8, hf[i]), z0);
        z1 = mfma(f1[1][i], __builtin_bit_cast(s16x8, hf[i]), z1);
      }
      PIN(z0); PIN(z1);
      if (round || none(aQ[0] != aQ[0])) break;
      POLL_LOOP {
        RELOAD();
        if (!d8) {
#pragma unroll
          for (int i = 0; i < 8; ++i) hf[i] = ld16(rsW, (i < 4 ? hb0 : hb1) + (i & 3) * 512);
        }
        u32 mxv = 0;
#pragma unroll
        for (int i = 0; i < 8; ++i) mxv = max(mxv, max4(hf[i]));
        if (none(mxv == SENT)) break;
      }
    }
    STAMP(9);
    *(LAS float*)pst = (p4 & 2) ? ((p4 & 1) ? aQ[3] : aQ[2]) : ((p4 & 1) ? aQ[1] : aQ[0]);
    *(LAS f32x4*)pz = z0; *(LAS f32x4*)(pz + 64) = z1;
    if (w == 7) st8(rsW, fh, tagLo, clk());
  } else {
    if (w == 0) {
      q_published((((l >> 3) & 3) * 4 + 3) * 8 + b8);
      hint_wait4(ws + WS_GH + (xcd * 4 + (sl & 3)) * 128, tagLo, HR_DELAY);
      LSET(F_H);
    }
    LWAIT1(F_W + (w >> 1) * 4);
#pragma unroll
    for (int T = 0; T < 8; ++T) f2[T] = trfrag(pf2 + ((T ^ fsw) << 5), 1024);
  }
  BARRIER();
  {
    const f32x4 t0 = *(LAS f32x4*)psr;
    const f32x4 z0 = *(LAS f32x4*)pr, z1 = *(LAS f32x4*)(pr + 9 * 144), z2 = *(LAS f32x4*)(pr + 18 * 144), z3 = *(LAS f32x4*)(pr + 27 * 144);
    const f32x4 c0 = *(LAS f32x4*)pc, c1 = *(LAS f32x4*)(pc + 9 * 144), c2 = *(LAS f32x4*)(pc + 18 * 144), c3 = *(LAS f32x4*)(pc + 27 * 144);
    FENCE();
    const float mine = ((t0[0] + t0[1]) + (t0[2] + t0[3])) * (1.f / 1024.f);
    const float other = dpp<DPP_R8>(mine);
    const float mean = d8 ? other : mine, ex2 = d8 ? mine : other;
    u32 mv = pk2(mean, fmaxf(fmaf(-mean, mean, ex2), 0.f));
    asm("" : "+v"(mv));
    const float den = rbf(__builtin_amdgcn_sqrtf(rbf(bfhi(mv) + 1e-5f)));
    const float inv = __builtin_amdgcn_rcpf(den);
    const float mb = bflo(mv);
    float gv[4];
#pragma unroll
    for (int i = 0; i < 4; ++i) gv[i] = fmaf(-mb, (c0[i] + c1[i]) + (c2[i] + c3[i]), (z0[i] + z1[i]) + (z2[i] + z3[i])) * inv;
    const s16x2 zz = {0, 0};
    const int g0 = __builtin_bit_cast(int, __builtin_elementwise_max(__builtin_bit_cast(s16x2, pk2(gv[0], gv[1])), zz));
    const int g1 = __builtin_bit_cast(int, __builtin_elementwise_max(__builtin_bit_cast(s16x2, pk2(gv[2], gv[3])), zz));
    v4u B0, B1;
    B0[0] = __builtin_amdgcn_update_dpp(0, g0, DPP_ID, 0xf, 0x3, false);
    B0[1] = __builtin_amdgcn_update_dpp(0, g1, DPP_ID, 0xf, 0x3, false);
    B0[2] = __builtin_amdgcn_update_dpp(0, g0, DPP_R8, 0xf, 0x3, false);
    B0[3] = __builtin_amdgcn_update_dpp(0, g1, DPP_R8, 0xf, 0x3, false);
    B1[0] = __builtin_amdgcn_update_dpp(0, g0, DPP_R8, 0xf, 0xc, false);
    B1[1] = __builtin_amdgcn_update_dpp(0, g1, DPP_R8, 0xf, 0xc, false);
    B1[2] = __builtin_amdgcn_update_dpp(0, g0, DPP_ID, 0xf, 0xc, false);
    B1[3] = __builtin_amdgcn_update_dpp(0, g1, DPP_ID, 0xf, 0xc, false);
#pragma unroll
    for (int i = 0; i < 2; ++i) {
      f32x4 u0 = mfma(f2[4 * i], __builtin_bit_cast(s16x8, B0), zero4), u1 = mfma(f2[4 * i + 1], __builtin_bit_cast(s16x8, B0), zero4);
      u0 = mfma(f2[4 * i + 2], __builtin_bit_cast(s16x8, B1), u0);
      u1 = mfma(f2[4 * i + 3], __builtin_bit_cast(s16x8, B1), u1);
      __builtin_amdgcn_raw_buffer_store_b128(v4u{pk2(u0[0], u0[1]), pk2(u0[2], u0[3]), pk2(u1[0], u1[1]), pk2(u1[2], u1[3])}, rsW, mo, i * 8 * MPJ, AUX_SC1);
      FENCE();
    }
  }
  STAMP(10);
  STAMP_FLUSH();
}

extern "C" void kernel_launch(void* const* d_in, const int* in_sizes, int n_in, void* d_out, int out_size, void* d_ws, size_t ws_size,
                              hipStream_t stream) {
  (void)hipFuncSetAttribute((const void*)fused_decode_layer, hipFuncAttributeMaxDynamicSharedMemorySize, L_BYTES);
  fused_decode_layer<<<NWG, NTHR, L_BYTES, stream>>>((const char*)d_in[0], (const char*)d_in[1], (const char*)d_in[2], (const char*)d_in[3],
                                                     (const char*)d_in[4], (const char*)d_in[6], (const char*)d_in[7], (char*)d_out, (char*)d_ws);
}
```

```cpp
#include <hip/hip_runtime.h>
#include <hip/hip_bf16.h>
#include <stdint.h>

#define DIAG 0
#define TOUCH_DELAY 2
#define WO_DELAY 6
#define QH_DELAY 4
#define O_DELAY 62
#define OQ_LEAD 42
#define OV_LEAD 12
#define RG_DELAY 100
#define HR_DELAY 96
#define F_DELAY 96
#define OP_NAP 50
#define CODE_LINES 15
#define W2_MID 1
#define ZM 0
#define SZ_W1 ((ZM & 1) ? 0 : 1024 * 3584 * 2)
#define SZ_W2 ((ZM & 2) ? 0 : 3584 * 1024 * 2)
#define SZ_WO ((ZM & 4) ? 0 : 4096 * 1024 * 2)
#define SZ_V ((ZM & 8) ? 0 : 8 * 4 * 4096 * 128 * 2)
#define SZ_K ((ZM & 16) ? 0 : 8 * 4 * 4096 * 128 * 2)
#define SZ_WQ ((ZM & 32) ? 0 : 1024 * 4096 * 2)

#define LAS __attribute__((address_space(3)))
typedef unsigned u32;
typedef unsigned long long u64;
typedef unsigned v4u __attribute__((ext_vector_type(4)));
typedef unsigned v2u __attribute__((ext_vector_type(2)));
typedef float f32x4 __attribute__((ext_vector_type(4)));
typedef float f32x2 __attribute__((ext_vector_type(2)));
typedef short s16x2 __attribute__((ext_vector_type(2)));
typedef short s16x4 __attribute__((ext_vector_type(4)));
typedef short s16x8 __attribute__((ext_vector_type(8)));
typedef __bf16 bf16x2 __attribute__((ext_vector_type(2)));
typedef __bf16 bf16x8 __attribute__((ext_vector_type(8)));
typedef __amdgpu_buffer_rsrc_t rsrc_t;

typedef unsigned s8u __attribute__((ext_vector_type(8)));

namespace {

constexpr int NWG = 256, NTHR = 512;
constexpr u32 SENT = 0xFFFFFFFFu;
constexpr int AUX_SC1 = 16;
constexpr int SPIN_CAP = 1 << 18;
constexpr int LDS_CAP = 1 << 25;
constexpr float LOG2E = 1.4426950408889634f;

constexpr int WS_EPOCH = 0;
constexpr int WS_H = 32768;
constexpr int WS_QG = 65536;
constexpr int WS_QH = 0xA0000;
constexpr int WS_OH = 0xB0000;
constexpr int WS_GH = 0xC0000;
constexpr int WS_G2 = 0xE0000;
constexpr int WS_FH = 0xD0000;
constexpr int WS_OP = 1 << 20;
constexpr int WS_WP = WS_OP + 524288;
constexpr int WS_MP = WS_WP + 524288;
constexpr int MPJ = 112 * 128;
constexpr int WS_END = WS_MP + 128 * MPJ;
constexpr int WS_STAMP = 8 << 20;

constexpr int L_R0 = 0, L_R1 = 65536, L_MISC = 131072;
constexpr int L_ACT = L_MISC;
constexpr int L_P = L_ACT + 8 * 1056;
constexpr int L_ST = L_P + 5376;
constexpr int L_FL = L_ST + 512;
constexpr int F_KR = 0, F_KD = 16, F_V = 32, F_H = 48, F_W = 64;
constexpr int L_JUNK = L_ST + 1024;
constexpr int L_BYTES = L_ST + 2048;

__device__ __forceinline__ float bflo(u32 v) { return __uint_as_float(v << 16); }
__device__ __forceinline__ float bfhi(u32 v) { return __uint_as_float(v & 0xFFFF0000u); }
__device__ __forceinline__ u32 pk2(float lo, float hi) {
  bf16x2 r = {(__bf16)lo, (__bf16)hi};
  return __builtin_bit_cast(u32, r);
}
__device__ __forceinline__ float rbf(float x) { return (float)(__bf16)x; }

template <int CTRL> __device__ __forceinline__ float dpp(float x) {
  return __builtin_bit_cast(float, __builtin_amdgcn_mov_dpp(__builtin_bit_cast(int, x), CTRL, 0xf, 0xf, true));
}
constexpr int DPP_X1 = 0xB1, DPP_X2 = 0x4E, DPP_HM = 0x141, DPP_M = 0x140, DPP_R8 = 0x128, DPP_R4 = 0x124, DPP_ID = 0xE4;
__device__ __forceinline__ float sum16_32(float x) {
  auto s = __builtin_amdgcn_permlane16_swap(__float_as_uint(x), __float_as_uint(x), false, false);
  x = __uint_as_float(s[0]) + __uint_as_float(s[1]);
  auto t = __builtin_amdgcn_permlane32_swap(__float_as_uint(x), __float_as_uint(x), false, false);
  return __uint_as_float(t[0]) + __uint_as_float(t[1]);
}
__device__ __forceinline__ float wave_sum(float x) {
  x += dpp<DPP_X1>(x); x += dpp<DPP_X2>(x); x += dpp<DPP_HM>(x); x += dpp<DPP_M>(x);
  return sum16_32(x);
}
__device__ __forceinline__ f32x4 mfma(s16x8 a, s16x8 b, f32x4 c) {
  return __builtin_amdgcn_mfma_f32_16x16x32_bf16(__builtin_bit_cast(bf16x8, a), __builtin_bit_cast(bf16x8, b), c, 0, 0, 0);
}
__device__ __forceinline__ f32x4 mfma(v4u a, v4u b, f32x4 c) {
  return __builtin_amdgcn_mfma_f32_16x16x32_bf16(__builtin_bit_cast(bf16x8, a), __builtin_bit_cast(bf16x8, b), c, 0, 0, 0);
}
__device__ __forceinline__ s16x8 trfrag(LAS char* p, int half_off) {
  typedef short v4i16_t __attribute__((ext_vector_type(4)));
  s16x4 lo = __builtin_amdgcn_ds_read_tr16_b64_v4i16((LAS v4i16_t*)p);
  s16x4 hi = __builtin_amdgcn_ds_read_tr16_b64_v4i16((LAS v4i16_t*)(p + half_off));
  return __builtin_shufflevector(lo, hi, 0, 1, 2, 3, 4, 5, 6, 7);
}
#define DMA4_ASM(POL) \
  asm volatile("s_mov_b32 m0, %6\n\ts_nop 0\n\t" \
               "buffer_load_dwordx4 %1, %5, %0 offen " POL " lds\n\ts_add_u32 %0, %0, %7\n\t" \
               "buffer_load_dwordx4 %2, %5, %0 offen offset:1024 " POL " lds\n\ts_add_u32 %0, %0, %7\n\t" \
               "buffer_load_dwordx4 %3, %5, %0 offen offset:2048 " POL " lds\n\ts_add_u32 %0, %0, %7\n\t" \
               "buffer_load_dwordx4 %4, %5, %0 offen offset:3072 " POL " lds" \
               : "+s"(soff) : "v"(v0), "v"(v1), "v"(v2), "v"(v3), "s"(rs), "s"(lds), "s"(sdelta) : "memory", "scc")
template <int NT = 1>
__device__ __forceinline__ u32 dma4(rsrc_t rs, u32 v0, u32 v1, u32 v2, u32 v3, u32 soff, u32 sdelta, u32 lds) {
  if constexpr (NT) DMA4_ASM("nt"); else DMA4_ASM("");
  return soff;
}
__device__ __forceinline__ void dma4c(rsrc_t rs, u32 v0, u32 v1, u32 v2, u32 v3, u32 soff, u32 lds) {
  asm volatile("s_mov_b32 m0, %6\n\ts_nop 0\n\t"
               "buffer_load_dwordx4 %1, %5, %0 offen nt lds\n\t"
               "buffer_load_dwordx4 %2, %5, %0 offen offset:1024 nt lds\n\t"
               "buffer_load_dwordx4 %3, %5, %0 offen offset:2048 nt lds\n\t"
               "buffer_load_dwordx4 %4, %5, %0 offen offset:3072 nt lds"
               :: "s"(soff), "v"(v0), "v"(v1), "v"(v2), "v"(v3), "s"(rs), "s"(lds) : "memory");
}
__device__ __forceinline__ void touch(rsrc_t rs, u32 voff, u32 soff, u32 lds) {
  asm volatile("s_mov_b32 m0, %0\n\ts_nop 0\n\tbuffer_load_dword %1, %2, %3 offen lds" ::"s"(lds), "v"(voff), "s"(rs), "s"(soff) : "memory");
}
#define LOAD_TAG() do { u64 e_; asm volatile("s_load_dwordx2 %0, %1, 0x0\n\ts_waitcnt lgkmcnt(0)" : "=s"(e_) : "s"(ws) : "memory"); tagLo = (u32)e_ + 1u; tagHi = (u32)(e_ >> 32) + 1u; } while (0)
#define POLL_LOOP _Pragma("nounroll") for (int it = 0; it < SPIN_CAP; ++it)
#define RELOAD() asm volatile("" ::: "memory")
#define BARRIER() asm volatile("s_waitcnt lgkmcnt(0)\n\ts_barrier" ::: "memory")
#define RAWBAR() asm volatile("s_barrier" ::: "memory")
#define VMWAIT(n) asm volatile("s_waitcnt vmcnt(%0)" :: "i"(n) : "memory")
#define FENCE() __builtin_amdgcn_sched_barrier(0)
#define PIN(x) asm volatile("" : "+v"(x))
template <int N> __device__ __forceinline__ void nap() {
  if constexpr (N > 0) { __builtin_amdgcn_s_sleep(N > 127 ? 127 : N); nap<(N > 127 ? N - 127 : 0)>(); }
}
#define LSET(off) do { RELOAD(); *(volatile LAS u32*)(L + L_FL + (off)) = 1u; } while (0)
#define LWAIT1(off) do { _Pragma("nounroll") for (int it_ = 0; it_ < LDS_CAP; ++it_) { \
    if (__builtin_amdgcn_readfirstlane(*(volatile LAS u32*)(L + L_FL + (off))) != 0u) break; } RELOAD(); } while (0)
#define LWAIT4(off) do { _Pragma("nounroll") for (int it_ = 0; it_ < LDS_CAP; ++it_) { const v4u f_ = *(volatile LAS v4u*)(L + L_FL + (off)); \
    if (__builtin_amdgcn_readfirstlane(f_.x & f_.y & f_.z & f_.w) != 0u) break; } RELOAD(); } while (0)
__device__ __forceinline__ u32 clk() { return (u32)__builtin_amdgcn_s_memrealtime(); }
__device__ __forceinline__ void until(u32 t) {
#pragma nounroll
  for (int it = 0; it < 160; ++it) { if ((int)(clk() - t) >= 0) break; }
}
__device__ __forceinline__ u32 later(u32 a, u32 b) { return (int)(a - b) > 0 ? a : b; }
__device__ __forceinline__ void hint_wait(const char* p, u32 tag, u32 ticks) {
  u64 v;
#pragma nounroll
  for (int it = 0; it < 64; ++it) {
    asm volatile("s_load_dwordx2 %0, %1, 0x0 glc\n\ts_waitcnt lgkmcnt(0)" : "=s"(v) : "s"(p) : "memory");
    if ((u32)v == tag) break;
  }
  until((u32)(v >> 32) + ticks);
}
__device__ __forceinline__ void hint_wait4(const char* p, u32 tag, u32 ticks) {
  s8u v;
#pragma nounroll
  for (int it = 0; it < 64; ++it) {
    asm volatile("s_load_dwordx8 %0, %1, 0x0 glc\n\ts_waitcnt lgkmcnt(0)" : "=s"(v) : "s"(p) : "memory");
    if ((((v[0] ^ tag) | (v[2] ^ tag)) | ((v[4] ^ tag) | (v[6] ^ tag))) == 0u) break;
  }
  until(later(later(v[1], v[3]), later(v[5], v[7])) + ticks);
}
typedef unsigned s16u __attribute__((ext_vector_type(16)));
__device__ __forceinline__ void hint_wait8t(const char* p, u32 tag, u32 ticks) {
  s16u v;
#pragma nounroll
  for (int it = 0; it < 64; ++it) {
    asm volatile("s_load_dwordx16 %0, %1, 0x0 glc\n\ts_waitcnt lgkmcnt(0)" : "=s"(v) : "s"(p) : "memory");
    if (((((v[0] ^ tag) | (v[2] ^ tag)) | ((v[4] ^ tag) | (v[6] ^ tag))) | (((v[8] ^ tag) | (v[10] ^ tag)) | ((v[12] ^ tag) | (v[14] ^ tag)))) == 0u) break;
  }
  until(later(later(later(v[1], v[3]), later(v[5], v[7])), later(later(v[9], v[11]), later(v[13], v[15]))) + ticks);
}
__device__ __forceinline__ void hint_wait8(const char* p, u32 tag) {
#pragma nounroll
  for (int it = 0; it < 64; ++it) {
    s8u v;
    asm volatile("s_load_dwordx8 %0, %1, 0x0 glc\n\ts_waitcnt lgkmcnt(0)" : "=s"(v) : "s"(p) : "memory");
    if ((((v[0] ^ tag) | (v[1] ^ tag)) | ((v[2] ^ tag) | (v[3] ^ tag)) | ((v[4] ^ tag) | (v[5] ^ tag)) | ((v[6] ^ tag) | (v[7] ^ tag))) == 0u) break;
  }
}
__device__ __forceinline__ u32 max4(v4u v) { return max(max(v.x, v.y), max(v.z, v.w)); }
__device__ __forceinline__ float max2(float a, float b) { float r; asm("v_max_f32 %0, %1, %2" : "=v"(r) : "v"(a), "v"(b)); return r; }
__device__ __forceinline__ float max3(float a, float b, float c) { float r; asm("v_max3_f32 %0, %1, %2, %3" : "=v"(r) : "v"(a), "v"(b), "v"(c)); return r; }
__device__ __forceinline__ float max8_16_32(float x) {
  float y;
  asm("s_nop 1\n\tv_max_f32_dpp %0, %0, %0 row_ror:8 row_mask:0xf bank_mask:0xf\n\tv_mov_b32 %1, %0\n\ts_nop 1\n\t"
      "v_permlane16_swap_b32 %0, %1\n\tv_max_f32 %0, %0, %1\n\tv_mov_b32 %1, %0\n\ts_nop 1\n\t"
      "v_permlane32_swap_b32 %0, %1\n\tv_max_f32 %0, %0, %1" : "+v"(x), "=&v"(y));
  return x;
}
__device__ __forceinline__ v4u ld16(rsrc_t rs, u32 off) { return __builtin_amdgcn_raw_buffer_load_b128(rs, off, 0, AUX_SC1); }
__device__ __forceinline__ void st16(rsrc_t rs, u32 off, v4u v) { __builtin_amdgcn_raw_buffer_store_b128(v, rs, off, 0, AUX_SC1); }
__device__ __forceinline__ void st8(rsrc_t rs, u32 off, u32 a, u32 b) { __builtin_amdgcn_raw_buffer_store_b64(v2u{a, b}, rs, off, 0, AUX_SC1); }
__device__ __forceinline__ bool none(bool p) { return __builtin_amdgcn_ballot_w64(p) == 0ull; }
__device__ __forceinline__ rsrc_t mkrs(const void* p, int bytes) { return __builtin_amdgcn_make_buffer_rsrc((void*)p, 0, bytes, 0x00020000); }

__device__ __forceinline__ float dot2(u32 a, u32 b, float c) {
  return __builtin_amdgcn_fdot2_f32_bf16(__builtin_bit_cast(bf16x2, a), __builtin_bit_cast(bf16x2, b), c, false);
}
__device__ __forceinline__ v4u ln_half(const v4u c, const v4u o) {
  float s = 0.f, q = 0.f;
#pragma unroll
  for (int i = 0; i < 4; ++i) { s = dot2(c[i], 0x3F803F80u, s); q = dot2(c[i], c[i], q); s = dot2(o[i], 0x3F803F80u, s); q = dot2(o[i], o[i], q); }
  const float mean = wave_sum(s) * (1.f / 1024.f);
  const float var = fmaxf(fmaf(-mean, mean, wave_sum(q) * (1.f / 1024.f)), 0.f);
  const u32 mv = pk2(mean, var);
  const float mb = bflo(mv);
  const float den = rbf(__builtin_amdgcn_sqrtf(rbf(bfhi(mv) + 1e-5f)));
  const float inv = __builtin_amdgcn_rcpf(den);
  v4u r;
#pragma unroll
  for (int i = 0; i < 4; ++i) {
    const u32 d = pk2(bflo(c[i]) - mb, bfhi(c[i]) - mb);
    const float d0 = bflo(d), d1 = bfhi(d);
    float q0 = d0 * inv, q1 = d1 * inv;
    q0 = fmaf(fmaf(-q0, den, d0), inv, q0);
    q1 = fmaf(fmaf(-q1, den, d1), inv, q1);
    r[i] = pk2(q0, q1);
  }
  return r;
}

#define STAMP(i) do {} while (0)
#define STAMP_DECL() do {} while (0)
#define STAMP_FLUSH() do {} while (0)

}

__global__ void __launch_bounds__(NTHR, 2)
fused_decode_layer(const char* x, const char* kc, const char* vc, const char* wq, const char* wo, const char* w1, const char* w2, char* out, char* ws) {
  extern __shared__ __attribute__((aligned(16))) char smem[];
  STAMP_DECL();
  STAMP(0);
  {
    u32 t0, t1, t2, t3, t4;
    asm volatile(
        "s_load_dwordx8 s[64:71], %[ka], 0x0\n\t"
        "s_getpc_b64 s[82:83]\n\t"
        "v_readfirstlane_b32 s72, %[tid]\n\t"
        "v_and_b32 %[t4], 63, %[tid]\n\t"
        "s_mov_b32 s79, 0x20000\n\t"
        "s_mov_b32 s78, %[szq]\n\t"
        "s_lshr_b32 s72, s72, 6\n\t"
        "s_and_b32 s73, s72, 3\n\t"
        "v_lshrrev_b32 %[t1], 4, %[t4]\n\t"
        "s_bfe_u32 s84, %[c], 0x20003\n\t"
        "s_bfe_u32 s74, %[c], 0x10006\n\t"
        "s_lshl1_add_u32 s84, s84, s74\n\t"
        "s_bfe_u32 s85, %[c], 0x10005\n\t"
        "s_lshl2_add_u32 s85, s85, s73\n\t"
        "s_and_b32 s74, %[c], 0x80\n\t"
        "s_cmp_gt_u32 s72, 3\n\t"
        "s_cbranch_scc1 Lpro_hi\n\t"
        "s_cmp_eq_u32 s74, 0\n\t"
        "s_cbranch_scc1 Lpro_bl\n\t"
        "s_cmp_lg_u32 s72, 3\n\t"
        "s_cbranch_scc1 Lpro_al\n\t"
        "s_bfe_u32 s74, %[c], 0x40003\n\t"
        "s_mul_i32 s74, s74, %[cl]*64\n\t"
        "s_and_b32 s82, s82, 0xffffff00\n\t"
        "s_add_u32 s82, s82, s74\n\t"
        "s_addc_u32 s83, s83, 0\n\t"
        ".irp o,0,1,2,3,4,5,6,7,8,9,10,11,12,13,14,15,16,17\n\t"
        ".if \\o < %[cl]\n\t"
        "s_load_dword s75, s[82:83], \\o*64+1024\n\t"
        ".endif\n\t"
        ".endr\n\t"
        "s_barrier\n\t"
        "s_branch Lpro_end\n\t"
        "Lpro_al:\n\t"
        "v_lshlrev_b32 %[t2], 2, %[t4]\n\t"
        "v_add_u32 %[t2], %[lfl], %[t2]\n\t"
        "v_mov_b32 %[t1], 0\n\t"
        "ds_write_b32 %[t2], %[t1]\n\t"
        "s_lshl1_add_u32 s74, s73, s84\n\t"
        "s_and_b32 s74, s74, 7\n\t"
        "s_add_u32 s75, s74, %[c]\n\t"
        "s_and_b32 s75, s75, 7\n\t"
        "s_lshl3_add_u32 s75, s74, s75\n\t"
        "s_lshl_b32 s75, s75, 7\n\t"
        "s_lshl1_add_u32 s74, s85, 1\n\t"
        "s_lshl_b32 s74, s74, 19\n\t"
        "s_or_b32 s74, s74, s75\n\t"
        "v_lshlrev_b32 %[t0], 13, %[t4]\n\t"
        "s_mov_b32 m0, %[junk]\n\t"
        "s_waitcnt lgkmcnt(0)\n\t"
        "s_mov_b32 s76, s70\n\t"
        "s_and_b32 s77, s71, 0xffff\n\t"
        "buffer_load_dword %[t0], s[76:79], s74 offen  lds\n\t"
        "s_add_u32 s85, s85, 1\n\t"
        "s_add_u32 s73, s73, 1\n\t"
        "s_cmp_eq_u32 s73, 3\n\t"
        "s_cbranch_scc1 Lpro_al\n\t"
        "s_barrier\n\t"
        "s_branch Lpro_end\n\t"
        "Lpro_hi:\n\t"
        "s_lshl_b32 m0, s73, 14\n\t"
        "s_cmp_lg_u32 s74, 0\n\t"
        "s_cbranch_scc1 Lpro_ah\n\t"
        "v_and_b32 %[t1], 1, %[t1]\n\t"
        "v_lshlrev_b32 %[t1], 1, %[t1]\n\t"
        "v_and_b32 %[t0], 7, %[t4]\n\t"
        "v_xor_b32 %[t0], %[t0], %[t1]\n\t"
        "v_lshrrev_b32 %[t2], 3, %[t4]\n\t"
        "v_lshlrev_b32 %[t2], 13, %[t2]\n\t"
        "v_lshl_or_b32 %[t0], %[t0], 4, %[t2]\n\t"
        "v_xor_b32 %[t1], 64, %[t0]\n\t"
        "s_add_u32 s75, s84, %[c]\n\t"
        "s_and_b32 s75, s75, 7\n\t"
        "s_lshl3_add_u32 s75, s84, s75\n\t"
        "s_lshl_b32 s75, s75, 7\n\t"
        "s_lshl_b32 s74, s85, 20\n\t"
        "s_or_b32 s74, s74, s75\n\t"
        "s_mov_b32 s75, 0xfc00\n\t"
        "s_mov_b32 s73, 4\n\t"
        "s_waitcnt lgkmcnt(0)\n\t"
        "s_mov_b32 s76, s70\n\t"
        "s_and_b32 s77, s71, 0xffff\n\t"
        "Lpro_bh_loop:\n\t"
        "buffer_load_dwordx4 %[t0], s[76:79], s74 offen nt lds\n\t"
        "s_add_u32 s74, s74, s75\n\t"
        "buffer_load_dwordx4 %[t1], s[76:79], s74 offen offset:1024 nt lds\n\t"
        "s_add_u32 s74, s74, s75\n\t"
        "buffer_load_dwordx4 %[t0], s[76:79], s74 offen offset:2048 nt lds\n\t"
        "s_add_u32 s74, s74, s75\n\t"
        "buffer_load_dwordx4 %[t1], s[76:79], s74 offen offset:3072 nt lds\n\t"
        "s_add_u32 m0, m0, 0x1000\n\t"
        "s_add_u32 s74, s74, 0x10c00\n\t"
        "s_sub_u32 s73, s73, 1\n\t"
        "s_cmp_lg_u32 s73, 0\n\t"
        "s_cbranch_scc1 Lpro_bh_loop\n\t"
        "s_branch Lpro_end\n\t"
        "Lpro_bl:\n\t"
        "v_lshlrev_b32 %[t0], 4, %[t4]\n\t"
        "v_and_b32 %[t1], 31, %[t4]\n\t"
        "v_lshlrev_b32 %[t1], 13, %[t1]\n\t"
        "s_lshl_b32 s74, s73, 12\n\t"
        "s_add_u32 m0, s74, %[r1]\n\t"
        "s_waitcnt lgkmcnt(0)\n\t"
        "s_and_b32 s65, s65, 0xffff\n\t"
        "s_movk_i32 s66, 0x4000\n\t"
        "s_mov_b32 s67, s79\n\t"
        "buffer_load_dwordx4 %[t0], s[64:67], s74 offen lds\n\t"
        "buffer_load_dwordx4 %[t0], s[64:67], s74 offen offset:1024 lds\n\t"
        "buffer_load_dwordx4 %[t0], s[64:67], s74 offen offset:2048 lds\n\t"
        "buffer_load_dwordx4 %[t0], s[64:67], s74 offen offset:3072 lds\n\t"
        "s_cmp_lg_u32 s72, 0\n\t"
        "s_cbranch_scc1 Lpro_end\n\t"
        "s_lshr_b32 s74, s70, 7\n\t"
        "s_sub_u32 s74, 3, s74\n\t"
        "s_and_b32 s74, s74, 7\n\t"
        "s_sub_u32 s75, s74, %[c]\n\t"
        "s_and_b32 s75, s75, 7\n\t"
        "s_lshl3_add_u32 s75, s75, s74\n\t"
        "s_lshl_b32 s75, s75, 7\n\t"
        "s_bfe_u32 s82, %[c], 0x30003\n\t"
        "s_bfe_u32 s83, %[c], 0x10006\n\t"
        "s_lshl2_add_u32 s82, s82, s83\n\t"
        "s_lshl_b32 s82, s82, 18\n\t"
        "s_or_b32 s74, s82, s75\n\t"
        "s_mov_b32 m0, %[junk]\n\t"
        "s_mov_b32 s76, s70\n\t"
        "s_and_b32 s77, s71, 0xffff\n\t"
        "buffer_load_dword %[t1], s[76:79], s74 offen lds\n\t"
        "s_branch Lpro_end\n\t"
        "Lpro_ah:\n\t"
        "v_and_b32 %[t4], 15, %[t4]\n\t"
        "v_xor_b32 %[t0], %[t4], %[t1]\n\t"
        "v_lshlrev_b32 %[t1], 8, %[t1]\n\t"
        "v_lshl_or_b32 %[t0], %[t0], 4, %[t1]\n\t"
        "v_xor_b32 %[t1], 64, %[t0]\n\t"
        "v_xor_b32 %[t2], 0x80, %[t0]\n\t"
        "v_xor_b32 %[t3], 0x80, %[t1]\n\t"
        "s_bfe_u32 s74, %[c], 0x20005\n\t"
        "s_and_b32 s75, %[c], 7\n\t"
        "s_lshl3_add_u32 s74, s74, s75\n\t"
        "s_bfe_u32 s75, %[c], 0x20003\n\t"
        "s_lshl4_add_u32 s74, s74, s75\n\t"
        "s_or_b32 s74, s74, 12\n\t"
        "s_lshl2_add_u32 s74, s74, s73\n\t"
        "s_lshl_b32 s74, s74, 14\n\t"
        "s_mov_b32 s78, %[szk]\n\t"
        "s_mov_b32 s73, 4\n\t"
        "s_waitcnt lgkmcnt(0)\n\t"
        "s_barrier\n\t"
        "s_mov_b32 s76, s66\n\t"
        "s_and_b32 s77, s67, 0xffff\n\t"
        "Lpro_k_loop:\n\t"
        "buffer_load_dwordx4 %[t0], s[76:79], s74 offen nt lds\n\t"
        "buffer_load_dwordx4 %[t1], s[76:79], s74 offen offset:1024 nt lds\n\t"
        "buffer_load_dwordx4 %[t2], s[76:79], s74 offen offset:2048 nt lds\n\t"
        "buffer_load_dwordx4 %[t3], s[76:79], s74 offen offset:3072 nt lds\n\t"
        "s_add_u32 m0, m0, 0x1000\n\t"
        "s_addk_i32 s74, 0x1000\n\t"
        "s_sub_u32 s73, s73, 1\n\t"
        "s_cmp_lg_u32 s73, 0\n\t"
        "s_cbranch_scc1 Lpro_k_loop\n\t"
        "Lpro_end:\n\t"
        ".pushsection .text,1\n\t"
        ".fill 512, 4, 0xBF800000\n\t"
        ".popsection\n\t"
        "s_waitcnt lgkmcnt(0)"
        : [t0] "=&v"(t0), [t1] "=&v"(t1), [t2] "=&v"(t2), [t3] "=&v"(t3), [t4] "=&v"(t4)
        : [ka] "s"(__builtin_amdgcn_kernarg_segment_ptr()), [tid] "v"(threadIdx.x), [c] "s"(blockIdx.x), [cl] "i"(CODE_LINES),
          [junk] "i"(L_JUNK), [r1] "i"(L_R1),
          [szk] "i"(SZ_K), [szq] "i"(SZ_WQ), [lfl] "i"(L_FL)
        : "s64", "s65", "s66", "s67", "s68", "s69", "s70", "s71", "s72", "s73", "s74", "s75", "s76", "s77", "s78", "s79",
          "s82", "s83", "s84", "s85", "scc", "memory");
  }
  STAMP(1);
  LAS char* L = (LAS char*)smem;
  const int tid = threadIdx.x, l = tid & 63, w = __builtin_amdgcn_readfirstlane(tid >> 6), c = blockIdx.x;
  const int g = l >> 4, xcd = c & 7, sl = (c >> 3) & 15, u = w & 3;
  const bool hiw = w >= 4;
  const int bk = (sl >> 2) * 8 + xcd;
  const u32 kvb = (bk * 4096 + 3072 + 256 * (sl & 3)) * 256;
  const int q4 = (l >> 2) & 3, p4 = l & 3, b8 = l & 7, d8 = (l >> 3) & 1;
  const int fsw = q4 | ((g & 1) << 2);
  const rsrc_t rsW = mkrs(ws, WS_END);
  const f32x4 zero4 = {0.f, 0.f, 0.f, 0.f};
  u32 tagLo = 0, tagHi = 0;
  auto q_published = [&](int cb) {
    const int s_ = cb >> 3, bd = 2 * (s_ & 3) + (s_ >> 3);
    const u32 off = WS_QG + xcd * 65536 + (((s_ & 3) * 2 + ((s_ >> 2) & 1)) * 16 + (bd & 1) * 8 + ((cb + bd) & 7)) * 512;
    POLL_LOOP {
      RELOAD();
      const v4u f0 = ld16(rsW, off);
      if (none(((f0.y ^ tagLo) | (f0.w ^ tagHi)) != 0u)) break;
      nap<4>();
    }
  };

  auto selector = [&]() {
    const u32 one = (l & 8) ? 0u : ((l & 1) ? 0x3F800000u : 0x00003F80u);
    const int si = b8 >> 1;
    return v4u{si == 0 ? one : 0u, si == 1 ? one : 0u, si == 2 ? one : 0u, si == 3 ? one : 0u};
  };

  if (c < 128) {
    const int band = 2 * (sl & 3) + (sl >> 3), cg = 8 * band + ((xcd + band) & 7), kh = (sl >> 2) & 1;
    if (!hiw) {
      LAS char* px = L + L_R1 + w * 4096 + l * 16;
      if (w == 0) VMWAIT(1); else VMWAIT(0);
      STAMP(13);
      const v4u xc0 = *(LAS v4u*)(px + kh * 1024), xo0 = *(LAS v4u*)(px + (kh ^ 1) * 1024);
      const v4u xc1 = *(LAS v4u*)(px + 2048 + kh * 1024), xo1 = *(LAS v4u*)(px + 2048 + (kh ^ 1) * 1024);
      *(LAS v4u*)(L + L_ACT + (2 * w) * 1056 + l * 16) = ln_half(xc0, xo0);
      *(LAS v4u*)(L + L_ACT + (2 * w + 1) * 1056 + l * 16) = ln_half(xc1, xo1);
      STAMP(12);
    }
    const v4u sent4 = {SENT, SENT, SENT, SENT};
    const int j = c, kvo = sl >> 2, nq = sl & 3, ksl = kvo * 8 + xcd;
    BARRIER();
    if (w == 7) {
#pragma unroll
      for (int i = 0; i < 4; ++i) __builtin_amdgcn_raw_buffer_store_b128(sent4, rsW, WS_WP + (32 * nq + (l >> 3)) * 4096 + ksl * 128 + b8 * 16, i * 8 * 4096, AUX_SC1);
      st16(rsW, (nq == 3 && l < 32) ? WS_H + ksl * 512 + l * 16 : WS_END, sent4);
    }
    const int bq = 2 * u + (l >> 5), cp = l & 31;
    LAS char* pq = L + L_P + bq * 272 + cp * 8;
    u32 qo = WS_QG + (((bq * 4 + (cg >> 4)) * 2 + kh) * 1024 + 64 * (cg & 15) + 2 * cp) * 8;
    {
      const int kh2 = w >> 2;
      s16x8 bf[8];
      LAS char* pb = L + L_ACT + b8 * 1056 + kh2 * 512 + 16 * g;
#pragma unroll
      for (int ks = 0; ks < 8; ++ks) bf[ks] = *(LAS s16x8*)(pb + ks * 64);
      LAS char* pa = L + L_R0 + kh2 * 32768 + (8 * g + q4) * 128 + ((u ^ (((q4 >> 1) & 1) | ((g & 1) << 1))) << 5) + 8 * p4;
      LAS char* pw = L + L_P + (kh2 * 8 + b8) * 272 + (16 * u + 4 * g) * 4;
      u32 ho = (w == 6 && l < 8) ? WS_QH + (l * 16 + (cg >> 2)) * 128 + ((cg & 3) * 2 + kh) * 4 : WS_END;
      PIN(pa); PIN(pw); PIN(pq); PIN(qo); PIN(ho);
      LOAD_TAG();
      if (w == 7) VMWAIT(5); else if (hiw) VMWAIT(0);
      STAMP(2);
      RAWBAR();
      if (w == 6) __builtin_amdgcn_raw_buffer_store_b32(tagLo, rsW, ho, 0, AUX_SC1);
      s16x8 af[8];
#pragma unroll
      for (int ks = 0; ks < 8; ++ks) af[ks] = trfrag(pa + ks * 4096, 512);
      FENCE();
      f32x4 acc = zero4;
#pragma unroll
      for (int ks = 0; ks < 8; ++ks) acc = mfma(af[ks], bf[ks], acc);
      if (!d8) *(LAS f32x4*)pw = acc;
    }
    if (w == 7) VMWAIT(0);
    BARRIER();
    if (!hiw) {
      const f32x2 s = *(LAS f32x2*)pq + *(LAS f32x2*)(pq + 8 * 272);
      st16(rsW, qo, v4u{__float_as_uint(s[0]), tagLo, __float_as_uint(s[1]), tagHi});
      STAMP(3);
      {
        nap<1>();
#pragma unroll
        for (int i = 0; i < 4; ++i) st16(rsW, u * 4 + i < 14 ? WS_MP + j * MPJ + (u * 4 + i) * 1024 + l * 16 : WS_END, sent4);
      }
      nap<TOUCH_DELAY>();
      const rsrc_t rv = mkrs(vc, SZ_V), ro_ = mkrs(wo, SZ_WO);
      touch(rv, g * 16384 + (l & 15) * 128, kvb + (4 + u) * 2048, L_JUNK);
      nap<WO_DELAY>();
      const u32 vo = (l >> 5) * 2048 + (((((l & 31) >> 1) ^ (l >> 5)) << 1) | (l & 1)) * 16;
#pragma unroll
      for (int i = 0; i < 4; ++i) {
        const int h = 2 * u + (i >> 1), half = i & 1;
        const u32 vx = vo ^ (128 * half);
        dma4(ro_, vx, vx ^ 64, vx, vx ^ 64, ((kvo * 8 + h) * 128 + 16 * xcd + 8 * half) * 2048 + 512 * nq, 4096 - 1024, L_R0 + (8 * h + 4 * half) * 1024);
      }
      STAMP(4);
    } else {
      nap<OP_NAP>();
    }
    {
      u32 ob = WS_OP + (ksl * 8 + w) * 2048 + l * 16;
      LAS char* po = L + L_ACT + w * 288 + b8 * 32 + ((l >> 3) & 3) * 8 + (l >> 5) * 4;
      u32 wpo = d8 ? WS_END : WS_WP + (4 * (8 * nq + w) + g) * 4096 + ksl * 128 + b8 * 16;
      LAS char* pb = L + L_ACT + b8 * 288 + 16 * g;
      LAS char* pa0 = L + L_R0 + (8 * g + q4) * 512 + (((2 * w) ^ fsw) << 5) + 8 * p4;
      u32 gho = l < 4 ? WS_G2 + (nq * 4 + l) * 256 + ksl * 8 : WS_END;
      PIN(gho); PIN(ob); PIN(po); PIN(wpo); PIN(pb); PIN(pa0);
      hint_wait4(ws + WS_OH + ((w * 4 + kvo) * 8 + xcd) * 128, tagLo, O_DELAY);
      STAMP(6);
      v4u d0, d1;
      asm volatile("buffer_load_dwordx4 %0, %2, %3, 0 offen sc1\n\tbuffer_load_dwordx4 %1, %2, %3, 0 offen offset:1024 sc1" : "=&v"(d0), "=&v"(d1) : "v"(ob), "s"(rsW) : "memory");
      if (!hiw) VMWAIT(2);
      STAMP(5);
      RAWBAR();
      if (w == 7) {
        const v2u hv = {tagLo, clk()};
        asm volatile("buffer_store_dwordx2 %0, %1, %2, 0 offen sc1" :: "v"(hv), "v"(gho), "s"(rsW) : "memory");
      }
      s16x8 wf[2][4];
#pragma unroll
      for (int T = 0; T < 2; ++T)
#pragma unroll
        for (int ks = 0; ks < 4; ++ks) wf[T][ks] = trfrag((LAS char*)(size_t)((u32)(size_t)pa0 ^ (32 * T)) + ks * 16384, 2048);
      asm volatile("s_cmp_eq_u32 %2, 7\n\ts_cbranch_scc1 Lo_done\n\ts_waitcnt vmcnt(0)\nLo_done:\n\ts_waitcnt vmcnt(1)" : "+v"(d0), "+v"(d1) : "s"(w) : "memory", "scc");
      FENCE();
      const u32 okey = 2u * tagLo + 1u;
      if (!none(((d0.y ^ d0.w ^ okey) | (d1.y ^ d1.w ^ okey)) != 0u)) {
        POLL_LOOP {
          RELOAD();
          d0 = ld16(rsW, ob); d1 = ld16(rsW, ob + 1024);
          if (none(((d0.y ^ d0.w ^ okey) | (d1.y ^ d1.w ^ okey)) != 0u)) break;
        }
      }
      const float l0 = __uint_as_float(d0.w ^ tagHi), l1 = __uint_as_float(d1.w ^ tagHi);
      float mx = max2(l0, l1);
      { auto t = __builtin_amdgcn_permlane32_swap(__float_as_uint(mx), __float_as_uint(mx), false, false); mx = max2(__uint_as_float(t[0]), __uint_as_float(t[1])); }
      const float w0 = __builtin_amdgcn_exp2f(l0 - mx), w1_ = __builtin_amdgcn_exp2f(l1 - mx);
      float tot = w0 + w1_;
      { auto t = __builtin_amdgcn_permlane32_swap(__float_as_uint(tot), __float_as_uint(tot), false, false); tot = __uint_as_float(t[0]) + __uint_as_float(t[1]); }
      const float it_ = __builtin_amdgcn_rcpf(tot);
      const float v0 = fmaf(w0, bflo(d0.x), w1_ * bflo(d1.x)), v1 = fmaf(w0, bfhi(d0.x), w1_ * bfhi(d1.x));
      const float v2 = fmaf(w0, bflo(d0.z), w1_ * bflo(d1.z)), v3 = fmaf(w0, bfhi(d0.z), w1_ * bfhi(d1.z));
      auto t0 = __builtin_amdgcn_permlane32_swap(__float_as_uint(v0), __float_as_uint(v2), false, false);
      auto t1 = __builtin_amdgcn_permlane32_swap(__float_as_uint(v1), __float_as_uint(v3), false, false);
      *(LAS u32*)po = pk2((__uint_as_float(t0[0]) + __uint_as_float(t0[1])) * it_, (__uint_as_float(t1[0]) + __uint_as_float(t1[1])) * it_);
      STAMP(8);
      BARRIER();
      s16x8 bf[4];
#pragma unroll
      for (int ks = 0; ks < 4; ++ks) bf[ks] = *(LAS s16x8*)(pb + ks * 64);
      FENCE();
      f32x4 a0 = zero4, a1 = zero4;
#pragma unroll
      for (int ks = 0; ks < 4; ++ks) { a0 = mfma(wf[0][ks], bf[ks], a0); a1 = mfma(wf[1][ks], bf[ks], a1); }
      st16(rsW, wpo, v4u{pk2(a0[0], a0[1]), pk2(a0[2], a0[3]), pk2(a1[0], a1[1]), pk2(a1[2], a1[3])});
      STAMP(9);
    }
    {
      RAWBAR();
      const int id = 28 * W2_MID + w * 16 + sl;
      if (id < 112) touch(mkrs(w2, SZ_W2), l * 128, (xcd * 14 + id % 14) * 65536 + (id / 14) * 8192, L_JUNK);
    }
    if (w >= 6) {
      const int fb = 4 * (w & 1) + p4;
      u32 mb = WS_MP + j * MPJ + (l >> 2) * 128 + fb * 16;
      u32 oo = (l & 44) == 0 ? fb * 2048 + (32 * (j >> 2) + 4 * (j & 3) + 16 * (g & 1)) * 2 : 16384;
      u32 hro = WS_H + j * 128 + fb * 16 + (g & 1) * 8;
      v4u sel = selector();
      PIN(mb); PIN(oo); PIN(hro); PIN(sel);
      nap<24>();
      q_published(((j >> 5) * 4 + 3) * 8 + ((j >> 2) & 7));
      hint_wait(ws + WS_FH + (sl < 14 ? j : j - 16) * 128, tagLo, F_DELAY);
      STAMP(7);
      v2u hres;
      f32x4 acc;
      POLL_LOOP {
        RELOAD();
        hres = __builtin_amdgcn_raw_buffer_load_b64(rsW, hro, 0, AUX_SC1);
        v4u d[7];
#pragma unroll
        for (int i = 0; i < 7; ++i) d[i] = __builtin_amdgcn_raw_buffer_load_b128(rsW, mb + (i & 1) * 2048, (i >> 1) * 4096, AUX_SC1);
        FENCE();
        acc = zero4;
#pragma unroll
        for (int i = 0; i < 7; ++i) acc = mfma(sel, d[i], acc);
        const bool hbad = max(hres.x, hres.y) == SENT;
        if (none(hbad || acc[0] != acc[0])) break;
        u32 mxv = 0;
#pragma unroll
        for (int i = 0; i < 7; ++i) mxv = max(mxv, max4(d[i]));
        if (none(hbad || mxv == SENT)) break;
      }
      STAMP(13);
#pragma unroll
      for (int i = 0; i < 4; ++i) { acc[i] += dpp<DPP_R8>(acc[i]); acc[i] += dpp<DPP_R4>(acc[i]); }
      u32 y01 = pk2(acc[0], acc[1]), y23 = pk2(acc[2], acc[3]);
      PIN(y01); PIN(y23);
      const v2u r = {pk2(bflo(hres.x) + bflo(y01), bfhi(hres.x) + bfhi(y01)), pk2(bflo(hres.y) + bflo(y23), bfhi(hres.y) + bfhi(y23))};
      __builtin_amdgcn_raw_buffer_store_b64(r, mkrs(out, 8 * 1024 * 2), oo, 0, 0);
      if (j == 0 && tid == 448) st8(rsW, WS_EPOCH, tagLo, tagHi);
      STAMP(11);
    }
    STAMP_FLUSH();
    return;
  }

  const int s = sl & 3, b = bk >> 2, kvh = bk & 3;
  const int m = xcd * 14 + sl;
  const int h0 = (m & 1) * 512 + 128 * u, h1 = h0 ^ 512;
  if (hiw) {
    const rsrc_t rv = mkrs(vc, SZ_V), r1 = mkrs(w1, SZ_W1), r2 = mkrs(w2, SZ_W2);
    const u32 vv = g * 256 + (((((l & 15) >> 1) ^ g) << 1) | (l & 1)) * 16;
#pragma unroll
    for (int i = 0; i < 2; ++i) dma4c(rv, vv, vv, vv ^ 128, vv ^ 128, kvb + u * 16384 + i * 4096, L_R1 + u * 16384 + i * 4096);
    LOAD_TAG();
    LAS char* pa = L + L_R1 + (8 * g + q4) * 256 + (((2 * u) ^ fsw) << 5) + 8 * p4;
    LAS char* pf_ = L + L_P + b8 * 544 + 16 * g;
    LAS char* ps = L + L_ST + b8 * 16;
    u32 oo = WS_OP + (((kvh * 8 + 2 * u + d8) * 8 + b) * 4 + s) * 512 + g * 128 + b8 * 16;
    u32 v1 = (l >> 2) * 7168 + ((((l >> 1) & 1) ^ ((l >> 4) & 1)) << 5) + (l & 1) * 16;
    PIN(pa); PIN(pf_); PIN(ps); PIN(oo); PIN(v1);
    VMWAIT(8);
    LSET(F_KR + u * 4);
    STAMP(2);
    LWAIT4(F_KD);
#pragma unroll
    for (int i = 2; i < 4; ++i) dma4c(rv, vv, vv, vv ^ 128, vv ^ 128, kvb + u * 16384 + i * 4096, L_R1 + u * 16384 + i * 4096);
    VMWAIT(8);
    RAWBAR();
    const u32 tq = clk();
    s16x8 pf[8], vf[2][8];
    auto read_v = [&](int first) {
#pragma unroll
      for (int ks = first; ks < 8; ks += 2) {
        vf[0][ks] = trfrag(pa + ks * 8192, 1024);
        vf[1][ks] = trfrag((LAS char*)(size_t)((u32)(size_t)pa ^ 32) + ks * 8192, 1024);
      }
    };
    read_v(0);
    {
      const int lc = (3 - (int)(((u32)(size_t)w1) >> 7) - 7 * xcd) & 7;
      touch(r1, (lc < 7 && l < 16) ? l * 7168 : 0x7fffffff, (sl * 64 + u * 16) * 7168 + (xcd * 7 + lc) * 128, L_JUNK);
    }
    VMWAIT(1);
    LSET(F_V + u * 4);
    LWAIT4(F_V);
    STAMP(3);
    if (w == 7) st8(rsW, l < 8 ? WS_OH + (bk * 8 + l) * 128 + s * 8 : WS_END, tagLo, later(tq + OQ_LEAD, clk() + OV_LEAD));
    read_v(1);
    BARRIER();
    STAMP(5);
    const f32x4 m4 = *(LAS f32x4*)ps, s4 = *(LAS f32x4*)(ps + 128);
#pragma unroll
    for (int ks = 0; ks < 8; ++ks) pf[ks] = *(LAS s16x8*)(pf_ + ks * 64);
    FENCE();
    f32x4 ac[2][4];
#pragma unroll
    for (int i = 0; i < 4; ++i)
#pragma unroll
      for (int T = 0; T < 2; ++T) ac[T][i] = mfma(vf[T][2 * i + 1], pf[2 * i + 1], mfma(vf[T][2 * i], pf[2 * i], zero4));
    const float mm = max2(max3(m4[0], m4[1], m4[2]), m4[3]);
    f32x4 f;
#pragma unroll
    for (int i = 0; i < 4; ++i) f[i] = __builtin_amdgcn_exp2f(m4[i] - mm);
    const float tot = fmaf(f[0], s4[0], f[1] * s4[1]) + fmaf(f[2], s4[2], f[3] * s4[3]);
    const u32 lk = __float_as_uint(mm + __builtin_amdgcn_logf(tot)) ^ tagHi;
    f *= __builtin_amdgcn_rcpf(tot);
    const f32x4 o0 = ac[0][0] * f[0] + ac[0][1] * f[1] + ac[0][2] * f[2] + ac[0][3] * f[3];
    const f32x4 o1 = ac[1][0] * f[0] + ac[1][1] * f[1] + ac[1][2] * f[2] + ac[1][3] * f[3];
    u32 x0 = pk2(o0[0], o0[1]), x1 = pk2(o0[2], o0[3]), y0 = pk2(o1[0], o1[1]), y1 = pk2(o1[2], o1[3]);
    PIN(x0); PIN(x1); PIN(y0); PIN(y1);
    st16(rsW, oo, v4u{d8 ? y0 : x0, lk ^ (2u * tagLo + 1u), d8 ? y1 : x1, lk});
    STAMP(6);
    RAWBAR();
    if (sl < 14) {
      const u32 v2o = vv + g * (2048 - 256);
      auto load_w2 = [&](int i) {
        const u32 vx = v2o ^ (128 * (i & 1)), so = (32 * m + 8 * i) * 2048 + 512 * u, ld = L_R1 + 16384 * u + 2048 * i;
        asm volatile("s_mov_b32 m0, %2\n\ts_nop 0\n\tbuffer_load_dwordx4 %0, %1, %3 offen nt lds\n\tbuffer_load_dwordx4 %0, %1, %4 offen offset:1024 nt lds\n\t"
                     "s_add_u32 m0, m0, 0x1f00\n\ts_nop 0\n\tbuffer_load_dwordx4 %0, %1, %3 offen offset:256 nt lds\n\tbuffer_load_dwordx4 %0, %1, %4 offen offset:1280 nt lds"
                     :: "v"(vx), "s"(r2), "s"(ld), "s"(so), "s"(so + 4 * 2048 - 1024) : "memory", "scc");
      };
#pragma unroll
      for (int i = 0; i < 2; ++i) dma4<0>(r1, v1, v1, v1, v1, (h0 + 64 * i) * 7168 + 64 * m, 16 * 7168 - 1024, L_R0 + (h0 + 64 * i) * 64);
#pragma unroll
      for (int i = 0; i < W2_MID; ++i) load_w2(i);
      STAMP(4);
#pragma unroll
      for (int i = 0; i < 2; ++i) dma4(r1, v1, v1, v1, v1, (h1 + 64 * i) * 7168 + 64 * m, 16 * 7168 - 1024, L_R0 + (h1 + 64 * i) * 64);
      STAMP(12);
#pragma unroll
      for (int i = W2_MID; i < 4; ++i) load_w2(i);
      STAMP(7);
    }
  } else {
    LOAD_TAG();
    LAS char* pk = L + L_R0 + (64 * u + (l & 15)) * 256;
    LAS char* pb = L + L_ACT + b8 * 288 + 16 * g;
    LAS char* pqw = L + L_ACT + 2 * u * 288 + l * 4;
    LAS char* pp = L + L_P + b8 * 544 + (64 * u + 16 * d8 + 4 * g) * 2;
    LAS char* ps = l < 8 ? L + L_ST + b8 * 16 + u * 4 : L + L_JUNK + 256;
    u32 qb = WS_QG + bk * 16384 + u * 2048 + l * 16;
    PIN(pb); PIN(pp); PIN(ps); PIN(pqw); PIN(qb);
    hint_wait8(ws + WS_QH + (b * 16 + kvh * 4 + u) * 128, tagLo);
    nap<QH_DELAY>();
    STAMP(12);
    v4u d[4];
#pragma unroll
    for (int i = 0; i < 4; ++i) d[i] = __builtin_amdgcn_raw_buffer_load_b128(rsW, qb + (i & 1) * 1024, (i >> 1) * 8192, AUX_SC1);
    LSET(F_KD + u * 4);
    LWAIT1(F_KR + u * 4);
    s16x8 kf[4][4];
#pragma unroll
    for (int ks = 0; ks < 4; ++ks) {
      const int pos = ((4 * ks + g) ^ (l & 15)) * 16;
#pragma unroll
      for (int T = 0; T < 4; ++T) kf[T][ks] = *(LAS s16x8*)(pk + T * 4096 + pos);
    }
    FENCE();
    auto q_bad = [&]() { return (((d[0].y ^ tagLo) | (d[1].y ^ tagLo)) | ((d[2].y ^ tagLo) | (d[3].y ^ tagLo))) | (((d[0].w ^ tagHi) | (d[1].w ^ tagHi)) | ((d[2].w ^ tagHi) | (d[3].w ^ tagHi))); };
    if (!none(q_bad() != 0u)) {
      POLL_LOOP {
        RELOAD();
#pragma unroll
        for (int i = 0; i < 4; ++i) d[i] = __builtin_amdgcn_raw_buffer_load_b128(rsW, qb + (i & 1) * 1024, (i >> 1) * 8192, AUX_SC1);
        if (none(q_bad() != 0u)) break;
      }
    }
#pragma unroll
    for (int i = 0; i < 2; ++i)
      *(LAS u32*)(pqw + i * 288) = pk2(__uint_as_float(d[i].x) + __uint_as_float(d[i + 2].x), __uint_as_float(d[i].z) + __uint_as_float(d[i + 2].z));
    STAMP(2);
    BARRIER();
    STAMP(3);
    s16x8 bf[4];
#pragma unroll
    for (int ks = 0; ks < 4; ++ks) bf[ks] = *(LAS s16x8*)(pb + ks * 64);
    FENCE();
    f32x4 ac[4] = {zero4, zero4, zero4, zero4};
#pragma unroll
    for (int ks = 0; ks < 4; ++ks) { ac[0] = mfma(kf[0][ks], bf[ks], ac[0]); ac[1] = mfma(kf[1][ks], bf[ks], ac[1]); }
    FENCE();
#pragma unroll
    for (int ks = 0; ks < 4; ++ks) { ac[2] = mfma(kf[2][ks], bf[ks], ac[2]); ac[3] = mfma(kf[3][ks], bf[ks], ac[3]); }
    float sc[8];
#pragma unroll
    for (int pr = 0; pr < 2; ++pr)
#pragma unroll
      for (int r = 0; r < 4; r += 2) {
        u32 t = pk2(d8 ? ac[2 * pr + 1][r] : ac[2 * pr][r], d8 ? ac[2 * pr + 1][r + 1] : ac[2 * pr][r + 1]);
        asm("" : "+v"(t));
        u32 t2 = pk2(bflo(t) * 0.08838834764831845f, bfhi(t) * 0.08838834764831845f);
        asm("" : "+v"(t2));
        sc[4 * pr + r] = bflo(t2); sc[4 * pr + r + 1] = bfhi(t2);
      }
    const float mx = max8_16_32(max3(max3(sc[0], sc[1], sc[2]), max3(sc[3], sc[4], sc[5]), max2(sc[6], sc[7]))) * LOG2E;
    float pr_[8];
#pragma unroll
    for (int r = 0; r < 8; ++r) pr_[r] = __builtin_amdgcn_exp2f(fmaf(sc[r], LOG2E, -mx));
    *(LAS v2u*)pp = v2u{pk2(pr_[0], pr_[1]), pk2(pr_[2], pr_[3])};
    *(LAS v2u*)(pp + 64) = v2u{pk2(pr_[4], pr_[5]), pk2(pr_[6], pr_[7])};
    float sm = ((pr_[0] + pr_[1]) + (pr_[2] + pr_[3])) + ((pr_[4] + pr_[5]) + (pr_[6] + pr_[7]));
    sm += dpp<DPP_R8>(sm);
    sm = sum16_32(sm);
    *(LAS float*)ps = mx; *(LAS float*)(ps + 128) = sm;
    STAMP(4);
    BARRIER();
    RAWBAR();
  }
  if (sl >= 14) {
    const int f = (sl - 14) * 8 + xcd, rj = f * 8 + w, grp = f >> 2;
    v4u sel = selector();
    u32 wb = WS_WP + rj * 4096 + l * 16, hb = l < 8 ? WS_H + rj * 128 + l * 16 : WS_END;
    u32 rho = l < 8 ? WS_GH + ((f & 3) * 8 + l) * 128 + grp * 8 : WS_END;
    v2u xres = __builtin_amdgcn_raw_buffer_load_b64(mkrs(x, 8 * 1024 * 2), b8 * 2048 + (32 * (rj >> 2) + 4 * (rj & 3) + 16 * (g & 1)) * 2, 0, 0);
    PIN(sel); PIN(wb); PIN(hb); PIN(rho); PIN(xres);
    if (w == 4) q_published(l < 32 ? ((l >> 3) * 4 + grp) * 8 + b8 : (grp * 4 + 3) * 8 + (f & 3) * 2 + (l & 1));
    if (!hiw) hint_wait8t(ws + WS_G2 + f * 256 + u * 64, tagLo, RG_DELAY);
    RAWBAR();
    if (w == 7) st8(rsW, rho, tagLo, clk());
    STAMP(11);
    f32x4 acc = zero4;
    POLL_LOOP {
      RELOAD();
      v4u d[4];
#pragma unroll
      for (int i = 0; i < 4; ++i) d[i] = ld16(rsW, wb + i * 1024);
      FENCE();
      acc = zero4;
#pragma unroll
      for (int i = 0; i < 4; ++i) acc = mfma(sel, d[i], acc);
      if (none(acc[0] != acc[0])) break;
      if (none(max(max(max4(d[0]), max4(d[1])), max(max4(d[2]), max4(d[3]))) == SENT)) break;
    }
    u32 a01 = pk2(acc[0] + dpp<DPP_R8>(acc[0]), acc[1] + dpp<DPP_R8>(acc[1]));
    u32 a23 = pk2(acc[2] + dpp<DPP_R8>(acc[2]), acc[3] + dpp<DPP_R8>(acc[3]));
    PIN(a01); PIN(a23);
    const u32 h01 = pk2(bflo(xres.x) + bflo(a01), bfhi(xres.x) + bfhi(a01));
    const u32 h23 = pk2(bflo(xres.y) + bflo(a23), bfhi(xres.y) + bfhi(a23));
    auto s0 = __builtin_amdgcn_permlane16_swap(h01, h01, false, false);
    auto s1 = __builtin_amdgcn_permlane16_swap(h23, h23, false, false);
    st16(rsW, hb, v4u{s0[0], s1[0], s0[1], s1[1]});
    STAMP(13);
    STAMP_FLUSH();
    return;
  }
  s16x8 f2[8];
  LAS char* pr = L + L_P + b8 * 144 + g * 32 + d8 * 16;
  LAS char* pc = L + L_P + 8 * 144 + g * 32 + d8 * 16;
  LAS char* psr = L + L_ST + d8 * 128 + b8 * 16;
  LAS char* pf2 = L + L_R1 + w * 8192 + (8 * g + q4) * 256 + 8 * p4;
  u32 mo = WS_MP + (4 * (4 * w + d8) + g) * MPJ + m * 128 + b8 * 16;
  PIN(mo); PIN(psr); PIN(pr); PIN(pc); PIN(pf2);
  if (hiw) {
    LAS char* pa1 = L + L_R0 + (4 * g + q4) * 64 + ((g & 1) << 5) + 8 * p4;
    LAS char* pz = (l & 15) < 9 ? L + L_P + (u * 9 + (l & 15)) * 144 + 16 * g : L + L_JUNK + 256;
    LAS char* pst = (!d8 && (g == 2 || g == (b8 >> 2))) ? L + L_ST + (g < 2 ? 128 : 0) + b8 * 16 + u * 4 : L + L_JUNK + 512;
    u32 hb0 = WS_H + h0 * 16 + g * 128 + b8 * 16, hb1 = hb0 ^ 8192;
    u32 fh = tid == 448 ? WS_FH + (c - 128) * 128 : WS_END;
    PIN(pa1); PIN(pz); PIN(pst); PIN(hb0); PIN(hb1); PIN(fh);
    v4u hf[8];
#pragma unroll
    for (int i = 0; i < 8; ++i) { hf[i] = v4u{0x3F803F80u, 0x3F803F80u, 0x3F803F80u, 0x3F803F80u}; PIN(hf[i]); }
    VMWAIT(16 - 4 * W2_MID);
    s16x8 f1[2][8];
#pragma unroll
    for (int nt = 0; nt < 2; ++nt)
#pragma unroll
      for (int ks = 0; ks < 8; ++ks) f1[nt][ks] = trfrag((LAS char*)(size_t)((u32)(size_t)pa1 ^ (32 * nt)) + (ks < 4 ? h0 : h1) * 64 + (ks & 3) * 2048, 1024);
    LWAIT1(F_H);
    STAMP(11);
    if (!d8) {
#pragma unroll
      for (int i = 0; i < 8; ++i) hf[i] = ld16(rsW, (i < 4 ? hb0 : hb1) + (i & 3) * 512);
    }
    VMWAIT(8);
    LSET(F_W + u * 4);
    STAMP(8);
    LWAIT1(F_W + (w >> 1) * 4);
#pragma unroll
    for (int T = 0; T < 8; ++T) f2[T] = trfrag(pf2 + ((T ^ fsw) << 5), 1024);
    FENCE();
    f32x4 z0 = zero4, z1 = zero4, aQ = zero4;
#pragma nounroll
    for (int round = 0; round < 2; ++round) {
      z0 = zero4; z1 = zero4; aQ = zero4;
#pragma unroll
      for (int i = 0; i < 8; ++i) {
        aQ = mfma(hf[i], hf[i], aQ);
        z0 = mfma(f1[0][i], __builtin_bit_cast(s16x8, hf[i]), z0);
        z1 = mfma(f1[1][i], __builtin_bit_cast(s16x8, hf[i]), z1);
      }
      PIN(z0); PIN(z1);
      if (round || none(aQ[0] != aQ[0])) break;
      POLL_LOOP {
        RELOAD();
        if (!d8) {
#pragma unroll
          for (int i = 0; i < 8; ++i) hf[i] = ld16(rsW, (i < 4 ? hb0 : hb1) + (i & 3) * 512);
        }
        u32 mxv = 0;
#pragma unroll
        for (int i = 0; i < 8; ++i) mxv = max(mxv, max4(hf[i]));
        if (none(mxv == SENT)) break;
      }
    }
    STAMP(9);
    *(LAS float*)pst = (p4 & 2) ? ((p4 & 1) ? aQ[3] : aQ[2]) : ((p4 & 1) ? aQ[1] : aQ[0]);
    *(LAS f32x4*)pz = z0; *(LAS f32x4*)(pz + 64) = z1;
    if (w == 7) st8(rsW, fh, tagLo, clk());
  } else {
    if (w == 0) {
      q_published((((l >> 3) & 3) * 4 + 3) * 8 + b8);
      hint_wait4(ws + WS_GH + (xcd * 4 + (sl & 3)) * 128, tagLo, HR_DELAY);
      LSET(F_H);
    }
    LWAIT1(F_W + (w >> 1) * 4);
#pragma unroll
    for (int T = 0; T < 8; ++T) f2[T] = trfrag(pf2 + ((T ^ fsw) << 5), 1024);
  }
  BARRIER();
  {
    const f32x4 t0 = *(LAS f32x4*)psr;
    const f32x4 z0 = *(LAS f32x4*)pr, z1 = *(LAS f32x4*)(pr + 9 * 144), z2 = *(LAS f32x4*)(pr + 18 * 144), z3 = *(LAS f32x4*)(pr + 27 * 144);
    const f32x4 c0 = *(LAS f32x4*)pc, c1 = *(LAS f32x4*)(pc + 9 * 144), c2 = *(LAS f32x4*)(pc + 18 * 144), c3 = *(LAS f32x4*)(pc + 27 * 144);
    FENCE();
    const float mine = ((t0[0] + t0[1]) + (t0[2] + t0[3])) * (1.f / 1024.f);
    const float other = dpp<DPP_R8>(mine);
    const float mean = d8 ? other : mine, ex2 = d8 ? mine : other;
    u32 mv = pk2(mean, fmaxf(fmaf(-mean, mean, ex2), 0.f));
    asm("" : "+v"(mv));
    const float den = rbf(__builtin_amdgcn_sqrtf(rbf(bfhi(mv) + 1e-5f)));
    const float inv = __builtin_amdgcn_rcpf(den);
    const float mb = bflo(mv);
    float gv[4];
#pragma unroll
    for (int i = 0; i < 4; ++i) gv[i] = fmaf(-mb, (c0[i] + c1[i]) + (c2[i] + c3[i]), (z0[i] + z1[i]) + (z2[i] + z3[i])) * inv;
    const s16x2 zz = {0, 0};
    const int g0 = __builtin_bit_cast(int, __builtin_elementwise_max(__builtin_bit_cast(s16x2, pk2(gv[0], gv[1])), zz));
    const int g1 = __builtin_bit_cast(int, __builtin_elementwise_max(__builtin_bit_cast(s16x2, pk2(gv[2], gv[3])), zz));
    v4u B0, B1;
    B0[0] = __builtin_amdgcn_update_dpp(0, g0, DPP_ID, 0xf, 0x3, false);
    B0[1] = __builtin_amdgcn_update_dpp(0, g1, DPP_ID, 0xf, 0x3, false);
    B0[2] = __builtin_amdgcn_update_dpp(0, g0, DPP_R8, 0xf, 0x3, false);
    B0[3] = __builtin_amdgcn_update_dpp(0, g1, DPP_R8, 0xf, 0x3, false);
    B1[0] = __builtin_amdgcn_update_dpp(0, g0, DPP_R8, 0xf, 0xc, false);
    B1[1] = __builtin_amdgcn_update_dpp(0, g1, DPP_R8, 0xf, 0xc, false);
    B1[2] = __builtin_amdgcn_update_dpp(0, g0, DPP_ID, 0xf, 0xc, false);
    B1[3] = __builtin_amdgcn_update_dpp(0, g1, DPP_ID, 0xf, 0xc, false);
#pragma unroll
    for (int i = 0; i < 2; ++i) {
      f32x4 u0 = mfma(f2[4 * i], __builtin_bit_cast(s16x8, B0), zero4), u1 = mfma(f2[4 * i + 1], __builtin_bit_cast(s16x8, B0), zero4);
      u0 = mfma(f2[4 * i + 2], __builtin_bit_cast(s16x8, B1), u0);
      u1 = mfma(f2[4 * i + 3], __builtin_bit_cast(s16x8, B1), u1);
      __builtin_amdgcn_raw_buffer_store_b128(v4u{pk2(u0[0], u0[1]), pk2(u0[2], u0[3]), pk2(u1[0], u1[1]), pk2(u1[2], u1[3])}, rsW, mo, i * 8 * MPJ, AUX_SC1);
      FENCE();
    }
  }
  STAMP(10);
  STAMP_FLUSH();
}

extern "C" void kernel_launch(void* const* d_in, const int* in_sizes, int n_in, void* d_out, int out_size, void* d_ws, size_t ws_size,
                              hipStream_t stream) {
  (void)hipFuncSetAttribute((const void*)fused_decode_layer, hipFuncAttributeMaxDynamicSharedMemorySize, L_BYTES);
  fused_decode_layer<<<NWG, NTHR, L_BYTES, stream>>>((const char*)d_in[0], (const char*)d_in[1], (const char*)d_in[2], (const char*)d_in[3],
                                                     (const char*)d_in[4], (const char*)d_in[6], (const char*)d_in[7], (char*)d_out, (char*)d_ws);
}
```
